```python
import torch

def _softmax(x, axis=-1):
    return torch.softmax(x, dim=axis)
def _gelu(x):
    return torch.nn.functional.gelu(x, approximate="tanh")
def _silu(x):
    return torch.nn.functional.silu(x)
def _sigmoid(x):
    return torch.sigmoid(x)
def _layernorm(x, axis=-1, eps=1e-5):
    m = x.mean(dim=axis, keepdim=True)
    v = x.var(dim=axis, keepdim=True, unbiased=False)
    return (x - m) / torch.sqrt(v + eps)
def _scatter_add(tgt, idx, src, axis=0):
    return tgt.clone().index_add_(axis, idx, src)
def _segment_sum(src, seg, n):
    out = torch.zeros((n,) + src.shape[1:], dtype=src.dtype)
    return out.index_add_(0, seg, src)
def _pad(x, pw):
    flat = tuple(v for pair in reversed(pw) for v in pair)
    return torch.nn.functional.pad(x, flat)

kernel_name = 'sequential_preLN__sliding_block__glu_gated_geglu__prompt'

def setup_inputs():

    _w = (lambda a: (lambda t: t.to(torch.bfloat16) if t.is_floating_point() else t)(torch.as_tensor(a)))
    def _A_setup():
        B, S, D = 1, 2048, 640
        return {
            "x": _w((lambda *s: torch.randn(*s, dtype=torch.bfloat16) * 0.05)(B, S, D)),
            "wqkv": _w((lambda *s: torch.randn(*s, dtype=torch.bfloat16) * 0.05)(D, 3 * D)),
            "wo": _w((lambda *s: torch.randn(*s, dtype=torch.bfloat16) * 0.05)(D, D)),
        }
    _A_in = _A_setup()

    def _B_setup():
        B, S, Dm, Df = 1, 2048, 640, 1536
        return {
            "x": _w((lambda *s: torch.randn(*s, dtype=torch.bfloat16) * 0.05)(B, S, Dm)),
            "w_gate": _w((lambda *s: torch.randn(*s, dtype=torch.bfloat16) * 0.8)(Dm, Df) * (Dm ** -0.5)),
            "w_up": _w((lambda *s: torch.randn(*s, dtype=torch.bfloat16) * 0.8)(Dm, Df) * (Dm ** -0.5)),
            "w_down": _w((lambda *s: torch.randn(*s, dtype=torch.bfloat16) * 0.8)(Df, Dm) * (Df ** -0.5)),
        }
    _B_in = _B_setup()

    return {
        "A_x": _A_in["x"],
        "A_wqkv": _A_in["wqkv"],
        "A_wo": _A_in["wo"],
        "B_x": _B_in["x"],
        "B_w_gate": _B_in["w_gate"],
        "B_w_up": _B_in["w_up"],
        "B_w_down": _B_in["w_down"],
    }

def reference(A_x, A_wqkv, A_wo, B_x, B_w_gate, B_w_up, B_w_down):
    def _A_ref(x, wqkv, wo):
        B, S, D = x.shape
        H = 32; Dh = D // H; W = 256
        qkv = (lambda x, *d: x.permute(*d))((x @ wqkv).reshape(B, S, 3, H, Dh), 2, 0, 3, 1, 4)
        q, k, v = qkv[0], qkv[1], qkv[2]
        scores = torch.einsum("bhqd,bhkd->bhqk", q, k) * (Dh ** -0.5)
        idx = torch.arange(S)
        mask = (idx[:, None] - idx[None, :] >= 0) & (idx[:, None] - idx[None, :] < W)
        scores = torch.where(mask[None, None], scores, -1e9)
        p = _softmax(scores, axis=-1)
        o = torch.einsum("bhqk,bhkd->bhqd", p, v)
        return (lambda x, *d: x.permute(*d))(o, 0, 2, 1, 3).reshape(B, S, D) @ wo

    def _B_ref(x, w_gate, w_up, w_down):
        g = torch.einsum("bsd,df->bsf", x, w_gate)
        u = torch.einsum("bsd,df->bsf", x, w_up)
        h = _gelu(g) * u
        return torch.einsum("bsf,fd->bsd", h, w_down)

    x = A_x
    h = x + _A_ref(_layernorm(x, axis=-1), A_wqkv, A_wo)
    h2 = h + _B_ref(_layernorm(h, axis=-1), B_w_gate, B_w_up, B_w_down)
    return h2
```

```cpp
#include <hip/hip_runtime.h>
#include <stdint.h>
#include <type_traits>

#define LAS __attribute__((address_space(3)))
#define DI __device__ __forceinline__
typedef unsigned short u16;
typedef short bf16x8 __attribute__((ext_vector_type(8)));
typedef short bf16x4 __attribute__((ext_vector_type(4)));
typedef __bf16 bf16x2_t __attribute__((ext_vector_type(2)));
typedef float f32x2 __attribute__((ext_vector_type(2)));
typedef float f32x4 __attribute__((ext_vector_type(4)));
typedef float f32x16 __attribute__((ext_vector_type(16)));
typedef unsigned u32x4 __attribute__((ext_vector_type(4)));
typedef unsigned u32x2 __attribute__((ext_vector_type(2)));
typedef LAS char* ldsp;

constexpr int S_ = 2048, D_ = 640, NH = 32, DF = 1536;
constexpr float LN_EPS = 1e-5f;
constexpr float QSCALE = 0.22360679774997896f * 1.4426950408889634f;

constexpr int QK_TILE = 1280;
constexpr int V_TILE = 1344;
constexpr size_t WS_Q = 0;
constexpr size_t WS_K = WS_Q + (size_t)NH * 64 * QK_TILE;
constexpr size_t WS_V = WS_K + (size_t)NH * 64 * QK_TILE;
constexpr size_t WS_O = WS_V + (size_t)NH * 64 * V_TILE;
constexpr size_t WS_H = WS_O + (size_t)S_ * D_ * 2;
constexpr size_t WS_ACT = WS_H + (size_t)S_ * D_ * 2;
constexpr size_t WS_ST = WS_ACT + (size_t)S_ * DF * 2;
constexpr size_t WS_CS = WS_ST + (size_t)S_ * 8 * 8;

template <int I, int N, class F> DI void sfor(F&& f) {
  if constexpr (I < N) { f(std::integral_constant<int, I>{}); sfor<I + 1, N>(f); }
}
DI unsigned cvtpk(float lo, float hi) { f32x2 v = {lo, hi}; return __builtin_bit_cast(unsigned, __builtin_convertvector(v, bf16x2_t)); }
DI float bflo(unsigned u) { return __uint_as_float(u << 16); }
DI float bfhi(unsigned u) { return __uint_as_float(u & 0xffff0000u); }
DI float dot2(unsigned a, unsigned b, float c) {
  return __builtin_amdgcn_fdot2_f32_bf16(__builtin_bit_cast(bf16x2_t, a), __builtin_bit_cast(bf16x2_t, b), c, false);
}
DI f32x4 mfma16(bf16x8 a, bf16x8 b, f32x4 c) { return __builtin_amdgcn_mfma_f32_16x16x32_bf16(a, b, c, 0, 0, 0); }
DI void dma16(const void* sbase, unsigned voff, unsigned lds_dst) {
  asm volatile("s_mov_b32 m0, %2\n\ts_nop 0\n\tglobal_load_lds_dwordx4 %0, %1" ::"v"(voff), "s"(sbase), "s"(lds_dst) : "memory");
}
template <int OFF> DI void gload16(bf16x8& dst, const void* sbase, unsigned voff) {
  asm volatile("global_load_dwordx4 %0, %1, %2 offset:%3" : "=v"(dst) : "v"(voff), "s"(sbase), "n"(OFF) : "memory");
}
template <class T> DI void gload16v(T& dst, const void* sbase, unsigned voff) {
  asm volatile("global_load_dwordx4 %0, %1, %2" : "=v"(dst) : "v"(voff), "s"(sbase) : "memory");
}
template <int OFF> DI void gload8(u32x2& dst, const void* sbase, unsigned voff) {
  asm volatile("global_load_dwordx2 %0, %1, %2 offset:%3" : "=v"(dst) : "v"(voff), "s"(sbase), "n"(OFF) : "memory");
}
template <int N> DI void wait_bar() { asm volatile("s_waitcnt vmcnt(%0) lgkmcnt(0)\n\ts_barrier" ::"n"(N) : "memory"); }
template <class T> DI void pin(T& v) { asm volatile("" : "+v"(v)); }
DI bf16x8 lds_ld16(ldsp p) { return *(const LAS bf16x8*)p; }
DI bf16x4 lds_tr(ldsp p) { return __builtin_amdgcn_ds_read_tr16_b64_v4i16((LAS bf16x4*)p); }
DI bf16x8 lds_tr2(ldsp p0, ldsp p1) {
  bf16x4 a = lds_tr(p0), b = lds_tr(p1);
  return __builtin_shufflevector(a, b, 0, 1, 2, 3, 4, 5, 6, 7);
}
DI float xsum16_32(float v) { v += __shfl_xor(v, 16); v += __shfl_xor(v, 32); return v; }

constexpr int K1_NSTG = 4, K1_STG = 32768, K1_NT = 10, K1_LDS = K1_NSTG * K1_STG + 2048;

template <bool ISV>
DI void qkv_tile(ldsp lds, const u16* __restrict__ x, const u16* __restrict__ wqkv, char* __restrict__ ws, int mt, int nt) {
  const int tid = threadIdx.x, lane = tid & 63, w = __builtin_amdgcn_readfirstlane(tid >> 6);
  const int wm = w >> 1, wn = w & 1, fr = lane & 15, g = lane >> 4, q = fr >> 2, p = lane & 3;
  const unsigned lds0 = (unsigned)(uintptr_t)lds;
  const char* xb = (const char*)x + (size_t)mt * 128 * 1280;
  const char* wb = (const char*)wqkv + nt * 256;
  unsigned voffA[4], voffW[4];
#pragma unroll
  for (int i = 0; i < 4; ++i) {
    const int r = (w * 4 + i) * 8 + (lane >> 3), c = (lane & 7) ^ ((r >> 1) & 7);
    voffA[i] = r * 1280 + c * 16;
    const int k = (w * 4 + i) * 4 + (lane >> 4), cp = lane & 15, P = (cp >> 1) ^ ((k & 3) | ((k >> 1) & 4));
    voffW[i] = k * 3840 + (2 * P + (cp & 1)) * 16;
  }
  auto issue = [&](int t) {
    const unsigned slot = lds0 + (t % K1_NSTG) * K1_STG + w * 4096;
#pragma unroll
    for (int i = 0; i < 4; ++i) dma16(xb + t * 128, voffA[i], slot + i * 1024);
#pragma unroll
    for (int i = 0; i < 4; ++i) dma16(wb + (size_t)t * 64 * 3840, voffW[i], slot + 16384 + i * 1024);
  };
  int aoff[2][2], woff[4];
#pragma unroll
  for (int ks = 0; ks < 2; ++ks)
#pragma unroll
    for (int hh = 0; hh < 2; ++hh) aoff[ks][hh] = (wm * 64 + ((2 * hh) ^ (2 * wn)) * 16 + fr) * 128 + (((4 * ks + g) ^ (fr >> 1)) * 16);
#pragma unroll
  for (int j = 0; j < 4; ++j) woff[j] = 16384 + (8 * g + q) * 256 + (((wn * 4 + (j ^ (2 * wm))) ^ (q | ((g & 1) << 2))) * 32) + p * 8;

  f32x4 acc[4][4];
#pragma unroll
  for (int j = 0; j < 4; ++j)
#pragma unroll
    for (int i = 0; i < 4; ++i) acc[j][i] = (f32x4){0.f, 0.f, 0.f, 0.f};
  float rs[2] = {0.f, 0.f}, rq[2] = {0.f, 0.f}, cs[2] = {0.f, 0.f};
  bf16x8 xf[2][4], wf[2][4];
  auto rd = [&](int t, int ks) {
    ldsp sb = lds + (t % K1_NSTG) * K1_STG;
#pragma unroll
    for (int i = 0; i < 4; ++i) xf[ks][i] = lds_ld16(sb + aoff[ks][i >> 1] + (i & 1) * 2048);
#pragma unroll
    for (int j = 0; j < 4; ++j) wf[ks][j] = lds_tr2(sb + woff[j] + ks * 32 * 256, sb + woff[j] + (ks * 32 + 4) * 256);
  };
  auto mm = [&](int ks) {
#pragma unroll
    for (int j = 0; j < 4; ++j)
#pragma unroll
      for (int i = 0; i < 4; ++i) acc[j][i] = ISV ? mfma16(xf[ks][i], wf[ks][j], acc[j][i]) : mfma16(wf[ks][j], xf[ks][i], acc[j][i]);
#pragma unroll
    for (int i = 0; i < 2; ++i) {
      const u32x4 a = __builtin_bit_cast(u32x4, xf[ks][i]), b = __builtin_bit_cast(u32x4, wf[ks][i]);
#pragma unroll
      for (int e = 0; e < 4; ++e) { rs[i] = dot2(a[e], 0x3f803f80u, rs[i]); rq[i] = dot2(a[e], a[e], rq[i]); cs[i] = dot2(b[e], 0x3f803f80u, cs[i]); }
    }
  };
#pragma unroll
  for (int t = 0; t < K1_NSTG; ++t) issue(t);
  wait_bar<8 * (K1_NSTG - 1)>();
  rd(0, 0);
  sfor<0, K1_NT>([&](auto tc) {
    constexpr int t = decltype(tc)::value;
    rd(t, 1);
    mm(0);
    if constexpr (t + 1 < K1_NT) {
      constexpr int last = (t + K1_NSTG - 1 < K1_NT - 1) ? t + K1_NSTG - 1 : K1_NT - 1;
      wait_bar<8 * (last - (t + 1))>();
      if constexpr (t + K1_NSTG < K1_NT) issue(t + K1_NSTG);
      rd(t + 1, 0);
    }
    mm(1);
  });
  LAS f32x2* rst = (LAS f32x2*)(lds + K1_NSTG * K1_STG);
  LAS float* cst = (LAS float*)(lds + K1_NSTG * K1_STG + 1024);
#pragma unroll
  for (int i = 0; i < 2; ++i) {
    const float s = xsum16_32(rs[i]) * (1.f / D_), qq = xsum16_32(rq[i]) * (1.f / D_), c = xsum16_32(cs[i]);
    const float var = fmaxf(qq - s * s, 0.f);
    if (g == 0) {
      rst[wm * 64 + (i ^ (2 * wn)) * 16 + fr] = (f32x2){s, __builtin_amdgcn_rsqf(var + LN_EPS)};
      cst[wn * 64 + (i ^ (2 * wm)) * 16 + fr] = c;
    }
  }
  asm volatile("s_waitcnt lgkmcnt(0)\n\ts_barrier" ::: "memory");
  if constexpr (!ISV) {
    const bool isq = nt < 5;
    char* ob = ws + (isq ? WS_Q : WS_K);
    const float sc = isq ? QSCALE : 1.f;
    const int nrel0 = (isq ? nt : nt - 5) * 128;
#pragma unroll
    for (int j = 0; j < 4; ++j) {
      const int nl = wn * 64 + (j ^ (2 * wm)) * 16 + 4 * g, n = nrel0 + nl, h = n / 20, d = n - h * 20;
      const f32x4 c4 = *(const LAS f32x4*)(cst + nl);
#pragma unroll
      for (int i = 0; i < 4; ++i) {
        const int ml = wm * 64 + (i ^ (2 * wn)) * 16 + fr, tok = mt * 128 + ml, r = tok & 31;
        const f32x2 st = rst[ml];
        const float a = st.y * sc;
        const f32x4 v = (acc[j][i] - st.x * c4) * a;
        const int off = (h * 64 + (tok >> 5)) * QK_TILE + (d < 16 ? (d >> 3) * 512 + r * 16 + (d & 7) * 2 : 1024 + r * 8);
        *(u32x2*)(ob + off) = (u32x2){cvtpk(v[0], v[1]), cvtpk(v[2], v[3])};
      }
    }
  } else {
    char* ob = ws + WS_V;
#pragma unroll
    for (int i = 0; i < 4; ++i) {
      const int ml = wm * 64 + (i ^ (2 * wn)) * 16 + 4 * g, tok = mt * 128 + ml, tt = tok & 31;
      const f32x4 s01 = *(const LAS f32x4*)(rst + ml), s23 = *(const LAS f32x4*)(rst + ml + 2);
      const int toff = (tok >> 5) * V_TILE + (tt >> 4) * 672 + ((tt >> 2) & 1) * 336 + ((tt >> 3) & 1) * 8;
#pragma unroll
      for (int j = 0; j < 4; ++j) {
        const int nl = wn * 64 + (j ^ (2 * wm)) * 16 + fr, n = (nt - 10) * 128 + nl, h = n / 20, d = n - h * 20;
        const float c = cst[nl];
        const f32x4 a = acc[j][i];
        const float v0 = (a[0] - s01[0] * c) * s01[1], v1 = (a[1] - s01[2] * c) * s01[3], v2 = (a[2] - s23[0] * c) * s23[1], v3 = (a[3] - s23[2] * c) * s23[3];
        char* o = ob + h * 64 * V_TILE + toff + d * 16;
        *(u32x2*)o = (u32x2){cvtpk(v0, v1), cvtpk(v2, v3)};
        if (d == 19) *(u32x2*)(o + 16) = (u32x2){0x3f803f80u, 0x3f803f80u};
      }
    }
  }
}

DI void colsum_block(ldsp lds, const u16* __restrict__ wg, const u16* __restrict__ wu, char* __restrict__ ws, int b) {
  const int tid = threadIdx.x, c = tid % 24, rg = tid / 24;
  const u16* wsrc = (b < 8 ? wg : wu) + (b & 7) * 192 + c * 8;
  float s[8];
#pragma unroll
  for (int e = 0; e < 8; ++e) s[e] = 0.f;
  if (rg < 10) {
#pragma unroll 16
    for (int r = 0; r < 64; ++r) {
      const u32x4 v = *(const u32x4*)(wsrc + (size_t)(rg * 64 + r) * DF);
#pragma unroll
      for (int e = 0; e < 4; ++e) { s[2 * e] += bflo(v[e]); s[2 * e + 1] += bfhi(v[e]); }
    }
    LAS float* t = (LAS float*)lds + rg * 192 + c * 8;
    *(LAS f32x4*)t = (f32x4){s[0], s[1], s[2], s[3]};
    *(LAS f32x4*)(t + 4) = (f32x4){s[4], s[5], s[6], s[7]};
  }
  __syncthreads();
  if (tid < 192) {
    float a = 0.f;
#pragma unroll
    for (int r = 0; r < 10; ++r) a += ((LAS float*)lds)[r * 192 + tid];
    ((float*)(ws + WS_CS))[b * 192 + tid] = a;
  }
}

__global__ __launch_bounds__(256, 1) void k_qkv(const u16* __restrict__ x, const u16* __restrict__ wqkv, const u16* __restrict__ wg,
                                                 const u16* __restrict__ wu, char* __restrict__ ws) {
  extern __shared__ __attribute__((aligned(16))) char smem[];
  ldsp lds = (ldsp)smem;
  const int xcd = blockIdx.x & 7, idx = blockIdx.x >> 3;
  if (idx >= 30) { colsum_block(lds, wg, wu, ws, xcd * 2 + idx - 30); return; }
  const int mt = 2 * xcd + idx / 15, nt = idx % 15;
  if (nt >= 10) qkv_tile<true>(lds, x, wqkv, ws, mt, nt);
  else qkv_tile<false>(lds, x, wqkv, ws, mt, nt);
}

constexpr int K2_LDS = 16 * QK_TILE + 16 * V_TILE + 256;

template <bool EDGE> DI void attn_body(ldsp lds, char* __restrict__ ws, int c, int h) {
  const int tid = threadIdx.x, lane = tid & 63, w = __builtin_amdgcn_readfirstlane(tid >> 6);
  const int r32 = lane & 31, hi = lane >> 5;
  const unsigned lds0 = (unsigned)(uintptr_t)lds;
  const char* kb = ws + WS_K + (size_t)(h * 64 + 8 * c - 8) * QK_TILE;
  const char* vb = ws + WS_V + (size_t)(h * 64 + 8 * c - 8) * V_TILE;
  for (int i = w; i < 41; i += 8) {
    const bool isk = i < 20;
    const int half = isk ? 8 * QK_TILE : 8 * V_TILE;
    unsigned off = (isk ? i : i - 20) * 1024 + lane * 16;
    if (EDGE && off < (unsigned)half) off += half;
    dma16(isk ? kb : vb, off, lds0 + i * 1024);
  }
  const char* qb = ws + WS_Q + (size_t)(h * 64 + 8 * c + w) * QK_TILE;
  const bf16x8 qm = *(const bf16x8*)(qb + lane * 16);
  bf16x4 qt = *(const bf16x4*)(qb + 1024 + r32 * 8);
  if (hi) qt = (bf16x4){0, 0, 0, 0};
  wait_bar<0>();
  ldsp kp = lds + w * QK_TILE;
  ldsp vp = lds + 16 * QK_TILE + w * V_TILE + hi * 336 + r32 * 16;
  f32x16 s[9];
#pragma unroll
  for (int j = 0; j < 9; ++j) {
    const bf16x8 km = lds_ld16(kp + j * QK_TILE + lane * 16);
    const bf16x4 kt = *(const LAS bf16x4*)(kp + j * QK_TILE + 1024 + r32 * 8);
    f32x16 z = {};
    z = __builtin_amdgcn_mfma_f32_32x32x16_bf16(km, qm, z, 0, 0, 0);
    s[j] = __builtin_amdgcn_mfma_f32_32x32x8bf16_1k(kt, qt, z, 0, 0, 0);
    if (EDGE && j < 8) {
      const bool valid = w + j >= 8;
#pragma unroll
      for (int r = 0; r < 16; ++r) s[j][r] = valid ? s[j][r] : -__builtin_inff();
    }
  }
  const int dq = r32 - 4 * hi;
#pragma unroll
  for (int r = 0; r < 16; ++r) s[0][r] = ((r & 3) + 8 * (r >> 2) <= dq) ? s[8][r] : s[0][r];
  float m0 = s[0][0], m1 = s[0][1];
#pragma unroll
  for (int j = 0; j < 8; ++j)
#pragma unroll
    for (int r = (j == 0 ? 2 : 0); r < 16; r += 2) { m0 = __builtin_elementwise_maximum(m0, s[j][r]); m1 = __builtin_elementwise_maximum(m1, s[j][r + 1]); }
  float m = __builtin_elementwise_maximum(m0, m1);
  {
    auto rr = __builtin_amdgcn_permlane32_swap(__float_as_uint(m), __float_as_uint(m), false, false);
    m = __builtin_elementwise_maximum(__uint_as_float(rr[0]), __uint_as_float(rr[1]));
  }
  f32x16 o = {};
#pragma unroll
  for (int j = 0; j < 8; ++j) {
    u32x4 pk[2];
#pragma unroll
    for (int r = 0; r < 16; r += 2)
      pk[r >> 3][(r >> 1) & 3] = cvtpk(__builtin_amdgcn_exp2f(s[j][r] - m), __builtin_amdgcn_exp2f(s[j][r + 1] - m));
    if (j == 0) {
#pragma unroll
      for (int ks = 0; ks < 2; ++ks) {
        u32x4 p8, p0;
#pragma unroll
        for (int e = 0; e < 4; ++e) {
          const int r = ks * 8 + 2 * e, k0 = (r & 3) + 8 * (r >> 2);
          const unsigned msk = (k0 <= dq ? 0xffffu : 0u) | (k0 + 1 <= dq ? 0xffff0000u : 0u);
          p8[e] = pk[ks][e] & msk; p0[e] = pk[ks][e] & ~msk;
        }
        o = __builtin_amdgcn_mfma_f32_32x32x16_bf16(lds_ld16(vp + ks * 672), __builtin_bit_cast(bf16x8, p0), o, 0, 0, 0);
        o = __builtin_amdgcn_mfma_f32_32x32x16_bf16(lds_ld16(vp + 8 * V_TILE + ks * 672), __builtin_bit_cast(bf16x8, p8), o, 0, 0, 0);
      }
    } else {
#pragma unroll
      for (int ks = 0; ks < 2; ++ks)
        o = __builtin_amdgcn_mfma_f32_32x32x16_bf16(lds_ld16(vp + j * V_TILE + ks * 672), __builtin_bit_cast(bf16x8, pk[ks]), o, 0, 0, 0);
    }
  }
  float l;
  {
    auto rr = __builtin_amdgcn_permlane32_swap(__float_as_uint(o[8]), __float_as_uint(o[8]), false, false);
    l = __uint_as_float(rr[1]);
    if (hi == 0) l = __uint_as_float(rr[1]);
  }
  const float il = __builtin_amdgcn_rcpf(l);
  const int tok = 256 * c + 32 * w + r32;
  char* ob = ws + WS_O + (size_t)(tok >> 4) * 20 * 1024 + (tok & 15) * 16;
  const u32x4 w0 = {cvtpk(o[0] * il, o[1] * il), cvtpk(o[2] * il, o[3] * il), cvtpk(o[4] * il, o[5] * il), cvtpk(o[6] * il, o[7] * il)};
  *(u32x4*)(ob + (h >> 1) * 1024 + (2 * (h & 1) + hi) * 256) = w0;
  if (hi == 0) *(u32x2*)(ob + (16 + (h >> 3)) * 1024 + (h & 3) * 256 + ((h >> 2) & 1) * 8) = (u32x2){cvtpk(o[8] * il, o[9] * il), cvtpk(o[10] * il, o[11] * il)};
}

__global__ __launch_bounds__(512, 2) void k_attn(char* __restrict__ ws) {
  extern __shared__ __attribute__((aligned(16))) char smem[];
  const int c = blockIdx.x & 7, h = blockIdx.x >> 3;
  if (c == 0) attn_body<true>((ldsp)smem, ws, c, h);
  else attn_body<false>((ldsp)smem, ws, c, h);
}

constexpr int N80_STG = 128 * 160;
template <bool DOWN> struct N80 {
  static constexpr int NT = DOWN ? 12 : 5, NSTG = DOWN ? 6 : 5, KSTEPS = NT * 4, LDS = NSTG * N80_STG;
};

template <bool DOWN>
__global__ __launch_bounds__(256, 1) void k_n80(const u16* __restrict__ x, const u16* __restrict__ wmat, char* __restrict__ ws, u16* __restrict__ out) {
  using C = N80<DOWN>;
  extern __shared__ __attribute__((aligned(16))) char smem[];
  ldsp lds = (ldsp)smem;
  const int tid = threadIdx.x, lane = tid & 63, w = __builtin_amdgcn_readfirstlane(tid >> 6);
  const int fr = lane & 15, g = lane >> 4, q = fr >> 2, p = lane & 3;
  const unsigned lds0 = (unsigned)(uintptr_t)lds;
  const int xcd = blockIdx.x & 7, idx = blockIdx.x >> 3, mt = xcd * 4 + (idx >> 3), ct = idx & 7;
  const int rt = mt * 4 + w;
  const char* ab = ws + (DOWN ? WS_ACT : WS_O) + (size_t)rt * C::KSTEPS * 1024;
  const char* wb = (const char*)wmat + ct * 160;
  const unsigned av = lane * 16;
  unsigned voffW[5], voffT[5];
#pragma unroll
  for (int i = 0; i < 5; ++i) {
    const int o = (w * 5 + i) * 1024 + lane * 16, k = o / 160, cb = o - k * 160;
    if (DOWN) { voffW[i] = k * 1280 + cb; voffT[i] = 0; }
    else {
      voffW[i] = (20 * (2 * (k >> 5) + ((k >> 3) & 1)) + 8 * ((k >> 4) & 1) + (k & 7)) * 1280 + cb;
      voffT[i] = (20 * (k >> 2) + 16 + (k & 3)) * 1280 + cb;
    }
  }
  const int odd = ct & 1, ksb = (ct >> 1) * 5 + (odd ? 3 : 0), kss = (ct >> 1) * 5 + 2;
  char* hb = ws + WS_H + (size_t)rt * 20 * 1024 + lane * 16;
  const int tok = rt * 16 + fr, col0 = ct * 80 + 4 * g;
  u32x2 res[5];
  u32x4 ra, rb;
  if (DOWN) {
    const char* hs = ws + WS_H + (size_t)rt * 20 * 1024;
    gload16v(ra, hs + ksb * 1024, av); gload16v(rb, hs + (ksb + 1) * 1024, av); gload8<0>(res[4], hs + kss * 1024 + odd * 8, av);
  } else {
    const unsigned xo = (unsigned)(tok * D_ + col0) * 2;
    gload8<0>(res[0], x, xo); gload8<32>(res[1], x, xo); gload8<64>(res[2], x, xo); gload8<96>(res[3], x, xo); gload8<128>(res[4], x, xo);
  }

  bf16x8 af[C::NSTG][4], wf[2][5];
  auto issue = [&](auto tc) {
    constexpr int t = decltype(tc)::value;
    const unsigned slot = lds0 + (t % C::NSTG) * N80_STG + w * 5120;
#pragma unroll
    for (int i = 0; i < 5; ++i) {
      if (DOWN) dma16(wb + (size_t)t * 128 * 1280, voffW[i], slot + i * 1024);
      else if (t < 4) dma16(wb + (size_t)t * 160 * 1280, voffW[i], slot + i * 1024);
      else dma16(wb, voffT[i], slot + i * 1024);
    }
    const char* a = ab + t * 4096;
    gload16<0>(af[t % C::NSTG][0], a, av); gload16<1024>(af[t % C::NSTG][1], a, av);
    gload16<2048>(af[t % C::NSTG][2], a, av); gload16<3072>(af[t % C::NSTG][3], a, av);
  };
  const int woff = (4 * g + q) * 160 + p * 8;
  auto rd = [&](int t, int ks) {
    ldsp sb = lds + (t % C::NSTG) * N80_STG + woff + ks * 32 * 160;
#pragma unroll
    for (int j = 0; j < 5; ++j) wf[ks & 1][j] = lds_tr2(sb + j * 32, sb + 16 * 160 + j * 32);
  };
  f32x4 acc[5];
#pragma unroll
  for (int j = 0; j < 5; ++j) acc[j] = (f32x4){0.f, 0.f, 0.f, 0.f};
  sfor<0, C::NSTG>([&](auto tc) { issue(tc); });
  wait_bar<9 * (C::NSTG - 1)>();
  if (DOWN) { pin(ra); pin(rb); pin(res[4]); }
  else {
#pragma unroll
    for (int j = 0; j < 5; ++j) pin(res[j]);
  }
#pragma unroll
  for (int i = 0; i < 4; ++i) pin(af[0][i]);
  rd(0, 0);
  sfor<0, C::NT>([&](auto tc) {
    constexpr int t = decltype(tc)::value;
#pragma unroll
    for (int ks = 0; ks < 3; ++ks) {
      rd(t, ks + 1);
#pragma unroll
      for (int j = 0; j < 5; ++j) acc[j] = mfma16(wf[ks & 1][j], af[t % C::NSTG][ks], acc[j]);
    }
    bf16x8 a3 = af[t % C::NSTG][3];
    if constexpr (t + 1 < C::NT) {
      constexpr int last = (t + C::NSTG - 1 < C::NT - 1) ? t + C::NSTG - 1 : C::NT - 1;
      wait_bar<9 * (last - (t + 1))>();
#pragma unroll
      for (int i = 0; i < 4; ++i) pin(af[(t + 1) % C::NSTG][i]);
      rd(t + 1, 0);
    }
#pragma unroll
    for (int j = 0; j < 5; ++j) acc[j] = mfma16(wf[1][j], a3, acc[j]);
    if constexpr (t + C::NSTG < C::NT) issue(std::integral_constant<int, t + C::NSTG>{});
  });
  if (DOWN) {
    const u32x2 a0 = {ra[0], ra[1]}, a1 = {ra[2], ra[3]}, b0 = {rb[0], rb[1]}, b1 = {rb[2], rb[3]}, s1 = res[4];
    res[0] = odd ? s1 : a0; res[1] = odd ? a0 : a1; res[2] = odd ? a1 : b0; res[3] = odd ? b0 : b1; res[4] = odd ? b1 : s1;
  }
  u32x2 hv[5];
  float s = 0.f;
#pragma unroll
  for (int j = 0; j < 5; ++j) {
    hv[j] = (u32x2){cvtpk(acc[j][0] + bflo(res[j][0]), acc[j][1] + bfhi(res[j][0])), cvtpk(acc[j][2] + bflo(res[j][1]), acc[j][3] + bfhi(res[j][1]))};
    s += (bflo(hv[j][0]) + bfhi(hv[j][0])) + (bflo(hv[j][1]) + bfhi(hv[j][1]));
  }
  if (DOWN) {
#pragma unroll
    for (int j = 0; j < 5; ++j) *(u32x2*)(out + (size_t)tok * D_ + col0 + 16 * j) = hv[j];
  } else {
    const u32x2 a0 = odd ? hv[1] : hv[0], a1 = odd ? hv[2] : hv[1], b0 = odd ? hv[3] : hv[2], b1 = odd ? hv[4] : hv[3], s1 = odd ? hv[0] : hv[4];
    *(u32x4*)(hb + ksb * 1024) = (u32x4){a0[0], a0[1], a1[0], a1[1]};
    *(u32x4*)(hb + (ksb + 1) * 1024) = (u32x4){b0[0], b0[1], b1[0], b1[1]};
    *(u32x2*)(hb + kss * 1024 + odd * 8) = s1;
    const float mean = xsum16_32(s) * (1.f / 80.f);
    float m2 = 0.f;
#pragma unroll
    for (int j = 0; j < 5; ++j) {
      const float d0 = bflo(hv[j][0]) - mean, d1 = bfhi(hv[j][0]) - mean, d2 = bflo(hv[j][1]) - mean, d3 = bfhi(hv[j][1]) - mean;
      m2 += (d0 * d0 + d1 * d1) + (d2 * d2 + d3 * d3);
    }
    m2 = xsum16_32(m2);
    if (g == 0) *(f32x2*)(ws + WS_ST + ((size_t)tok * 8 + ct) * 8) = (f32x2){mean, m2};
  }
}

constexpr int K4_NSTG = 6, K4_STG = 2 * 64 * 192, K4_NT = 10, K4_LDS = K4_NSTG * K4_STG + 1024;

__global__ __launch_bounds__(256, 1) void k_gateup(const u16* __restrict__ wg, const u16* __restrict__ wu, char* __restrict__ ws) {
  extern __shared__ __attribute__((aligned(16))) char smem[];
  ldsp lds = (ldsp)smem;
  const int tid = threadIdx.x, lane = tid & 63, w = __builtin_amdgcn_readfirstlane(tid >> 6);
  const int fr = lane & 15, g = lane >> 4, q = fr >> 2, p = lane & 3;
  const unsigned lds0 = (unsigned)(uintptr_t)lds;
  const int xcd = blockIdx.x & 7, idx = blockIdx.x >> 3, mt = xcd * 2 + (idx >> 4), ft = idx & 15;
  const int rt = mt * 8 + w * 2;
  const char* ab = ws + WS_H + (size_t)rt * 20 * 1024;
  const char* gb = (const char*)wg + ft * 192;
  const char* ub = (const char*)wu + ft * 192;
  const unsigned av = lane * 16;
  unsigned voffW[3];
#pragma unroll
  for (int i = 0; i < 3; ++i) {
    const int o = (w * 3 + i) * 1024 + lane * 16, k = o / 192, cp = (o - k * 192) >> 4;
    voffW[i] = k * 3072 + (cp ^ (((k >> 2) & 1) << 1)) * 16;
  }
  f32x4 stv[2];
#pragma unroll
  for (int i = 0; i < 2; ++i) gload16v(stv[i], ws + WS_ST + (size_t)(rt + i) * 1024, (unsigned)(fr * 64 + g * 16));
  if (w == 0) {
    const int l48 = lane < 48 ? lane : 47;
    dma16(ws + WS_CS + ft * 384, (unsigned)((l48 / 24) * DF * 4 + (l48 % 24) * 16), lds0 + K4_NSTG * K4_STG);
  }
  bf16x8 af[K4_NSTG][4], wf[2][12];
  auto issue = [&](auto tc) {
    constexpr int t = decltype(tc)::value;
    const unsigned slot = lds0 + (t % K4_NSTG) * K4_STG + w * 3072;
#pragma unroll
    for (int i = 0; i < 3; ++i) dma16(gb + (size_t)t * 64 * 3072, voffW[i], slot + i * 1024);
#pragma unroll
    for (int i = 0; i < 3; ++i) dma16(ub + (size_t)t * 64 * 3072, voffW[i], slot + 12288 + i * 1024);
    const char* a = ab + t * 2048;
    gload16<0>(af[t % K4_NSTG][0], a, av); gload16<1024>(af[t % K4_NSTG][1], a, av);
    gload16<0>(af[t % K4_NSTG][2], a + 20480, av); gload16<1024>(af[t % K4_NSTG][3], a + 20480, av);
  };
  int woff[2];
#pragma unroll
  for (int e = 0; e < 2; ++e) woff[e] = (4 * g + q) * 192 + ((e ^ (g & 1)) * 32) + p * 8;
  auto rd = [&](int t, int ks) {
    ldsp sb = lds + (t % K4_NSTG) * K4_STG + ks * 32 * 192;
#pragma unroll
    for (int j = 0; j < 12; ++j) {
      ldsp a = sb + (j / 6) * 12288 + woff[j & 1] + ((j % 6) >> 1) * 64;
      wf[ks][j] = lds_tr2(a, a + 16 * 192);
    }
  };
  f32x4 acc[2][12];
#pragma unroll
  for (int i = 0; i < 2; ++i)
#pragma unroll
    for (int j = 0; j < 12; ++j) acc[i][j] = (f32x4){0.f, 0.f, 0.f, 0.f};
  sfor<0, K4_NSTG>([&](auto tc) { issue(tc); });
  wait_bar<10 * (K4_NSTG - 1)>();
  pin(stv[0]); pin(stv[1]);
#pragma unroll
  for (int i = 0; i < 4; ++i) pin(af[0][i]);
  rd(0, 0);
  sfor<0, K4_NT>([&](auto tc) {
    constexpr int t = decltype(tc)::value;
    rd(t, 1);
#pragma unroll
    for (int j = 0; j < 12; ++j)
#pragma unroll
      for (int i = 0; i < 2; ++i) acc[i][j] = mfma16(wf[0][j], af[t % K4_NSTG][2 * i], acc[i][j]);
    if constexpr (t + 1 < K4_NT) {
      constexpr int last = (t + K4_NSTG - 1 < K4_NT - 1) ? t + K4_NSTG - 1 : K4_NT - 1;
      wait_bar<10 * (last - (t + 1))>();
#pragma unroll
      for (int i = 0; i < 4; ++i) pin(af[(t + 1) % K4_NSTG][i]);
      rd(t + 1, 0);
    }
#pragma unroll
    for (int j = 0; j < 12; ++j)
#pragma unroll
      for (int i = 0; i < 2; ++i) acc[i][j] = mfma16(wf[1][j], af[t % K4_NSTG][2 * i + 1], acc[i][j]);
    if constexpr (t + K4_NSTG < K4_NT) issue(std::integral_constant<int, t + K4_NSTG>{});
  });
  float mean[2], rstd[2];
#pragma unroll
  for (int i = 0; i < 2; ++i) {
    const f32x4 v = stv[i];
    const float mu = xsum16_32(v[0] + v[2]) * 0.125f, d0 = v[0] - mu, d1 = v[2] - mu;
    const float m2 = xsum16_32(v[1] + v[3] + 80.f * (d0 * d0 + d1 * d1));
    mean[i] = mu; rstd[i] = __builtin_amdgcn_rsqf(m2 * (1.f / D_) + LN_EPS);
  }
  const LAS float* csb = (const LAS float*)(lds + K4_NSTG * K4_STG) + 4 * g;
  constexpr float GC0 = -2.f * 0.7978845608028654f * 1.4426950408889634f, GC1 = GC0 * 0.044715f;
#pragma unroll
  for (int jp = 0; jp < 3; ++jp) {
    f32x4 cg[2], cu[2];
#pragma unroll
    for (int e = 0; e < 2; ++e) { cg[e] = *(const LAS f32x4*)(csb + (2 * jp + e) * 16); cu[e] = *(const LAS f32x4*)(csb + 96 + (2 * jp + e) * 16); }
#pragma unroll
    for (int i = 0; i < 2; ++i) {
      unsigned pk[4];
#pragma unroll
      for (int e = 0; e < 2; ++e) {
        float a[4];
#pragma unroll
        for (int r = 0; r < 4; ++r) {
          const float gv = (acc[i][2 * jp + e][r] - mean[i] * cg[e][r]) * rstd[i];
          const float uv = (acc[i][6 + 2 * jp + e][r] - mean[i] * cu[e][r]) * rstd[i];
          const float ex = __builtin_amdgcn_exp2f(gv * (GC0 + GC1 * gv * gv));
          a[r] = gv * uv * __builtin_amdgcn_rcpf(1.f + ex);
        }
        pk[2 * e] = cvtpk(a[0], a[1]); pk[2 * e + 1] = cvtpk(a[2], a[3]);
      }
      *(u32x4*)(ws + WS_ACT + ((size_t)(rt + i) * 48 + ft * 3 + jp) * 1024 + lane * 16) = (u32x4){pk[0], pk[1], pk[2], pk[3]};
    }
  }
}

extern "C" void kernel_launch(void* const* d_in, const int* in_sizes, int n_in, void* d_out, int out_size, void* d_ws, size_t ws_size,
                              hipStream_t stream) {
  const u16* x = (const u16*)d_in[0];
  const u16* wqkv = (const u16*)d_in[1];
  const u16* wo = (const u16*)d_in[2];
  const u16* wg = (const u16*)d_in[4];
  const u16* wu = (const u16*)d_in[5];
  const u16* wd = (const u16*)d_in[6];
  char* ws = (char*)d_ws;
  u16* out = (u16*)d_out;
  (void)hipFuncSetAttribute((const void*)k_qkv, hipFuncAttributeMaxDynamicSharedMemorySize, K1_LDS);
  (void)hipFuncSetAttribute((const void*)k_n80<false>, hipFuncAttributeMaxDynamicSharedMemorySize, N80<false>::LDS);
  (void)hipFuncSetAttribute((const void*)k_n80<true>, hipFuncAttributeMaxDynamicSharedMemorySize, N80<true>::LDS);
  (void)hipFuncSetAttribute((const void*)k_gateup, hipFuncAttributeMaxDynamicSharedMemorySize, K4_LDS);
  k_qkv<<<256, 256, K1_LDS, stream>>>(x, wqkv, wg, wu, ws);
  k_attn<<<256, 512, K2_LDS, stream>>>(ws);
  k_n80<false><<<256, 256, N80<false>::LDS, stream>>>(x, wo, ws, out);
  k_gateup<<<256, 256, K4_LDS, stream>>>(wg, wu, ws);
  k_n80<true><<<256, 256, N80<true>::LDS, stream>>>(x, wd, ws, out);
}
```

```cpp
#include <hip/hip_runtime.h>
#include <stdint.h>
#include <type_traits>

#define LAS __attribute__((address_space(3)))
#define DI __device__ __forceinline__
typedef unsigned short u16;
typedef short bf16x8 __attribute__((ext_vector_type(8)));
typedef short bf16x4 __attribute__((ext_vector_type(4)));
typedef __bf16 bf16x2_t __attribute__((ext_vector_type(2)));
typedef float f32x2 __attribute__((ext_vector_type(2)));
typedef float f32x4 __attribute__((ext_vector_type(4)));
typedef float f32x16 __attribute__((ext_vector_type(16)));
typedef unsigned u32x4 __attribute__((ext_vector_type(4)));
typedef unsigned u32x2 __attribute__((ext_vector_type(2)));
typedef LAS char* ldsp;
typedef __amdgpu_buffer_rsrc_t rsrc_t;

constexpr int S_ = 2048, D_ = 640, NH = 32, DF = 1536;
constexpr float LN_EPS = 1e-5f;
constexpr float QSCALE = 0.22360679774997896f * 1.4426950408889634f;
constexpr unsigned ONES2 = 0x3f803f80u;
#define AUX_ST 16

constexpr int V_TILE = 1344;
constexpr int WS_QK = 0;
constexpr int WS_V = WS_QK + 160 * S_ * 16;
constexpr int WS_O = WS_V + NH * 64 * V_TILE;
constexpr int WS_H = WS_O + S_ * D_ * 2;
constexpr int WS_ACT = WS_H + S_ * D_ * 2;
constexpr int WS_ST = WS_ACT + S_ * DF * 2;
constexpr int WS_CS = WS_ST + S_ * 8 * 8;
constexpr int WS_WO = WS_CS + 8 * 3072 * 4;
constexpr int WS_WGU = WS_WO + D_ * D_ * 2;
constexpr int WS_WD = WS_WGU + D_ * DF * 4;
constexpr int WS_END = WS_WD + D_ * DF * 2;

#define TS_DECL
#define TS()
#define TS_END(kid)

template <int I, int N, class F> DI void sfor(F&& f) {
  if constexpr (I < N) { f(std::integral_constant<int, I>{}); sfor<I + 1, N>(f); }
}
#define SB0() __builtin_amdgcn_sched_barrier(0)
DI unsigned cvtpk(float lo, float hi) { f32x2 v = {lo, hi}; return __builtin_bit_cast(unsigned, __builtin_convertvector(v, bf16x2_t)); }
DI float bflo(unsigned u) { return __uint_as_float(u << 16); }
DI float bfhi(unsigned u) { return __uint_as_float(u & 0xffff0000u); }
DI float dot2(unsigned a, unsigned b, float c) {
  return __builtin_amdgcn_fdot2_f32_bf16(__builtin_bit_cast(bf16x2_t, a), __builtin_bit_cast(bf16x2_t, b), c, false);
}
DI f32x4 mfma16(bf16x8 a, bf16x8 b, f32x4 c) { return __builtin_amdgcn_mfma_f32_16x16x32_bf16(a, b, c, 0, 0, 0); }
DI f32x16 mfma32(bf16x8 a, bf16x8 b, f32x16 c) { return __builtin_amdgcn_mfma_f32_32x32x16_bf16(a, b, c, 0, 0, 0); }
DI void set_m0(unsigned v) { asm volatile("s_mov_b32 m0, %0\n\ts_nop 0" ::"s"(v) : "memory"); }
template <int OFF> DI void dma(const void* sbase, unsigned voff) {
  asm volatile("global_load_lds_dwordx4 %0, %1 offset:%2" ::"v"(voff), "s"(sbase), "n"(OFF) : "memory");
}
template <int OFF> DI void dma_nt(const void* sbase, unsigned voff) {
  asm volatile("global_load_lds_dwordx4 %0, %1 offset:%2 nt" ::"v"(voff), "s"(sbase), "n"(OFF) : "memory");
}
template <int N> DI void wait_vm() { asm volatile("s_waitcnt vmcnt(%0)" ::"n"(N) : "memory"); }
template <int N> DI void wait_vm_bar() { asm volatile("s_waitcnt vmcnt(%0) lgkmcnt(0)\n\ts_barrier" ::"n"(N) : "memory"); }
DI void bar() { asm volatile("s_waitcnt lgkmcnt(0)\n\ts_barrier" ::: "memory"); }
template <class T> DI void pin(T& v) { asm volatile("" : "+v"(v)); }
DI bf16x8 lds_ld16(ldsp p) { return *(const LAS bf16x8*)p; }
DI bf16x4 lds_tr(ldsp p) { return __builtin_amdgcn_ds_read_tr16_b64_v4i16((LAS bf16x4*)p); }
DI bf16x8 lds_tr2(ldsp p0, ldsp p1) {
  bf16x4 a = lds_tr(p0), b = lds_tr(p1);
  return __builtin_shufflevector(a, b, 0, 1, 2, 3, 4, 5, 6, 7);
}
DI float xsum32(float v) {
  auto r = __builtin_amdgcn_permlane32_swap(__float_as_uint(v), __float_as_uint(v), false, false);
  return __uint_as_float(r[0]) + __uint_as_float(r[1]);
}
DI float xsum16_32(float v) {
  auto r = __builtin_amdgcn_permlane16_swap(__float_as_uint(v), __float_as_uint(v), false, false);
  return xsum32(__uint_as_float(r[0]) + __uint_as_float(r[1]));
}
DI rsrc_t mk_rsrc(void* p, int bytes) { return __builtin_amdgcn_make_buffer_rsrc(p, 0, bytes, 0x00020000); }
template <int AUX = 16> DI void st16(rsrc_t r, int off, u32x4 v) { __builtin_amdgcn_raw_buffer_store_b128(v, r, off, 0, AUX); }
template <int AUX = 16> DI void st8(rsrc_t r, int off, u32x2 v) { __builtin_amdgcn_raw_buffer_store_b64(v, r, off, 0, AUX); }
#define AUX3 0
#define AUX4 16

template <int P, int NT, int NS, int X, class F, class G0, class G> DI void loader_loop(F&& issue, G0&& early, G&& extra) {
  static_assert(NS >= 4, "three stages in flight + one being read");
  issue(0); issue(1);
  wait_vm_bar<P>();
  early();
  issue(2); issue(3);
  int slot = 4 % NS;
#pragma unroll 1
  for (int t = 1; t < NT - 3; ++t) {
    wait_vm_bar<2 * P>();
    issue(slot);
    slot = slot == NS - 1 ? 0 : slot + 1;
  }
  extra();
  wait_vm_bar<2 * P + X>(); wait_vm_bar<P + X>(); wait_vm_bar<X>();
}
DI void touch(const char* p, int lane, int nlines, unsigned lds_scratch) {
  set_m0(lds_scratch);
  asm volatile("global_load_lds_dword %0, %1" ::"v"((unsigned)((lane < nlines ? lane : nlines - 1) * 128)), "s"(p) : "memory");
}
template <int P> DI void dma_run(const char* src, unsigned dst, unsigned lane16) {
  static_assert(P <= 8, "offset range");
  set_m0(dst + 4096);
  sfor<0, P>([&](auto ic) { dma<decltype(ic)::value * 1024 - 4096>(src + 4096, lane16); });
}
template <int P, int NT, int NS, int SSTR, int LSTR, int X, class G0, class G> DI void linear_loader(const char* src, unsigned dst, unsigned lane16, G0&& early, G&& extra) {
  loader_loop<P, NT, NS, X>([&](int slot) { dma_run<P>(src, dst + slot * LSTR, lane16); src += SSTR; }, early, extra);
}

template <int KIND> DI void pack_issue(const u16* wsrc, const u16* wsrc2, int ks, int cg, unsigned ldsdst, int lane) {
  constexpr int PITCH = KIND == 1 ? DF * 2 : D_ * 2;
  set_m0(ldsdst);
  sfor<0, 4>([&](auto ic) {
    constexpr int g = decltype(ic)::value;
    const int t = (lane >> 5) & 1, q = (lane >> 3) & 3;
    int row;
    if (KIND == 0) row = ks < 16 ? 20 * (2 * ks + (g >> 1)) + 8 * t + 4 * (g & 1) + q : 20 * (8 * (ks - 16) + 2 * g + t) + 16 + q;
    else if (KIND == 1) row = 32 * (ks >> 1) + 16 * t + 4 * (2 * (ks & 1) + (g & 1)) + q;
    else row = 32 * ks + 16 * (g >> 1) + 8 * t + 4 * (g & 1) + q;
    dma_nt<g * 1024>((const char*)(KIND == 1 && g >= 2 ? wsrc2 : wsrc) + cg * 128 - 4096, (unsigned)(row * PITCH + (lane & 7) * 16 + 4096 - g * 1024));
  });
}
template <int KIND> DI void pack_finish(ldsp u, rsrc_t rw, int ks, int cg, int lane, LAS float* cs) {
  const int q = (lane & 15) >> 2, p = lane & 3;
  ldsp a = KIND == 1 ? u + ((lane >> 4) & 1) * 2048 + (8 * (lane >> 5) + q) * 128 + p * 8 : u + (8 * (lane >> 4) + q) * 128 + p * 8;
#pragma unroll
  for (int i = 0; i < 4; ++i) {
    const int nt = 4 * cg + i;
    const u32x4 f = __builtin_bit_cast(u32x4, lds_tr2(a + i * 32, a + i * 32 + 512));
    if (KIND == 1) {
      const int ft = nt / 6, j = nt - ft * 6;
      st16(rw, WS_WGU + ((ft * 40 + ks) * 6 + j) * 1024 + lane * 16, f);
      const unsigned sv = (lane & 15) == ((lane >> 4) & 1) ? ONES2 : 0u;
      const f32x4 c = mfma16(__builtin_bit_cast(bf16x8, (u32x4){sv, sv, sv, sv}), __builtin_bit_cast(bf16x8, f), (f32x4){0.f, 0.f, 0.f, 0.f});
      if (lane < 16) { cs[i * 32 + lane] = c[0]; cs[i * 32 + 16 + lane] = c[1]; }
    } else {
      const int ct = nt / 5, j = nt - ct * 5;
      st16(rw, (KIND == 0 ? WS_WO : WS_WD) + ((ct * (KIND == 0 ? 20 : 48) + ks) * 5 + j) * 1024 + lane * 16, f);
    }
  }
}

#define WD0 8
#define WD1 24
constexpr int K1_NS = 4, K1_STG = 32768, K1_NT = 10, K1_CTL = K1_NS * K1_STG, K1_LDS = K1_CTL + 4096;

template <bool ISV> DI void qkv_compute(ldsp lds, char* __restrict__ ws, int mt, int nt, int w, int lane) {
  const int wm = w >> 1, wn = w & 1, r32 = lane & 31, hi = lane >> 5, q = (lane & 15) >> 2, p = lane & 3, blk = (lane >> 4) & 1;
  TS_DECL;
  ldsp ctl = lds + K1_CTL;
  int aoff[4], woff[2];
#pragma unroll
  for (int s = 0; s < 4; ++s) aoff[s] = (wm * 64 + wn * 32 + r32) * 128 + (((2 * s + hi) ^ ((r32 >> 1) & 7)) * 16);
  const int astep = wn ? -4096 : 4096;
#pragma unroll
  for (int j = 0; j < 2; ++j) woff[j] = 16384 + (8 * hi + q) * 256 + (((wn * 2 + (j ^ wm)) ^ q) * 64) + blk * 32 + p * 8;
  f32x16 acc[2][2];
#pragma unroll
  for (int j = 0; j < 2; ++j)
#pragma unroll
    for (int i = 0; i < 2; ++i) { acc[j][i] = (f32x16){}; pin(acc[j][i]); }
  f32x4 rs = {}, rq = {}, cs = {};
  const unsigned sv = (lane & 15) == blk ? ONES2 : 0u;
  const bf16x8 sel = __builtin_bit_cast(bf16x8, (u32x4){sv, sv, sv, sv});
  bf16x8 xf[2][2], wf[2][2];
  auto rdx = [&](ldsp sb, int s, int i) { xf[s & 1][i] = lds_ld16(sb + aoff[s] + (i ? astep : 0)); };
  auto rdw = [&](ldsp sb, int s, int j) { wf[s & 1][j] = lds_tr2(sb + woff[j] + s * 4096, sb + woff[j] + s * 4096 + 1024); };
  auto mm = [&](int s, int j, int i) { acc[j][i] = ISV ? mfma32(xf[s & 1][i], wf[s & 1][j], acc[j][i]) : mfma32(wf[s & 1][j], xf[s & 1][i], acc[j][i]); };
  auto step = [&](ldsp nb, int s, int sn, bool rd) {
    const bf16x8 xa = xf[s & 1][0];
    const bf16x4 xl = __builtin_shufflevector(xa, xa, 0, 1, 2, 3), xh = __builtin_shufflevector(xa, xa, 4, 5, 6, 7);
    mm(s, 0, 0); if (rd) { rdx(nb, sn, 0); rdw(nb, sn, 0); } SB0();
    rq = __builtin_amdgcn_mfma_f32_4x4x4bf16_1k(xl, xl, rq, 0, 0, 0);
    mm(s, 0, 1); if (rd) { rdx(nb, sn, 1); rdw(nb, sn, 1); } SB0();
    rs = mfma16(sel, xa, rs);
    mm(s, 1, 0); SB0();
    rq = __builtin_amdgcn_mfma_f32_4x4x4bf16_1k(xh, xh, rq, 0, 0, 0);
    mm(s, 1, 1); SB0();
    cs = mfma16(sel, wf[s & 1][0], cs); SB0();
  };
  bar();
  TS();
  rdx(lds, 0, 0); rdw(lds, 0, 0); rdx(lds, 0, 1); rdw(lds, 0, 1); SB0();
  sfor<0, K1_NT>([&](auto tc) {
    constexpr int t = decltype(tc)::value;
    ldsp sb = lds + (t % K1_NS) * K1_STG;
    step(sb, 0, 1, true); step(sb, 1, 2, true); step(sb, 2, 3, true);
    if (t + 1 < K1_NT) { bar(); SB0(); }
    step(lds + ((t + 1) % K1_NS) * K1_STG, 3, 0, t + 1 < K1_NT);
  });
  TS();
  LAS f32x2* rst = (LAS f32x2*)(ctl + 1024);
  LAS float* cst = (LAS float*)(ctl + 2048);
  {
    const float sq = xsum32(p == 0 ? rq[0] : p == 1 ? rq[1] : p == 2 ? rq[2] : rq[3]);
    const float mu = __uint_as_float(__builtin_amdgcn_permlane16_swap(__float_as_uint(rs[0]), __float_as_uint(rs[1]), false, false)[0]) * (1.f / D_), var = fmaxf(sq * (1.f / D_) - mu * mu, 0.f);
    if (hi == 0) rst[wm * 64 + wn * 32 + r32] = (f32x2){mu, __builtin_amdgcn_rsqf(var + LN_EPS)};
    if (lane < 16) { cst[wn * 64 + wm * 32 + lane] = cs[0]; cst[wn * 64 + wm * 32 + 16 + lane] = cs[1]; }
  }
  bar();
  TS();
  const rsrc_t rw = mk_rsrc(ws, WS_END);
  if constexpr (!ISV) {
    const float sc = nt < 5 ? QSCALE : 1.f;
#pragma unroll
    for (int i = 0; i < 2; ++i) {
      const int ml = wm * 64 + (i ^ wn) * 32 + r32;
      const f32x2 st = rst[ml];
      const float a = st.y * sc, nm = -st.x * a;
      const int ob = WS_QK + ((nt * 16 + wn * 8 + hi) * S_ + mt * 128 + ml) * 16;
#pragma unroll
      for (int j = 0; j < 2; ++j)
#pragma unroll
        for (int bp = 0; bp < 2; ++bp) {
          unsigned pk[2][2];
#pragma unroll
          for (int e = 0; e < 2; ++e) {
            const int b = 2 * bp + e;
            const f32x4 c4 = *(const LAS f32x4*)(cst + wn * 64 + (j ^ wm) * 32 + 8 * b + 4 * hi);
            const f32x2 v0 = (f32x2){acc[j][i][4 * b], acc[j][i][4 * b + 1]} * a + nm * (f32x2){c4[0], c4[1]};
            const f32x2 v1 = (f32x2){acc[j][i][4 * b + 2], acc[j][i][4 * b + 3]} * a + nm * (f32x2){c4[2], c4[3]};
            pk[e][0] = cvtpk(v0[0], v0[1]); pk[e][1] = cvtpk(v1[0], v1[1]);
          }
          auto r0 = __builtin_amdgcn_permlane32_swap(pk[0][0], pk[1][0], false, false);
          auto r1 = __builtin_amdgcn_permlane32_swap(pk[0][1], pk[1][1], false, false);
          st16(rw, ob + ((j ^ wm) * 4 + bp * 2) * S_ * 16, (u32x4){r0[0], r1[0], r0[1], r1[1]});
        }
    }
  } else {
#pragma unroll
    for (int j = 0; j < 2; ++j) {
      const int nl = wn * 64 + (j ^ wm) * 32 + r32, n = (nt - 10) * 128 + nl, h = n / 20, d = n - h * 20;
      const float c = cst[nl];
      const int ob = WS_V + (h * 64 + mt * 4 + wm * 2) * V_TILE + hi * 336 + d * 16;
#pragma unroll
      for (int i = 0; i < 2; ++i)
#pragma unroll
        for (int s = 0; s < 2; ++s) {
          unsigned pk[4];
#pragma unroll
          for (int e = 0; e < 2; ++e) {
            const int b = 2 * s + e;
            const LAS f32x4* sp = (const LAS f32x4*)(rst + wm * 64 + (i ^ wn) * 32 + 8 * b + 4 * hi);
            const f32x4 s01 = sp[0], s23 = sp[1];
            pk[2 * e] = cvtpk((acc[j][i][4 * b] - s01[0] * c) * s01[1], (acc[j][i][4 * b + 1] - s01[2] * c) * s01[3]);
            pk[2 * e + 1] = cvtpk((acc[j][i][4 * b + 2] - s23[0] * c) * s23[1], (acc[j][i][4 * b + 3] - s23[2] * c) * s23[3]);
          }
          const int o = ob + (i ^ wn) * V_TILE + s * 672;
          st16(rw, o, (u32x4){pk[0], pk[1], pk[2], pk[3]});
          if (d == 19) st16(rw, o + 16, (u32x4){ONES2, ONES2, ONES2, ONES2});
        }
    }
  }
  TS();
  if (w == 0) { TS_END(0); }
}

DI void qkv_loader(ldsp lds, const u16* __restrict__ x, const u16* __restrict__ wqkv, int mt, int nt, int lw, int lane) {
  const bool isx = lw < 2;
  const int half = lw & 1;
  unsigned voff[8];
#pragma unroll
  for (int i = 0; i < 8; ++i) {
    const int r = (half * 8 + i) * 8 + (lane >> 3), c = (lane & 7) ^ ((r >> 1) & 7);
    const int k = (half * 8 + i) * 4 + (lane >> 4), cp = lane & 15;
    voff[i] = (isx ? r * 1280 + c * 16 : k * 3840 + (cp ^ ((k & 3) << 2)) * 16) + 8192 - i * 1024;
  }
  const char* src = (isx ? (const char*)x + (size_t)mt * 128 * 1280 : (const char*)wqkv + nt * 256) - 4096;
  const int sstr = isx ? 128 : 64 * 3840;
  const unsigned dst = (unsigned)(uintptr_t)lds + lw * 8192 + 4096;
  loader_loop<8, K1_NT, K1_NS, 0>([&](int slot) {
    set_m0(dst + slot * K1_STG);
    sfor<0, 8>([&](auto ic) { constexpr int i = decltype(ic)::value; dma<i * 1024 - 4096>(src, voff[i]); });
    src += sstr;
  }, [] {}, [] {});
}

__global__ __launch_bounds__(512, 2) void k_qkv(const u16* __restrict__ x, const u16* __restrict__ wqkv, const u16* __restrict__ wo, char* __restrict__ ws) {
  extern __shared__ __attribute__((aligned(16))) char smem[];
  ldsp lds = (ldsp)smem;
  const int lane = threadIdx.x & 63, w = __builtin_amdgcn_readfirstlane(threadIdx.x >> 6);
  const int xcd = blockIdx.x & 7, idx = blockIdx.x >> 3;
  const int nh = xcd & 1, nt = nh * 8 + (idx >> 2), mt = (xcd >> 1) * 4 + (idx & 3);
  if (w < 2) {
    const int id = blockIdx.x * 64 + lane;
    const bool isx = blockIdx.x < 128;
    const int i2 = id - 8192 < 7680 ? id - 8192 : 7679;
    const unsigned off = isx ? (id >> 2) * 1280 + 256 + w * 512 + (id & 3) * 128 : (128 + w * 256) * 3840 + i2 * 128;
    set_m0((unsigned)(uintptr_t)lds + K1_CTL + 3072 + w * 256);
    const char* src = isx ? (const char*)x : (const char*)wqkv;
    asm volatile("" : "+s"(src));
    if (w == 0) __builtin_amdgcn_s_sleep(WD0); else __builtin_amdgcn_s_sleep(WD1);
    asm volatile("global_load_lds_dword %0, %1" ::"v"(off), "s"(src) : "memory");
  }
  if (nt == 15) {
    const int u0 = mt * 8 + w;
    const rsrc_t rw = mk_rsrc(ws, WS_END);
#pragma unroll
    for (int r = 0; r < 2; ++r) { const int u = u0 + r * 128; if (u < 200) pack_issue<0>(wo, wo, u / 10, u % 10, (unsigned)(uintptr_t)lds + (w * 2 + r) * 4096, lane); }
    wait_vm<0>();
#pragma unroll
    for (int r = 0; r < 2; ++r) { const int u = u0 + r * 128; if (u < 200) pack_finish<0>(lds + (w * 2 + r) * 4096, rw, u / 10, u % 10, lane, nullptr); }
    return;
  }
  if (w >= 4) { qkv_loader(lds, x, wqkv, mt, nt, w - 4, lane); return; }
  if (nt >= 10) qkv_compute<true>(lds, ws, mt, nt, w, lane);
  else qkv_compute<false>(lds, ws, mt, nt, w, lane);
}

#define VLEAD 3
constexpr int K2_V = 24576, K2_PK = K2_V + 16 * V_TILE + 256, K2_CS = K2_PK + 8 * 4096, K2_MSK = K2_CS + 3072, K2_TM = K2_MSK + 2048, K2_LDS = K2_TM + 8192;

DI void attn_helper(ldsp lds, char* __restrict__ ws, int c, int h, int hw, int lane, const u16* __restrict__ wg, const u16* __restrict__ wu, const u16* __restrict__ wd) {
  const unsigned lds0 = (unsigned)(uintptr_t)lds;
  const int pm = 5 * (h >> 1) + 3 * (h & 1), pt = 5 * (h >> 1) + 2;
  const bool EDGE = c == 0;
  {
    const int tok0 = 256 * c - 256 + 128 * hw + ((EDGE && hw < 2) ? 256 : 0);
    const char* kb = ws + WS_QK + (80 * S_ + tok0) * 16;
    set_m0(lds0 + hw * 2048); dma<0>(kb + pm * S_ * 16, lane * 16); dma<1024>(kb + pm * S_ * 16, lane * 16);
    set_m0(lds0 + 8192 + hw * 2048); dma<0>(kb + (pm + 1) * S_ * 16, lane * 16); dma<1024>(kb + (pm + 1) * S_ * 16, lane * 16);
    set_m0(lds0 + 16384 + hw * 2048); dma<0>(kb + pt * S_ * 16, lane * 16); dma<1024>(kb + pt * S_ * 16, lane * 16);
  }
  const int b = blockIdx.x;
  if (lane == 0) *(LAS unsigned*)(lds + K2_CS + 2560 + 16 + hw * 4) = 0u;
  if (hw == 0 && lane == 0) *(LAS unsigned*)(lds + K2_CS + 2560) = 0u;
  if (hw == 1 && lane < 16) *(LAS u32x2*)(lds + K2_TM + lane * 512) = (u32x2){EDGE && lane < 8 ? 0xff80u : 0u, 0u};
  wait_vm<VLEAD>();
  {
    const char* vb = ws + WS_V + (h * 64 + 8 * c - 8) * V_TILE + 4096;
    set_m0(lds0 + K2_V + hw * 5120 + 4096);
    sfor<0, 6>([&](auto ic) {
      constexpr int i = decltype(ic)::value;
      unsigned off = hw * 5120 + lane * 16;
      if (EDGE && off + i * 1024 < 8u * V_TILE) off += 8 * V_TILE;
      dma<i * 1024 - 4096>(vb, off);
    });
    wait_vm_bar<6>();
    wait_vm<0>();
    if (lane == 0) *(volatile LAS unsigned*)(lds + K2_CS + 2560 + 16 + hw * 4) = 1u;
  }
  const rsrc_t rw = mk_rsrc(ws, WS_END);
  const unsigned u0 = lds0 + K2_PK + hw * 4096;
  if (b < 192) {
    const int pcg = b % 24, pks = (b / 24) * 5;
    pack_issue<1>(wg, wu, pks + hw, pcg, u0, lane);
    if (hw == 0) pack_issue<1>(wg, wu, pks + 4, pcg, u0 + 16384, lane);
    if (hw >= 2) touch(ws + WS_WO + (b & 1) * 409600 + ((b >> 3) * 2 + hw - 2) * 6400, lane, 50, lds0 + K2_CS + 2816);
    wait_vm<0>();
    LAS float* ct = (LAS float*)(lds + K2_CS);
    pack_finish<1>(lds + K2_PK + hw * 4096, rw, pks + hw, pcg, lane, ct + hw * 128);
    if (hw == 0) pack_finish<1>(lds + K2_PK + 16384, rw, pks + 4, pcg, lane, ct + 512);
    asm volatile("s_waitcnt lgkmcnt(0)" ::: "memory");
    unsigned old = 0;
    if (lane == 0) old = __hip_atomic_fetch_add((LAS unsigned*)(lds + K2_CS + 2560), 1u, __ATOMIC_RELAXED, __HIP_MEMORY_SCOPE_WORKGROUP);
    if (__builtin_amdgcn_readfirstlane(old) == 3) {
      asm volatile("" ::: "memory");
      const LAS f32x2* c2 = (const LAS f32x2*)ct + lane;
      const f32x2 a = ((c2[0] + c2[64]) + (c2[128] + c2[192])) + c2[256];
      const int e = pcg * 128 + lane * 2, ft = e / 192;
      st8(rw, WS_CS + ((ft * 8 + b / 24) * 192 + e - ft * 192) * 4, __builtin_bit_cast(u32x2, a));
    }
  } else {
    const int bb = b < 252 ? b - 192 : 0, pcg = bb % 10, pks = (bb / 10) * 8 + hw;
    pack_issue<2>(wd, wd, pks, pcg, u0, lane);
    pack_issue<2>(wd, wd, pks + 4, pcg, u0 + 16384, lane);
    if (hw >= 2) touch(ws + WS_WO + (b & 1) * 409600 + ((b >> 3) * 2 + hw - 2) * 6400, lane, 50, lds0 + K2_CS + 2816);
    wait_vm<0>();
    pack_finish<2>(lds + K2_PK + hw * 4096, rw, pks, pcg, lane, nullptr);
    pack_finish<2>(lds + K2_PK + hw * 4096 + 16384, rw, pks + 4, pcg, lane, nullptr);
  }
}

DI void attn_body(ldsp lds, char* __restrict__ ws, int c, int h, int w, int lane) {
  const int r32 = lane & 31, hi = lane >> 5;
  TS_DECL;
  const int pm = 5 * (h >> 1) + 3 * (h & 1), pt = 5 * (h >> 1) + 2;
  const int dq = r32 - 4 * hi;
  const float NEG = -__builtin_inff();
  f32x16 s[8], s8;
#pragma unroll
  for (int r = 0; r < 16; ++r) {
    const bool le = (r & 3) + 8 * (r >> 2) <= dq;
    s8[r] = le ? 0.f : NEG;
    s[0][r] = le ? NEG : 0.f;
  }
  if (w == 0) {
    u32x4 msk[2];
#pragma unroll
    for (int e = 0; e < 8; ++e) msk[e >> 2][e & 3] = (unsigned)((int)__float_as_uint(s8[2 * e]) >> 31) >> 16 | ((unsigned)((int)__float_as_uint(s8[2 * e + 1]) >> 31) << 16);
    *(LAS u32x4*)(lds + K2_MSK + lane * 16) = msk[0]; *(LAS u32x4*)(lds + K2_MSK + 1024 + lane * 16) = msk[1];
  }
  pin(s[0]); pin(s8);
  SB0();
  const char* qb = ws + WS_QK + (256 * c + 32 * w) * 16;
  const bf16x8 qm = *(const bf16x8*)(qb + ((pm + hi) * S_ + r32) * 16);
  u32x2 qtr = *(const u32x2*)(qb + (pt * S_ + r32) * 16 + (h & 1) * 8);
  if (hi) qtr = (u32x2){0x3f80u, 0u};
  bf16x4 qt = __builtin_bit_cast(bf16x4, qtr);
  pin(qt);
  TS();
  bar();
  TS();
  ldsp kp = lds + hi * 8192 + (w * 32 + r32) * 16;
  ldsp ktp = lds + (hi ? K2_TM + w * 512 : 16384 + (w * 32 + r32) * 16 + (h & 1) * 8);
  ldsp vp = lds + K2_V + w * V_TILE + hi * 336 + r32 * 16;
  bf16x8 km[9]; bf16x4 kt[9];
#pragma unroll
  for (int jj = 0; jj < 9; ++jj) { const int j = jj == 0 ? 8 : jj - 1; km[j] = lds_ld16(kp + j * 512); kt[j] = *(const LAS bf16x4*)(ktp + j * 512); }
  SB0();
  auto tail = [&](int j, f32x16 cin) { return __builtin_amdgcn_mfma_f32_32x32x8bf16_1k(kt[j], qt, cin, 0, 0, 0); };
  float mx[4];
  auto maxh = [&](int j, int r0) {
#pragma unroll
    for (int e = 0; e < 4; ++e) mx[e] = __builtin_elementwise_maximum(__builtin_elementwise_maximum(mx[e], s[j][r0 + 2 * e]), s[j][r0 + 2 * e + 1]);
  };
  s8 = mfma32(km[8], qm, s8); s[0] = mfma32(km[0], qm, s[0]); s8 = tail(8, s8);
  s[1] = mfma32(km[1], qm, (f32x16){}); s[0] = tail(0, s[0]);
  s[2] = mfma32(km[2], qm, (f32x16){}); s[1] = tail(1, s[1]); SB0();
  s[3] = mfma32(km[3], qm, (f32x16){});
#pragma unroll
  for (int r = 0; r < 8; ++r) s[0][r] = __builtin_elementwise_maximum(s[0][r], s8[r]);
  SB0();
  s[2] = tail(2, s[2]);
#pragma unroll
  for (int r = 8; r < 16; ++r) s[0][r] = __builtin_elementwise_maximum(s[0][r], s8[r]);
  pin(s[0]);
  SB0();
#pragma unroll
  for (int e = 0; e < 4; ++e) mx[e] = s[0][e];
  s[4] = mfma32(km[4], qm, (f32x16){}); maxh(0, 4); mx[0] = __builtin_elementwise_maximum(__builtin_elementwise_maximum(mx[0], s[0][12]), s[0][13]);
  mx[1] = __builtin_elementwise_maximum(__builtin_elementwise_maximum(mx[1], s[0][14]), s[0][15]); SB0();
  s[3] = tail(3, s[3]); maxh(1, 0); SB0();
  s[5] = mfma32(km[5], qm, (f32x16){}); maxh(1, 8); SB0();
  s[4] = tail(4, s[4]); maxh(2, 0); SB0();
  s[6] = mfma32(km[6], qm, (f32x16){}); maxh(2, 8); SB0();
  s[5] = tail(5, s[5]); maxh(3, 0); SB0();
  s[7] = mfma32(km[7], qm, (f32x16){}); maxh(3, 8); SB0();
  s[6] = tail(6, s[6]); maxh(4, 0); SB0();
  s[7] = tail(7, s[7]); maxh(4, 8);
#pragma unroll
  for (int j = 5; j < 8; ++j) { maxh(j, 0); maxh(j, 8); }
  const float m0 = __builtin_elementwise_maximum(mx[0], mx[1]), m1 = __builtin_elementwise_maximum(mx[2], mx[3]);
  float m = __builtin_elementwise_maximum(m0, m1);
  {
    auto rr = __builtin_amdgcn_permlane32_swap(__float_as_uint(m), __float_as_uint(m), false, false);
    m = __builtin_elementwise_maximum(__uint_as_float(rr[0]), __uint_as_float(rr[1]));
  }
  bf16x4 nm, one4;
  {
    const unsigned u0 = __float_as_uint(m) & 0xffff0000u;
    const float r1 = m - __uint_as_float(u0);
    const unsigned u1 = __float_as_uint(r1) & 0xffff0000u;
    const float r2 = r1 - __uint_as_float(u1);
    const u32x2 v = {hi ? 0u : ((u0 >> 16) | u1), hi ? 0u : (__float_as_uint(r2) >> 16)};
    nm = __builtin_bit_cast(bf16x4, v);
    one4 = __builtin_bit_cast(bf16x4, (u32x2){0xbf80bf80u, 0xbf80bf80u});
  }
  SB0();
  TS();
  f32x16 o = {};
  u32x4 pk[8][2];
  auto sub = [&](int j) { s[j] = __builtin_amdgcn_mfma_f32_32x32x8bf16_1k(one4, nm, s[j], 0, 0, 0); };
  auto ex8 = [&](int j, int ks) {
#pragma unroll
    for (int e = 0; e < 4; ++e) pk[j][ks][e] = cvtpk(__builtin_amdgcn_exp2f(s[j][8 * ks + 2 * e]), __builtin_amdgcn_exp2f(s[j][8 * ks + 2 * e + 1]));
  };
  auto vld = [&](int slot, int ks) { return lds_ld16(vp + slot * V_TILE + ks * 672); };
  auto pv = [&](bf16x8 v, u32x4 p) { o = mfma32(v, __builtin_bit_cast(bf16x8, p), o); };
  sub(7); sub(6); sub(5);
  __builtin_amdgcn_s_setprio(0);
  ex8(7, 0); ex8(7, 1);
  const u32x4 vrdy = *(volatile LAS u32x4*)(lds + K2_CS + 2560 + 16);
  ex8(6, 0); ex8(6, 1);
  pin(pk[7][0]); pin(pk[7][1]); pin(pk[6][0]); pin(pk[6][1]);
  if (__builtin_expect(__builtin_amdgcn_readfirstlane((vrdy[0] & vrdy[1]) & (vrdy[2] & vrdy[3])) == 0, 0)) {
    for (int sp = 0; sp < (1 << 20); ++sp) {
      const u32x4 v = *(volatile LAS u32x4*)(lds + K2_CS + 2560 + 16);
      if (__builtin_amdgcn_readfirstlane((v[0] & v[1]) & (v[2] & v[3]))) break;
      __builtin_amdgcn_s_sleep(1);
    }
  }
  asm volatile("" ::: "memory");
  bf16x8 vf[9][2];
  vf[7][0] = vld(7, 0); vf[7][1] = vld(7, 1);
  SB0();
  u32x4 mk[2];
#pragma unroll
  for (int j = 5; j >= 0; --j) {
    vf[j + 1][0] = vld(j + 1, 0); vf[j + 1][1] = vld(j + 1, 1);
    if (j == 0) { vf[0][0] = vld(0, 0); vf[8][0] = vld(8, 0); vf[0][1] = vld(0, 1); vf[8][1] = vld(8, 1); }
    if (j == 1) { mk[0] = *(const LAS u32x4*)(lds + K2_MSK + lane * 16); mk[1] = *(const LAS u32x4*)(lds + K2_MSK + 1024 + lane * 16); }
    if (j > 0) sub(j - 1);
    SB0();
    ex8(j, 0); pv(vf[j + 2][0], pk[j + 2][0]); SB0();
    ex8(j, 1); pv(vf[j + 2][1], pk[j + 2][1]); SB0();
  }
  pv(vf[1][0], pk[1][0]); pv(vf[1][1], pk[1][1]);
#pragma unroll
  for (int ks = 0; ks < 2; ++ks) {
    const u32x4 p0 = pk[0][ks] & mk[ks], p8 = pk[0][ks] ^ p0;
    pv(vf[0][ks], p0); pv(vf[8][ks], p8);
  }
  TS();
  const float l = __uint_as_float(__builtin_amdgcn_permlane32_swap(__float_as_uint(o[8]), __float_as_uint(o[8]), false, false)[1]);
  const float il = __builtin_amdgcn_rcpf(l);
  const int tok = 256 * c + 32 * w + r32;
  const rsrc_t rw = mk_rsrc(ws, WS_END);
  const int ob = WS_O + (tok >> 6) * 20 * 4096 + ((tok >> 4) & 3) * 1024 + (tok & 15) * 16;
  const u32x4 w0 = {cvtpk(o[0] * il, o[1] * il), cvtpk(o[2] * il, o[3] * il), cvtpk(o[4] * il, o[5] * il), cvtpk(o[6] * il, o[7] * il)};
  st16(rw, ob + (h >> 1) * 4096 + (2 * (h & 1) + hi) * 256, w0);
  if (hi == 0) st8(rw, ob + (16 + (h >> 3)) * 4096 + ((h >> 1) & 3) * 256 + (h & 1) * 8, (u32x2){cvtpk(o[8] * il, o[9] * il), cvtpk(o[10] * il, o[11] * il)});
  TS();
  if (w == 7 || w == 0) { TS_END(w == 7 ? 1 : 5); }
}

__global__ __launch_bounds__(768, 3) void k_attn(char* __restrict__ ws, const u16* __restrict__ wg, const u16* __restrict__ wu, const u16* __restrict__ wd) {
  extern __shared__ __attribute__((aligned(16))) char smem[];
  const int lane = threadIdx.x & 63, w = __builtin_amdgcn_readfirstlane(threadIdx.x >> 6);
  const int xcd = blockIdx.x & 7, idx = blockIdx.x >> 3, h = xcd * 4 + (idx >> 3), c = idx & 7;
  if (w < 4) attn_helper((ldsp)smem, ws, c, h, w, lane, wg, wu, wd);
  else attn_body((ldsp)smem, ws, c, h, w - 4, lane);
}

constexpr int N80_NS = 4, N80_STG = 18432, N80_CTL = N80_NS * N80_STG, N80_RES = N80_CTL + 20480, N80_LDS = N80_RES + 12288;

template <bool DOWN>
__global__ __launch_bounds__(512, 2) void k_n80(const u16* __restrict__ x, char* __restrict__ ws, u16* __restrict__ out) {
  constexpr int NT = DOWN ? 24 : 10;
  extern __shared__ __attribute__((aligned(16))) char smem[];
  ldsp lds = (ldsp)smem;
  ldsp ctl = lds + N80_CTL;
  const int lane = threadIdx.x & 63, w = __builtin_amdgcn_readfirstlane(threadIdx.x >> 6);
  const unsigned lds0 = (unsigned)(uintptr_t)lds;
  const int xcd = blockIdx.x & 7, idx = blockIdx.x >> 3, mt = (xcd >> 1) * 8 + (idx >> 2), ct = (xcd & 1) * 4 + (idx & 3);
  const int odd = ct & 1, pb = (ct >> 1) * 5 + (odd ? 3 : 0), ps = (ct >> 1) * 5 + 2;
  if (w >= 4) {
    const int lw = w - 4;
    auto extra = [&] {
      set_m0(lds0 + N80_RES + lw * 3072);
      if (DOWN) {
        const char* hs = ws + WS_H + ((mt >> 1) * 160 + (mt & 1) * 2 + (lw >> 1)) * 1024;
        const unsigned vo = (lane >> 5) * 4096 + (((lane >> 4) & 1) * 32 + (lw & 1) * 16 + (lane & 15)) * 16 + 2048;
        dma<0>(hs + pb * 8192 - 2048, vo); dma<1024>(hs + (pb + 1) * 8192 - 3072, vo); dma<2048>(hs + ps * 8192 - 4096, vo);
      } else {
        const char* xs = (const char*)x + (size_t)(mt * 64 + lw * 16) * 1280 + ct * 160;
        sfor<0, 3>([&](auto ic) {
          constexpr int i = decltype(ic)::value;
          const int ch = i * 64 + lane < 160 ? i * 64 + lane : 159, r = ch / 10;
          dma<i * 1024>(xs - 2048, (unsigned)(r * 1280 + (ch - r * 10) * 16 + 2048 - i * 1024));
        });
      }
    };
    if (w < 6) linear_loader<4, NT, N80_NS, 8192, N80_STG, 0>(ws + (DOWN ? WS_ACT : WS_O) + mt * NT * 8192 + lw * 4096, lds0 + lw * 4096, lane * 16, extra, [] {});
    else linear_loader<5, NT, N80_NS, 10240, N80_STG, 0>(ws + (DOWN ? WS_WD : WS_WO) + ct * NT * 10240 + (w - 6) * 5120, lds0 + 8192 + (w - 6) * 5120, lane * 16, extra, [] {});
    return;
  }
  const int fr = lane & 15, g = lane >> 4, wk = w & 1;
  TS_DECL;
  const int tok = mt * 64 + w * 16 + fr;
  const int hb = WS_H + (((mt >> 1) * 40 + (g >> 1)) * 4 + (mt & 1) * 2 + (w >> 1)) * 1024 + ((g & 1) * 32 + (w & 1) * 16 + fr) * 16;
  f32x4 acc[2][5];
#pragma unroll
  for (int i = 0; i < 2; ++i)
#pragma unroll
    for (int j = 0; j < 5; ++j) { acc[i][j] = (f32x4){0.f, 0.f, 0.f, 0.f}; pin(acc[i][j]); }
  bf16x8 af[2][2], wf[2][5];
  const int aoff = wk * 4096 + w * 1024 + lane * 16, aoff1 = aoff ^ 1024, woff = 8192 + wk * 5120 + lane * 16;
  auto rd1 = [&](ldsp sb, int bs, int n) {
    if (n == 0) af[bs][0] = lds_ld16(sb + aoff);
    else if (n == 2) af[bs][1] = lds_ld16(sb + aoff1);
    else if (n < 7) { const int j = n == 1 ? 0 : n - 2; wf[bs][j] = lds_ld16(sb + woff + j * 1024); }
  };
  bar();
  TS();
#pragma unroll
  for (int n = 0; n < 7; ++n) rd1(lds, 0, n);
  SB0();
  sfor<0, NT>([&](auto tc) {
    constexpr int t = decltype(tc)::value, bs = t & 1;
    if (t + 1 < NT) { bar(); SB0(); }
#pragma unroll
    for (int n = 0; n < 10; ++n) {
      acc[n & 1][n >> 1] = mfma16(wf[bs][n >> 1], af[bs][n & 1], acc[n & 1][n >> 1]);
      if (t + 1 < NT) rd1(lds + ((t + 1) % N80_NS) * N80_STG, bs ^ 1, n);
      SB0();
    }
  });
  TS();
#pragma unroll
  for (int j = 0; j < 5; ++j) *(LAS f32x4*)(ctl + (w * 5 + j) * 1024 + lane * 16) = acc[1][j];
  bar();
#pragma unroll
  for (int j = 0; j < 5; ++j) acc[0][j] += *(const LAS f32x4*)(ctl + ((w ^ 1) * 5 + j) * 1024 + lane * 16);
  TS();
  u32x2 res[5];
  if (DOWN) {
    ldsp rp = lds + N80_RES + w * 3072 + lane * 16;
#pragma unroll
    for (int j = 0; j < 5; ++j) {
      const int e = odd ? (j == 0 ? 5 : j - 1) : j;
      res[j] = *(const LAS u32x2*)(rp + (e >> 1) * 1024 + (e & 1) * 8);
    }
  } else {
#pragma unroll
    for (int j = 0; j < 5; ++j) res[j] = *(const LAS u32x2*)(lds + N80_RES + w * 3072 + fr * 160 + j * 32 + g * 8);
  }
  u32x2 hv[5];
  float s = 0.f;
#pragma unroll
  for (int j = 0; j < 5; ++j) {
    hv[j] = (u32x2){cvtpk(acc[0][j][0] + bflo(res[j][0]), acc[0][j][1] + bfhi(res[j][0])), cvtpk(acc[0][j][2] + bflo(res[j][1]), acc[0][j][3] + bfhi(res[j][1]))};
    s += (bflo(hv[j][0]) + bfhi(hv[j][0])) + (bflo(hv[j][1]) + bfhi(hv[j][1]));
  }
  if (DOWN) {
    char* op = (char*)out + (size_t)tok * 1280 + ct * 160;
#pragma unroll
    for (int jp = 0; jp < 2; ++jp) {
      auto r0 = __builtin_amdgcn_permlane16_swap(hv[2 * jp][0], hv[2 * jp + 1][0], false, false);
      auto r1 = __builtin_amdgcn_permlane16_swap(hv[2 * jp][1], hv[2 * jp + 1][1], false, false);
      *(u32x4*)(op + jp * 64 + (g & 1) * 32 + (g >> 1) * 16) = (u32x4){r0[0], r1[0], r0[1], r1[1]};
    }
    *(u32x2*)(op + 128 + g * 8) = hv[4];
  } else {
    const rsrc_t rw = mk_rsrc(ws, WS_END);
    const u32x2 a0 = odd ? hv[1] : hv[0], a1 = odd ? hv[2] : hv[1], b0 = odd ? hv[3] : hv[2], b1 = odd ? hv[4] : hv[3], s1 = odd ? hv[0] : hv[4];
    st16<AUX3>(rw, hb + pb * 8192, (u32x4){a0[0], a0[1], a1[0], a1[1]});
    st16<AUX3>(rw, hb + (pb + 1) * 8192, (u32x4){b0[0], b0[1], b1[0], b1[1]});
    st8<AUX3>(rw, hb + ps * 8192 + odd * 8, s1);
    const float mean = xsum16_32(s) * (1.f / 80.f);
    float m2 = 0.f;
#pragma unroll
    for (int j = 0; j < 5; ++j) {
      const float d0 = bflo(hv[j][0]) - mean, d1 = bfhi(hv[j][0]) - mean, d2 = bflo(hv[j][1]) - mean, d3 = bfhi(hv[j][1]) - mean;
      m2 += (d0 * d0 + d1 * d1) + (d2 * d2 + d3 * d3);
    }
    m2 = xsum16_32(m2);
    if (g == 0) st8<AUX3>(rw, WS_ST + (tok * 8 + ct) * 8, (u32x2){__float_as_uint(mean), __float_as_uint(m2)});
  }
  TS();
  if (w == 0) { TS_END(DOWN ? 4 : 2); }
}

constexpr int K4_NS = 4, K4_STG = 20480, K4_NT = 20, K4_CTL = K4_NS * K4_STG, K4_SD = K4_CTL + 2048, K4_LDS = K4_SD + 8192 + 6144;

__global__ __launch_bounds__(512, 2) void k_gateup(char* __restrict__ ws) {
  extern __shared__ __attribute__((aligned(16))) char smem[];
  ldsp lds = (ldsp)smem;
  ldsp ctl = lds + K4_CTL;
  const int lane = threadIdx.x & 63, w = __builtin_amdgcn_readfirstlane(threadIdx.x >> 6);
  const unsigned lds0 = (unsigned)(uintptr_t)lds;
  const int xcd = blockIdx.x & 7, idx = blockIdx.x >> 3, mt = (xcd >> 2) * 8 + (idx >> 2), ft = (xcd & 3) * 4 + (idx & 3);
  if (w >= 4) {
    auto extra = [&] { touch(ws + WS_WD + (xcd & 1) * 4 * 245760 + (idx * 4 + w - 4) * 7680, lane, 60, lds0 + K4_CTL); };
    if (w < 6) linear_loader<4, K4_NT, K4_NS, 8192, K4_STG, 1>(ws + WS_H + mt * K4_NT * 8192 + (w - 4) * 4096, lds0 + (w - 4) * 4096, lane * 16,
                                                               [&] { dma_run<4>(ws + WS_ST + mt * 8192 + (w - 4) * 4096, lds0 + K4_SD + (w - 4) * 4096, lane * 16); }, extra);
    else linear_loader<6, K4_NT, K4_NS, 12288, K4_STG, 1>(ws + WS_WGU + ft * K4_NT * 12288 + (w - 6) * 6144, lds0 + 8192 + (w - 6) * 6144, lane * 16,
                                                          [&] { dma_run<3>(ws + WS_CS + ft * 6144 + (w - 6) * 3072, lds0 + K4_SD + 8192 + (w - 6) * 3072, lane * 16); }, extra);
    return;
  }
  const int r32 = lane & 31, hi = lane >> 5, wm = w >> 1, wn = w & 1;
  TS_DECL;
  f32x16 acc[2][3];
#pragma unroll
  for (int i = 0; i < 2; ++i)
#pragma unroll
    for (int j = 0; j < 3; ++j) { acc[i][j] = (f32x16){}; pin(acc[i][j]); }
  bf16x8 af[2][2], wf[2][3];
  const int aoff = wm * 2048 + lane * 16, woff = 8192 + wn * 3072 + lane * 16;
  auto rda = [&](ldsp sb, int s, int i) { af[s][i] = lds_ld16(sb + aoff + s * 4096 + i * 1024); };
  auto rdw = [&](ldsp sb, int s, int j) { wf[s][j] = lds_ld16(sb + woff + s * 6144 + j * 1024); };
  auto mm = [&](int s, int i, int j) { acc[i][j] = mfma32(wf[s][j], af[s][i], acc[i][j]); };
  bar();
  const int tok = mt * 128 + wm * 64 + r32;
  f32x4 sv[2][2];
  float cv[8];
  const LAS float* cp = (const LAS float*)(lds + K4_SD + 8192) + w * 48 + (lane < 48 ? lane : 47);
  ldsp sp = lds + K4_SD + (wm * 64 + r32) * 64 + hi * 32;
  TS();
  rda(lds, 0, 0); rdw(lds, 0, 0); rda(lds, 0, 1); rdw(lds, 0, 1); rdw(lds, 0, 2); SB0();
  constexpr float GC0 = -2.f * 0.7978845608028654f * 1.4426950408889634f, GC1 = GC0 * 0.044715f;
  float ca[2], cb[2], nmu[2], var[2];
  sfor<0, K4_NT>([&](auto tc) {
    constexpr int t = decltype(tc)::value;
    constexpr bool more = t + 1 < K4_NT;
    ldsp sb = lds + (t % K4_NS) * K4_STG;
    ldsp nb = lds + ((t + 1) % K4_NS) * K4_STG;
    mm(0, 0, 0); rda(sb, 1, 0); rdw(sb, 1, 0); SB0();
    mm(0, 1, 0); rda(sb, 1, 1); rdw(sb, 1, 1); SB0();
    mm(0, 0, 1); rdw(sb, 1, 2); SB0();
    mm(0, 1, 1); SB0();
    mm(0, 0, 2); SB0();
    mm(0, 1, 2); SB0();
    if (more) { bar(); SB0(); }
    mm(1, 0, 0); if (more) { rda(nb, 0, 0); rdw(nb, 0, 0); } SB0();
    mm(1, 1, 0); if (more) { rda(nb, 0, 1); rdw(nb, 0, 1); } SB0();
    mm(1, 0, 1); if (more) rdw(nb, 0, 2); SB0();
    if constexpr (t >= 4 && t <= 7) { cv[2 * t - 8] = cp[(2 * t - 8) * 192]; cv[2 * t - 7] = cp[(2 * t - 7) * 192]; sv[(t - 4) >> 1][t & 1] = *(const LAS f32x4*)(sp + ((t - 4) >> 1) * 2048 + (t & 1) * 16); }
    if constexpr (t == 8) { if (lane < 48) ((LAS float*)(ctl + 1024))[w * 48 + lane] = ((cv[0] + cv[1]) + (cv[2] + cv[3])) + ((cv[4] + cv[5]) + (cv[6] + cv[7])); }
    if constexpr (t == 9 || t == 10) {
      constexpr int i = t - 9;
      const f32x4 s0 = sv[i][0], s1 = sv[i][1];
      const float mu = xsum32((s0[0] + s0[2]) + (s1[0] + s1[2])) * 0.125f;
      const float d0 = s0[0] - mu, d1 = s0[2] - mu, d2 = s1[0] - mu, d3 = s1[2] - mu;
      var[i] = xsum32((s0[1] + s0[3]) + (s1[1] + s1[3]) + 80.f * ((d0 * d0 + d1 * d1) + (d2 * d2 + d3 * d3))) * (1.f / D_) + LN_EPS;
      const float rstd = __builtin_amdgcn_rsqf(var[i]);
      ca[i] = GC0 * rstd; cb[i] = GC1 * rstd * rstd * rstd; nmu[i] = -mu;
    }
    mm(1, 1, 1);
    mm(1, 0, 2);
    mm(1, 1, 2);
    if constexpr (t == 9 || t == 10) { pin(ca[t - 9]); pin(cb[t - 9]); pin(nmu[t - 9]); pin(var[t - 9]); }
    SB0();
  });
  TS();
  const rsrc_t rw = mk_rsrc(ws, WS_END);
  const LAS float* csb = (const LAS float*)(ctl + 1024) + wn * 96 + 4 * hi;
  f32x4 cq[3][4];
#pragma unroll
  for (int j = 0; j < 3; ++j)
#pragma unroll
    for (int b = 0; b < 4; ++b) cq[j][b] = *(const LAS f32x4*)(csb + j * 32 + 8 * b);
#pragma unroll
  for (int i = 0; i < 2; ++i) {
    const int tk = tok + 32 * i;
    const int ob = WS_ACT + (((tk >> 6) * 48 + ft * 3) * 4 + ((tk >> 4) & 3)) * 1024 + (hi * 16 + (tk & 15)) * 16;
#pragma unroll
    for (int j = 0; j < 3; ++j) {
      unsigned pk[4];
#pragma unroll
      for (int r = 0; r < 8; r += 2) {
        const f32x2 gv = (f32x2){acc[i][j][r], acc[i][j][r + 1]} + nmu[i] * (f32x2){cq[j][r >> 2][r & 3], cq[j][r >> 2][(r & 3) + 1]};
        const f32x2 uv = (f32x2){acc[i][j][8 + r], acc[i][j][9 + r]} + nmu[i] * (f32x2){cq[j][2 + (r >> 2)][r & 3], cq[j][2 + (r >> 2)][(r & 3) + 1]};
        const f32x2 arg = gv * (gv * gv * cb[i] + ca[i]);
        const f32x2 ex = {__builtin_amdgcn_exp2f(arg[0]), __builtin_amdgcn_exp2f(arg[1])};
        const f32x2 den = ex * var[i] + var[i];
        const f32x2 rc = {__builtin_amdgcn_rcpf(den[0]), __builtin_amdgcn_rcpf(den[1])};
        const f32x2 a = gv * uv * rc;
        pk[r >> 1] = cvtpk(a[0], a[1]);
      }
      const int T = wn * 3 + j;
      st16<AUX4>(rw, ob + (T >> 1) * 4096 + (T & 1) * 512, (u32x4){pk[0], pk[1], pk[2], pk[3]});
    }
  }
  TS();
  if (w == 0) { TS_END(3); }
}

extern "C" void kernel_launch(void* const* d_in, const int* in_sizes, int n_in, void* d_out, int out_size, void* d_ws, size_t ws_size,
                              hipStream_t stream) {
  const u16* x = (const u16*)d_in[0];
  const u16* wqkv = (const u16*)d_in[1];
  const u16* wo = (const u16*)d_in[2];
  const u16* wg = (const u16*)d_in[4];
  const u16* wu = (const u16*)d_in[5];
  const u16* wd = (const u16*)d_in[6];
  char* ws = (char*)d_ws;
  u16* out = (u16*)d_out;
  (void)hipFuncSetAttribute((const void*)k_qkv, hipFuncAttributeMaxDynamicSharedMemorySize, K1_LDS);
  (void)hipFuncSetAttribute((const void*)k_attn, hipFuncAttributeMaxDynamicSharedMemorySize, K2_LDS);
  (void)hipFuncSetAttribute((const void*)k_n80<false>, hipFuncAttributeMaxDynamicSharedMemorySize, N80_LDS);
  (void)hipFuncSetAttribute((const void*)k_n80<true>, hipFuncAttributeMaxDynamicSharedMemorySize, N80_LDS);
  (void)hipFuncSetAttribute((const void*)k_gateup, hipFuncAttributeMaxDynamicSharedMemorySize, K4_LDS);
#define REP 1
  for (int r = 0; r < REP; ++r) k_qkv<<<256, 512, K1_LDS, stream>>>(x, wqkv, wo, ws);
  for (int r = 0; r < REP; ++r) k_attn<<<256, 768, K2_LDS, stream>>>(ws, wg, wu, wd);
  for (int r = 0; r < REP; ++r) k_n80<false><<<256, 512, N80_LDS, stream>>>(x, ws, out);
  for (int r = 0; r < REP; ++r) k_gateup<<<256, 512, K4_LDS, stream>>>(ws);
  for (int r = 0; r < REP; ++r) k_n80<true><<<256, 512, N80_LDS, stream>>>(x, ws, out);
}
```

```cpp
#include <hip/hip_runtime.h>
#include <stdint.h>
#include <type_traits>

#define LAS __attribute__((address_space(3)))
#define DI __device__ __forceinline__
typedef unsigned short u16;
typedef short bf16x8 __attribute__((ext_vector_type(8)));
typedef short bf16x4 __attribute__((ext_vector_type(4)));
typedef __bf16 bf16x2_t __attribute__((ext_vector_type(2)));
typedef float f32x2 __attribute__((ext_vector_type(2)));
typedef float f32x4 __attribute__((ext_vector_type(4)));
typedef float f32x16 __attribute__((ext_vector_type(16)));
typedef unsigned u32x4 __attribute__((ext_vector_type(4)));
typedef unsigned u32x2 __attribute__((ext_vector_type(2)));
typedef LAS char* ldsp;
typedef __amdgpu_buffer_rsrc_t rsrc_t;

constexpr int S_ = 2048, D_ = 640, NH = 32, DF = 1536;
constexpr float LN_EPS = 1e-5f;
constexpr float QSCALE = 0.22360679774997896f * 1.4426950408889634f;
constexpr unsigned ONES2 = 0x3f803f80u;
#define AUX_ST 16

constexpr int V_TILE = 1344;
constexpr int WS_QK = 0;
constexpr int WS_V = WS_QK + 160 * S_ * 16;
constexpr int WS_O = WS_V + NH * 64 * V_TILE;
constexpr int WS_H = WS_O + S_ * D_ * 2;
constexpr int WS_ACT = WS_H + S_ * D_ * 2;
constexpr int WS_ST = WS_ACT + S_ * DF * 2;
constexpr int WS_CS = WS_ST + S_ * 8 * 8;
constexpr int WS_WO = WS_CS + 8 * 3072 * 4;
constexpr int WS_WGU = WS_WO + D_ * D_ * 2;
constexpr int WS_WD = WS_WGU + D_ * DF * 4;
constexpr int WS_END = WS_WD + D_ * DF * 2;

#define TS_DECL
#define TS()
#define TS_END(kid)

template <int I, int N, class F> DI void sfor(F&& f) {
  if constexpr (I < N) { f(std::integral_constant<int, I>{}); sfor<I + 1, N>(f); }
}
#define SB0() __builtin_amdgcn_sched_barrier(0)
DI unsigned cvtpk(float lo, float hi) { f32x2 v = {lo, hi}; return __builtin_bit_cast(unsigned, __builtin_convertvector(v, bf16x2_t)); }
DI float bflo(unsigned u) { return __uint_as_float(u << 16); }
DI float bfhi(unsigned u) { return __uint_as_float(u & 0xffff0000u); }
DI float dot2(unsigned a, unsigned b, float c) {
  return __builtin_amdgcn_fdot2_f32_bf16(__builtin_bit_cast(bf16x2_t, a), __builtin_bit_cast(bf16x2_t, b), c, false);
}
DI f32x4 mfma16(bf16x8 a, bf16x8 b, f32x4 c) { return __builtin_amdgcn_mfma_f32_16x16x32_bf16(a, b, c, 0, 0, 0); }
DI f32x16 mfma32(bf16x8 a, bf16x8 b, f32x16 c) { return __builtin_amdgcn_mfma_f32_32x32x16_bf16(a, b, c, 0, 0, 0); }
DI void set_m0(unsigned v) { asm volatile("s_mov_b32 m0, %0\n\ts_nop 0" ::"s"(v) : "memory"); }
template <int OFF> DI void dma(const void* sbase, unsigned voff) {
  asm volatile("global_load_lds_dwordx4 %0, %1 offset:%2" ::"v"(voff), "s"(sbase), "n"(OFF) : "memory");
}
template <int OFF> DI void dma_nt(const void* sbase, unsigned voff) {
  asm volatile("global_load_lds_dwordx4 %0, %1 offset:%2 nt" ::"v"(voff), "s"(sbase), "n"(OFF) : "memory");
}
template <int N> DI void wait_vm() { asm volatile("s_waitcnt vmcnt(%0)" ::"n"(N) : "memory"); }
template <int N> DI void wait_vm_bar() { asm volatile("s_waitcnt vmcnt(%0) lgkmcnt(0)\n\ts_barrier" ::"n"(N) : "memory"); }
DI void bar() { asm volatile("s_waitcnt lgkmcnt(0)\n\ts_barrier" ::: "memory"); }
template <class T> DI void pin(T& v) { asm volatile("" : "+v"(v)); }
DI bf16x8 lds_ld16(ldsp p) { return *(const LAS bf16x8*)p; }
DI bf16x4 lds_tr(ldsp p) { return __builtin_amdgcn_ds_read_tr16_b64_v4i16((LAS bf16x4*)p); }
DI bf16x8 lds_tr2(ldsp p0, ldsp p1) {
  bf16x4 a = lds_tr(p0), b = lds_tr(p1);
  return __builtin_shufflevector(a, b, 0, 1, 2, 3, 4, 5, 6, 7);
}
DI float xsum32(float v) {
  auto r = __builtin_amdgcn_permlane32_swap(__float_as_uint(v), __float_as_uint(v), false, false);
  return __uint_as_float(r[0]) + __uint_as_float(r[1]);
}
DI float xsum16_32(float v) {
  auto r = __builtin_amdgcn_permlane16_swap(__float_as_uint(v), __float_as_uint(v), false, false);
  return xsum32(__uint_as_float(r[0]) + __uint_as_float(r[1]));
}
DI rsrc_t mk_rsrc(void* p, int bytes) { return __builtin_amdgcn_make_buffer_rsrc(p, 0, bytes, 0x00020000); }
template <int AUX = 16> DI void st16(rsrc_t r, int off, u32x4 v) { __builtin_amdgcn_raw_buffer_store_b128(v, r, off, 0, AUX); }
template <int AUX = 16> DI void st8(rsrc_t r, int off, u32x2 v) { __builtin_amdgcn_raw_buffer_store_b64(v, r, off, 0, AUX); }
#define AUX3 0
#define AUX4 16

template <int P, int NT, int NS, int X, class F, class G0, class G> DI void loader_loop(F&& issue, G0&& early, G&& extra) {
  static_assert(NS >= 4, "three stages in flight + one being read");
  issue(0); issue(1);
  wait_vm_bar<P>();
  early();
  issue(2); issue(3);
  int slot = 4 % NS;
#pragma unroll 1
  for (int t = 1; t < NT - 3; ++t) {
    wait_vm_bar<2 * P>();
    issue(slot);
    slot = slot == NS - 1 ? 0 : slot + 1;
  }
  extra();
  wait_vm_bar<2 * P + X>(); wait_vm_bar<P + X>(); wait_vm_bar<X>();
}
DI void touch(const char* p, int lane, int nlines, unsigned lds_scratch) {
  set_m0(lds_scratch);
  asm volatile("global_load_lds_dword %0, %1" ::"v"((unsigned)((lane < nlines ? lane : nlines - 1) * 128)), "s"(p) : "memory");
}
#define CODE_ADDR(dst, sym) asm volatile("s_getpc_b64 s[40:41]\n\ts_add_u32 s40, s40, " #sym "@rel32@lo+4\n\ts_addc_u32 s41, s41, " #sym "@rel32@hi+12\n\ts_mov_b64 %0, s[40:41]" : "=s"(dst) :: "s40", "s41", "scc")
template <int P> DI void dma_run(const char* src, unsigned dst, unsigned lane16) {
  static_assert(P <= 8, "offset range");
  set_m0(dst + 4096);
  sfor<0, P>([&](auto ic) { dma<decltype(ic)::value * 1024 - 4096>(src + 4096, lane16); });
}
template <int P, int NT, int NS, int SSTR, int LSTR, int X, class G0, class G> DI void linear_loader(const char* src, unsigned dst, unsigned lane16, G0&& early, G&& extra) {
  loader_loop<P, NT, NS, X>([&](int slot) { dma_run<P>(src, dst + slot * LSTR, lane16); src += SSTR; }, early, extra);
}

template <int KIND> DI void pack_issue(const u16* wsrc, const u16* wsrc2, int ks, int cg, unsigned ldsdst, int lane) {
  constexpr int PITCH = KIND == 1 ? DF * 2 : D_ * 2;
  set_m0(ldsdst);
  sfor<0, 4>([&](auto ic) {
    constexpr int g = decltype(ic)::value;
    const int t = (lane >> 5) & 1, q = (lane >> 3) & 3;
    int row;
    if (KIND == 0) row = ks < 16 ? 20 * (2 * ks + (g >> 1)) + 8 * t + 4 * (g & 1) + q : 20 * (8 * (ks - 16) + 2 * g + t) + 16 + q;
    else if (KIND == 1) row = 32 * (ks >> 1) + 16 * t + 4 * (2 * (ks & 1) + (g & 1)) + q;
    else row = 32 * ks + 16 * (g >> 1) + 8 * t + 4 * (g & 1) + q;
    dma_nt<g * 1024>((const char*)(KIND == 1 && g >= 2 ? wsrc2 : wsrc) + cg * 128 - 4096, (unsigned)(row * PITCH + (lane & 7) * 16 + 4096 - g * 1024));
  });
}
template <int KIND> DI void pack_finish(ldsp u, rsrc_t rw, int ks, int cg, int lane, LAS float* cs) {
  const int q = (lane & 15) >> 2, p = lane & 3;
  ldsp a = KIND == 1 ? u + ((lane >> 4) & 1) * 2048 + (8 * (lane >> 5) + q) * 128 + p * 8 : u + (8 * (lane >> 4) + q) * 128 + p * 8;
#pragma unroll
  for (int i = 0; i < 4; ++i) {
    const int nt = 4 * cg + i;
    const u32x4 f = __builtin_bit_cast(u32x4, lds_tr2(a + i * 32, a + i * 32 + 512));
    if (KIND == 1) {
      const int ft = nt / 6, j = nt - ft * 6;
      st16(rw, WS_WGU + ((ft * 40 + ks) * 6 + j) * 1024 + lane * 16, f);
      const unsigned sv = (lane & 15) == ((lane >> 4) & 1) ? ONES2 : 0u;
      const f32x4 c = mfma16(__builtin_bit_cast(bf16x8, (u32x4){sv, sv, sv, sv}), __builtin_bit_cast(bf16x8, f), (f32x4){0.f, 0.f, 0.f, 0.f});
      if (lane < 16) { cs[i * 32 + lane] = c[0]; cs[i * 32 + 16 + lane] = c[1]; }
    } else {
      const int ct = nt / 5, j = nt - ct * 5;
      st16(rw, (KIND == 0 ? WS_WO : WS_WD) + ((ct * (KIND == 0 ? 20 : 48) + ks) * 5 + j) * 1024 + lane * 16, f);
    }
  }
}

#define WD0 8
#define WD1 24
constexpr int K1_NS = 4, K1_STG = 32768, K1_NT = 10, K1_CTL = K1_NS * K1_STG, K1_LDS = K1_CTL + 4096;

template <bool ISV> DI void qkv_compute(ldsp lds, char* __restrict__ ws, int mt, int nt, int w, int lane) {
  const int wm = w >> 1, wn = w & 1, r32 = lane & 31, hi = lane >> 5, q = (lane & 15) >> 2, p = lane & 3, blk = (lane >> 4) & 1;
  TS_DECL;
  ldsp ctl = lds + K1_CTL;
  int aoff[4], woff[2];
#pragma unroll
  for (int s = 0; s < 4; ++s) aoff[s] = (wm * 64 + wn * 32 + r32) * 128 + (((2 * s + hi) ^ ((r32 >> 1) & 7)) * 16);
  const int astep = wn ? -4096 : 4096;
#pragma unroll
  for (int j = 0; j < 2; ++j) woff[j] = 16384 + (8 * hi + q) * 256 + (((wn * 2 + (j ^ wm)) ^ q) * 64) + blk * 32 + p * 8;
  f32x16 acc[2][2];
#pragma unroll
  for (int j = 0; j < 2; ++j)
#pragma unroll
    for (int i = 0; i < 2; ++i) { acc[j][i] = (f32x16){}; pin(acc[j][i]); }
  f32x4 rs = {}, rq = {}, cs = {};
  const unsigned sv = (lane & 15) == blk ? ONES2 : 0u;
  const bf16x8 sel = __builtin_bit_cast(bf16x8, (u32x4){sv, sv, sv, sv});
  bf16x8 xf[2][2], wf[2][2];
  auto rdx = [&](ldsp sb, int s, int i) { xf[s & 1][i] = lds_ld16(sb + aoff[s] + (i ? astep : 0)); };
  auto rdw = [&](ldsp sb, int s, int j) { wf[s & 1][j] = lds_tr2(sb + woff[j] + s * 4096, sb + woff[j] + s * 4096 + 1024); };
  auto mm = [&](int s, int j, int i) { acc[j][i] = ISV ? mfma32(xf[s & 1][i], wf[s & 1][j], acc[j][i]) : mfma32(wf[s & 1][j], xf[s & 1][i], acc[j][i]); };
  auto step = [&](ldsp nb, int s, int sn, bool rd) {
    const bf16x8 xa = xf[s & 1][0];
    const bf16x4 xl = __builtin_shufflevector(xa, xa, 0, 1, 2, 3), xh = __builtin_shufflevector(xa, xa, 4, 5, 6, 7);
    mm(s, 0, 0); if (rd) { rdx(nb, sn, 0); rdw(nb, sn, 0); } SB0();
    rq = __builtin_amdgcn_mfma_f32_4x4x4bf16_1k(xl, xl, rq, 0, 0, 0);
    mm(s, 0, 1); if (rd) { rdx(nb, sn, 1); rdw(nb, sn, 1); } SB0();
    rs = mfma16(sel, xa, rs);
    mm(s, 1, 0); SB0();
    rq = __builtin_amdgcn_mfma_f32_4x4x4bf16_1k(xh, xh, rq, 0, 0, 0);
    mm(s, 1, 1); SB0();
    cs = mfma16(sel, wf[s & 1][0], cs); SB0();
  };
  bar();
  TS();
  rdx(lds, 0, 0); rdw(lds, 0, 0); rdx(lds, 0, 1); rdw(lds, 0, 1); SB0();
  sfor<0, K1_NT>([&](auto tc) {
    constexpr int t = decltype(tc)::value;
    ldsp sb = lds + (t % K1_NS) * K1_STG;
    step(sb, 0, 1, true); step(sb, 1, 2, true); step(sb, 2, 3, true);
    if (t + 1 < K1_NT) { bar(); SB0(); }
    step(lds + ((t + 1) % K1_NS) * K1_STG, 3, 0, t + 1 < K1_NT);
  });
  TS();
  LAS f32x2* rst = (LAS f32x2*)(ctl + 1024);
  LAS float* cst = (LAS float*)(ctl + 2048);
  {
    const float sq = xsum32(p == 0 ? rq[0] : p == 1 ? rq[1] : p == 2 ? rq[2] : rq[3]);
    const float mu = __uint_as_float(__builtin_amdgcn_permlane16_swap(__float_as_uint(rs[0]), __float_as_uint(rs[1]), false, false)[0]) * (1.f / D_), var = fmaxf(sq * (1.f / D_) - mu * mu, 0.f);
    if (hi == 0) rst[wm * 64 + wn * 32 + r32] = (f32x2){mu, __builtin_amdgcn_rsqf(var + LN_EPS)};
    if (lane < 16) { cst[wn * 64 + wm * 32 + lane] = cs[0]; cst[wn * 64 + wm * 32 + 16 + lane] = cs[1]; }
  }
  bar();
  TS();
  const rsrc_t rw = mk_rsrc(ws, WS_END);
  if constexpr (!ISV) {
    const float sc = nt < 5 ? QSCALE : 1.f;
#pragma unroll
    for (int i = 0; i < 2; ++i) {
      const int ml = wm * 64 + (i ^ wn) * 32 + r32;
      const f32x2 st = rst[ml];
      const float a = st.y * sc, nm = -st.x * a;
      const int ob = WS_QK + ((nt * 16 + wn * 8 + hi) * S_ + mt * 128 + ml) * 16;
#pragma unroll
      for (int j = 0; j < 2; ++j)
#pragma unroll
        for (int bp = 0; bp < 2; ++bp) {
          unsigned pk[2][2];
#pragma unroll
          for (int e = 0; e < 2; ++e) {
            const int b = 2 * bp + e;
            const f32x4 c4 = *(const LAS f32x4*)(cst + wn * 64 + (j ^ wm) * 32 + 8 * b + 4 * hi);
            const f32x2 v0 = (f32x2){acc[j][i][4 * b], acc[j][i][4 * b + 1]} * a + nm * (f32x2){c4[0], c4[1]};
            const f32x2 v1 = (f32x2){acc[j][i][4 * b + 2], acc[j][i][4 * b + 3]} * a + nm * (f32x2){c4[2], c4[3]};
            pk[e][0] = cvtpk(v0[0], v0[1]); pk[e][1] = cvtpk(v1[0], v1[1]);
          }
          auto r0 = __builtin_amdgcn_permlane32_swap(pk[0][0], pk[1][0], false, false);
          auto r1 = __builtin_amdgcn_permlane32_swap(pk[0][1], pk[1][1], false, false);
          st16(rw, ob + ((j ^ wm) * 4 + bp * 2) * S_ * 16, (u32x4){r0[0], r1[0], r0[1], r1[1]});
        }
    }
  } else {
#pragma unroll
    for (int j = 0; j < 2; ++j) {
      const int nl = wn * 64 + (j ^ wm) * 32 + r32, n = (nt - 10) * 128 + nl, h = n / 20, d = n - h * 20;
      const float c = cst[nl];
      const int ob = WS_V + (h * 64 + mt * 4 + wm * 2) * V_TILE + hi * 336 + d * 16;
#pragma unroll
      for (int i = 0; i < 2; ++i)
#pragma unroll
        for (int s = 0; s < 2; ++s) {
          unsigned pk[4];
#pragma unroll
          for (int e = 0; e < 2; ++e) {
            const int b = 2 * s + e;
            const LAS f32x4* sp = (const LAS f32x4*)(rst + wm * 64 + (i ^ wn) * 32 + 8 * b + 4 * hi);
            const f32x4 s01 = sp[0], s23 = sp[1];
            pk[2 * e] = cvtpk((acc[j][i][4 * b] - s01[0] * c) * s01[1], (acc[j][i][4 * b + 1] - s01[2] * c) * s01[3]);
            pk[2 * e + 1] = cvtpk((acc[j][i][4 * b + 2] - s23[0] * c) * s23[1], (acc[j][i][4 * b + 3] - s23[2] * c) * s23[3]);
          }
          const int o = ob + (i ^ wn) * V_TILE + s * 672;
          st16(rw, o, (u32x4){pk[0], pk[1], pk[2], pk[3]});
          if (d == 19) st16(rw, o + 16, (u32x4){ONES2, ONES2, ONES2, ONES2});
        }
    }
  }
  TS();
  if (w == 0) { TS_END(0); }
}

DI void qkv_loader(ldsp lds, const u16* __restrict__ x, const u16* __restrict__ wqkv, int mt, int nt, int lw, int lane) {
  const bool isx = lw < 2;
  const int half = lw & 1;
  unsigned voff[8];
#pragma unroll
  for (int i = 0; i < 8; ++i) {
    const int r = (half * 8 + i) * 8 + (lane >> 3), c = (lane & 7) ^ ((r >> 1) & 7);
    const int k = (half * 8 + i) * 4 + (lane >> 4), cp = lane & 15;
    voff[i] = (isx ? r * 1280 + c * 16 : k * 3840 + (cp ^ ((k & 3) << 2)) * 16) + 8192 - i * 1024;
  }
  const char* src = (isx ? (const char*)x + (size_t)mt * 128 * 1280 : (const char*)wqkv + nt * 256) - 4096;
  const int sstr = isx ? 128 : 64 * 3840;
  const unsigned dst = (unsigned)(uintptr_t)lds + lw * 8192 + 4096;
  loader_loop<8, K1_NT, K1_NS, 0>([&](int slot) {
    set_m0(dst + slot * K1_STG);
    sfor<0, 8>([&](auto ic) { constexpr int i = decltype(ic)::value; dma<i * 1024 - 4096>(src, voff[i]); });
    src += sstr;
  }, [] {}, [] {});
}

extern "C" __global__ __launch_bounds__(512, 2) void k_qkv(const u16* __restrict__ x, const u16* __restrict__ wqkv, const u16* __restrict__ wo, char* __restrict__ ws) {
  extern __shared__ __attribute__((aligned(16))) char smem[];
  ldsp lds = (ldsp)smem;
  const int lane = threadIdx.x & 63, w = __builtin_amdgcn_readfirstlane(threadIdx.x >> 6);
  const int xcd = blockIdx.x & 7, idx = blockIdx.x >> 3;
  const int nh = xcd & 1, nt = nh * 8 + (idx >> 2), mt = (xcd >> 1) * 4 + (idx & 3);
  if (w == 2) {
    const char *c0, *c1;
    CODE_ADDR(c0, k_qkv); CODE_ADDR(c1, k_text_end);
    const unsigned lim = (unsigned)(c1 - c0), off = (idx * 16 + (lane & 15)) * 128;
    set_m0((unsigned)(uintptr_t)lds + K1_CTL + 3072 + 512);
    asm volatile("global_load_lds_dword %0, %1" ::"v"(off < lim ? off : lim), "s"(c0) : "memory");
  }
  if (w < 2) {
    const int id = blockIdx.x * 64 + lane;
    const bool isx = blockIdx.x < 128;
    const int i2 = id - 8192 < 7680 ? id - 8192 : 7679;
    const unsigned off = isx ? (id >> 2) * 1280 + 256 + w * 512 + (id & 3) * 128 : (128 + w * 256) * 3840 + i2 * 128;
    set_m0((unsigned)(uintptr_t)lds + K1_CTL + 3072 + w * 256);
    const char* src = isx ? (const char*)x : (const char*)wqkv;
    asm volatile("" : "+s"(src));
    if (w == 0) __builtin_amdgcn_s_sleep(WD0); else __builtin_amdgcn_s_sleep(WD1);
    asm volatile("global_load_lds_dword %0, %1" ::"v"(off), "s"(src) : "memory");
  }
  if (nt == 15) {
    const int u0 = mt * 8 + w;
    const rsrc_t rw = mk_rsrc(ws, WS_END);
#pragma unroll
    for (int r = 0; r < 2; ++r) { const int u = u0 + r * 128; if (u < 200) pack_issue<0>(wo, wo, u / 10, u % 10, (unsigned)(uintptr_t)lds + (w * 2 + r) * 4096, lane); }
    wait_vm<0>();
#pragma unroll
    for (int r = 0; r < 2; ++r) { const int u = u0 + r * 128; if (u < 200) pack_finish<0>(lds + (w * 2 + r) * 4096, rw, u / 10, u % 10, lane, nullptr); }
    return;
  }
  if (w >= 4) { qkv_loader(lds, x, wqkv, mt, nt, w - 4, lane); return; }
  if (nt >= 10) qkv_compute<true>(lds, ws, mt, nt, w, lane);
  else qkv_compute<false>(lds, ws, mt, nt, w, lane);
}

#define VLEAD 3
constexpr int K2_V = 24576, K2_PK = K2_V + 16 * V_TILE + 256, K2_CS = K2_PK + 8 * 4096, K2_MSK = K2_CS + 3072, K2_TM = K2_MSK + 2048, K2_LDS = K2_TM + 8192;

DI void attn_helper(ldsp lds, char* __restrict__ ws, int c, int h, int hw, int lane, const u16* __restrict__ wg, const u16* __restrict__ wu, const u16* __restrict__ wd) {
  const unsigned lds0 = (unsigned)(uintptr_t)lds;
  const int pm = 5 * (h >> 1) + 3 * (h & 1), pt = 5 * (h >> 1) + 2;
  const bool EDGE = c == 0;
  {
    const int tok0 = 256 * c - 256 + 128 * hw + ((EDGE && hw < 2) ? 256 : 0);
    const char* kb = ws + WS_QK + (80 * S_ + tok0) * 16;
    set_m0(lds0 + hw * 2048); dma<0>(kb + pm * S_ * 16, lane * 16); dma<1024>(kb + pm * S_ * 16, lane * 16);
    set_m0(lds0 + 8192 + hw * 2048); dma<0>(kb + (pm + 1) * S_ * 16, lane * 16); dma<1024>(kb + (pm + 1) * S_ * 16, lane * 16);
    set_m0(lds0 + 16384 + hw * 2048); dma<0>(kb + pt * S_ * 16, lane * 16); dma<1024>(kb + pt * S_ * 16, lane * 16);
  }
  const int b = blockIdx.x;
  if (lane == 0) *(LAS unsigned*)(lds + K2_CS + 2560 + 16 + hw * 4) = 0u;
  if (hw == 0 && lane == 0) *(LAS unsigned*)(lds + K2_CS + 2560) = 0u;
  if (hw == 1 && lane < 16) *(LAS u32x2*)(lds + K2_TM + lane * 512) = (u32x2){EDGE && lane < 8 ? 0xff80u : 0u, 0u};
  wait_vm<VLEAD>();
  {
    const char* vb = ws + WS_V + (h * 64 + 8 * c - 8) * V_TILE + 4096;
    set_m0(lds0 + K2_V + hw * 5120 + 4096);
    sfor<0, 6>([&](auto ic) {
      constexpr int i = decltype(ic)::value;
      unsigned off = hw * 5120 + lane * 16;
      if (EDGE && off + i * 1024 < 8u * V_TILE) off += 8 * V_TILE;
      dma<i * 1024 - 4096>(vb, off);
    });
    wait_vm_bar<6>();
    wait_vm<0>();
    if (lane == 0) *(volatile LAS unsigned*)(lds + K2_CS + 2560 + 16 + hw * 4) = 1u;
  }
  const rsrc_t rw = mk_rsrc(ws, WS_END);
  const unsigned u0 = lds0 + K2_PK + hw * 4096;
  if (b < 192) {
    const int pcg = b % 24, pks = (b / 24) * 5;
    pack_issue<1>(wg, wu, pks + hw, pcg, u0, lane);
    if (hw == 0) pack_issue<1>(wg, wu, pks + 4, pcg, u0 + 16384, lane);
    if (hw >= 2) touch(ws + WS_WO + (b & 1) * 409600 + ((b >> 3) * 2 + hw - 2) * 6400, lane, 50, lds0 + K2_CS + 2816);
    wait_vm<0>();
    LAS float* ct = (LAS float*)(lds + K2_CS);
    pack_finish<1>(lds + K2_PK + hw * 4096, rw, pks + hw, pcg, lane, ct + hw * 128);
    if (hw == 0) pack_finish<1>(lds + K2_PK + 16384, rw, pks + 4, pcg, lane, ct + 512);
    asm volatile("s_waitcnt lgkmcnt(0)" ::: "memory");
    unsigned old = 0;
    if (lane == 0) old = __hip_atomic_fetch_add((LAS unsigned*)(lds + K2_CS + 2560), 1u, __ATOMIC_RELAXED, __HIP_MEMORY_SCOPE_WORKGROUP);
    if (__builtin_amdgcn_readfirstlane(old) == 3) {
      asm volatile("" ::: "memory");
      const LAS f32x2* c2 = (const LAS f32x2*)ct + lane;
      const f32x2 a = ((c2[0] + c2[64]) + (c2[128] + c2[192])) + c2[256];
      const int e = pcg * 128 + lane * 2, ft = e / 192;
      st8(rw, WS_CS + ((ft * 8 + b / 24) * 192 + e - ft * 192) * 4, __builtin_bit_cast(u32x2, a));
    }
  } else {
    const int bb = b < 252 ? b - 192 : 0, pcg = bb % 10, pks = (bb / 10) * 8 + hw;
    pack_issue<2>(wd, wd, pks, pcg, u0, lane);
    pack_issue<2>(wd, wd, pks + 4, pcg, u0 + 16384, lane);
    if (hw >= 2) touch(ws + WS_WO + (b & 1) * 409600 + ((b >> 3) * 2 + hw - 2) * 6400, lane, 50, lds0 + K2_CS + 2816);
    wait_vm<0>();
    pack_finish<2>(lds + K2_PK + hw * 4096, rw, pks, pcg, lane, nullptr);
    pack_finish<2>(lds + K2_PK + hw * 4096 + 16384, rw, pks + 4, pcg, lane, nullptr);
  }
}

DI void attn_body(ldsp lds, char* __restrict__ ws, int c, int h, int w, int lane) {
  const int r32 = lane & 31, hi = lane >> 5;
  TS_DECL;
  const int pm = 5 * (h >> 1) + 3 * (h & 1), pt = 5 * (h >> 1) + 2;
  const int dq = r32 - 4 * hi;
  const float NEG = -__builtin_inff();
  f32x16 s[8], s8;
#pragma unroll
  for (int r = 0; r < 16; ++r) {
    const bool le = (r & 3) + 8 * (r >> 2) <= dq;
    s8[r] = le ? 0.f : NEG;
    s[0][r] = le ? NEG : 0.f;
  }
  if (w == 0) {
    u32x4 msk[2];
#pragma unroll
    for (int e = 0; e < 8; ++e) msk[e >> 2][e & 3] = (unsigned)((int)__float_as_uint(s8[2 * e]) >> 31) >> 16 | ((unsigned)((int)__float_as_uint(s8[2 * e + 1]) >> 31) << 16);
    *(LAS u32x4*)(lds + K2_MSK + lane * 16) = msk[0]; *(LAS u32x4*)(lds + K2_MSK + 1024 + lane * 16) = msk[1];
  }
  pin(s[0]); pin(s8);
  SB0();
  const char* qb = ws + WS_QK + (256 * c + 32 * w) * 16;
  const bf16x8 qm = *(const bf16x8*)(qb + ((pm + hi) * S_ + r32) * 16);
  u32x2 qtr = *(const u32x2*)(qb + (pt * S_ + r32) * 16 + (h & 1) * 8);
  if (hi) qtr = (u32x2){0x3f80u, 0u};
  bf16x4 qt = __builtin_bit_cast(bf16x4, qtr);
  pin(qt);
  TS();
  bar();
  TS();
  ldsp kp = lds + hi * 8192 + (w * 32 + r32) * 16;
  ldsp ktp = lds + (hi ? K2_TM + w * 512 : 16384 + (w * 32 + r32) * 16 + (h & 1) * 8);
  ldsp vp = lds + K2_V + w * V_TILE + hi * 336 + r32 * 16;
  bf16x8 km[9]; bf16x4 kt[9];
#pragma unroll
  for (int jj = 0; jj < 9; ++jj) { const int j = jj == 0 ? 8 : jj - 1; km[j] = lds_ld16(kp + j * 512); kt[j] = *(const LAS bf16x4*)(ktp + j * 512); }
  SB0();
  auto tail = [&](int j, f32x16 cin) { return __builtin_amdgcn_mfma_f32_32x32x8bf16_1k(kt[j], qt, cin, 0, 0, 0); };
  float mx[4];
  auto maxh = [&](int j, int r0) {
#pragma unroll
    for (int e = 0; e < 4; ++e) mx[e] = __builtin_elementwise_maximum(__builtin_elementwise_maximum(mx[e], s[j][r0 + 2 * e]), s[j][r0 + 2 * e + 1]);
  };
  s8 = mfma32(km[8], qm, s8); s[0] = mfma32(km[0], qm, s[0]); s8 = tail(8, s8);
  s[1] = mfma32(km[1], qm, (f32x16){}); s[0] = tail(0, s[0]);
  s[2] = mfma32(km[2], qm, (f32x16){}); s[1] = tail(1, s[1]); SB0();
  s[3] = mfma32(km[3], qm, (f32x16){});
#pragma unroll
  for (int r = 0; r < 8; ++r) s[0][r] = __builtin_elementwise_maximum(s[0][r], s8[r]);
  SB0();
  s[2] = tail(2, s[2]);
#pragma unroll
  for (int r = 8; r < 16; ++r) s[0][r] = __builtin_elementwise_maximum(s[0][r], s8[r]);
  pin(s[0]);
  SB0();
#pragma unroll
  for (int e = 0; e < 4; ++e) mx[e] = s[0][e];
  s[4] = mfma32(km[4], qm, (f32x16){}); maxh(0, 4); mx[0] = __builtin_elementwise_maximum(__builtin_elementwise_maximum(mx[0], s[0][12]), s[0][13]);
  mx[1] = __builtin_elementwise_maximum(__builtin_elementwise_maximum(mx[1], s[0][14]), s[0][15]); SB0();
  s[3] = tail(3, s[3]); maxh(1, 0); SB0();
  s[5] = mfma32(km[5], qm, (f32x16){}); maxh(1, 8); SB0();
  s[4] = tail(4, s[4]); maxh(2, 0); SB0();
  s[6] = mfma32(km[6], qm, (f32x16){}); maxh(2, 8); SB0();
  s[5] = tail(5, s[5]); maxh(3, 0); SB0();
  s[7] = mfma32(km[7], qm, (f32x16){}); maxh(3, 8); SB0();
  s[6] = tail(6, s[6]); maxh(4, 0); SB0();
  s[7] = tail(7, s[7]); maxh(4, 8);
#pragma unroll
  for (int j = 5; j < 8; ++j) { maxh(j, 0); maxh(j, 8); }
  const float m0 = __builtin_elementwise_maximum(mx[0], mx[1]), m1 = __builtin_elementwise_maximum(mx[2], mx[3]);
  float m = __builtin_elementwise_maximum(m0, m1);
  {
    auto rr = __builtin_amdgcn_permlane32_swap(__float_as_uint(m), __float_as_uint(m), false, false);
    m = __builtin_elementwise_maximum(__uint_as_float(rr[0]), __uint_as_float(rr[1]));
  }
  bf16x4 nm, one4;
  {
    const unsigned u0 = __float_as_uint(m) & 0xffff0000u;
    const float r1 = m - __uint_as_float(u0);
    const unsigned u1 = __float_as_uint(r1) & 0xffff0000u;
    const float r2 = r1 - __uint_as_float(u1);
    const u32x2 v = {hi ? 0u : ((u0 >> 16) | u1), hi ? 0u : (__float_as_uint(r2) >> 16)};
    nm = __builtin_bit_cast(bf16x4, v);
    one4 = __builtin_bit_cast(bf16x4, (u32x2){0xbf80bf80u, 0xbf80bf80u});
  }
  SB0();
  TS();
  f32x16 o = {};
  u32x4 pk[8][2];
  auto sub = [&](int j) { s[j] = __builtin_amdgcn_mfma_f32_32x32x8bf16_1k(one4, nm, s[j], 0, 0, 0); };
  auto ex8 = [&](int j, int ks) {
#pragma unroll
    for (int e = 0; e < 4; ++e) pk[j][ks][e] = cvtpk(__builtin_amdgcn_exp2f(s[j][8 * ks + 2 * e]), __builtin_amdgcn_exp2f(s[j][8 * ks + 2 * e + 1]));
  };
  auto vld = [&](int slot, int ks) { return lds_ld16(vp + slot * V_TILE + ks * 672); };
  auto pv = [&](bf16x8 v, u32x4 p) { o = mfma32(v, __builtin_bit_cast(bf16x8, p), o); };
  sub(7); sub(6); sub(5);
  __builtin_amdgcn_s_setprio(0);
  ex8(7, 0); ex8(7, 1);
  const u32x4 vrdy = *(volatile LAS u32x4*)(lds + K2_CS + 2560 + 16);
  ex8(6, 0); ex8(6, 1);
  pin(pk[7][0]); pin(pk[7][1]); pin(pk[6][0]); pin(pk[6][1]);
  if (__builtin_expect(__builtin_amdgcn_readfirstlane((vrdy[0] & vrdy[1]) & (vrdy[2] & vrdy[3])) == 0, 0)) {
    for (int sp = 0; sp < (1 << 20); ++sp) {
      const u32x4 v = *(volatile LAS u32x4*)(lds + K2_CS + 2560 + 16);
      if (__builtin_amdgcn_readfirstlane((v[0] & v[1]) & (v[2] & v[3]))) break;
      __builtin_amdgcn_s_sleep(1);
    }
  }
  asm volatile("" ::: "memory");
  bf16x8 vf[9][2];
  vf[7][0] = vld(7, 0); vf[7][1] = vld(7, 1);
  SB0();
  u32x4 mk[2];
#pragma unroll
  for (int j = 5; j >= 0; --j) {
    vf[j + 1][0] = vld(j + 1, 0); vf[j + 1][1] = vld(j + 1, 1);
    if (j == 0) { vf[0][0] = vld(0, 0); vf[8][0] = vld(8, 0); vf[0][1] = vld(0, 1); vf[8][1] = vld(8, 1); }
    if (j == 1) { mk[0] = *(const LAS u32x4*)(lds + K2_MSK + lane * 16); mk[1] = *(const LAS u32x4*)(lds + K2_MSK + 1024 + lane * 16); }
    if (j > 0) sub(j - 1);
    SB0();
    ex8(j, 0); pv(vf[j + 2][0], pk[j + 2][0]); SB0();
    ex8(j, 1); pv(vf[j + 2][1], pk[j + 2][1]); SB0();
  }
  pv(vf[1][0], pk[1][0]); pv(vf[1][1], pk[1][1]);
#pragma unroll
  for (int ks = 0; ks < 2; ++ks) {
    const u32x4 p0 = pk[0][ks] & mk[ks], p8 = pk[0][ks] ^ p0;
    pv(vf[0][ks], p0); pv(vf[8][ks], p8);
  }
  TS();
  const float l = __uint_as_float(__builtin_amdgcn_permlane32_swap(__float_as_uint(o[8]), __float_as_uint(o[8]), false, false)[1]);
  const float il = __builtin_amdgcn_rcpf(l);
  const int tok = 256 * c + 32 * w + r32;
  const rsrc_t rw = mk_rsrc(ws, WS_END);
  const int ob = WS_O + (tok >> 6) * 20 * 4096 + ((tok >> 4) & 3) * 1024 + (tok & 15) * 16;
  const u32x4 w0 = {cvtpk(o[0] * il, o[1] * il), cvtpk(o[2] * il, o[3] * il), cvtpk(o[4] * il, o[5] * il), cvtpk(o[6] * il, o[7] * il)};
  st16(rw, ob + (h >> 1) * 4096 + (2 * (h & 1) + hi) * 256, w0);
  if (hi == 0) st8(rw, ob + (16 + (h >> 3)) * 4096 + ((h >> 1) & 3) * 256 + (h & 1) * 8, (u32x2){cvtpk(o[8] * il, o[9] * il), cvtpk(o[10] * il, o[11] * il)});
  TS();
  if (w == 7 || w == 0) { TS_END(w == 7 ? 1 : 5); }
}

extern "C" __global__ __launch_bounds__(768, 3) void k_attn(char* __restrict__ ws, const u16* __restrict__ wg, const u16* __restrict__ wu, const u16* __restrict__ wd) {
  extern __shared__ __attribute__((aligned(16))) char smem[];
  const int lane = threadIdx.x & 63, w = __builtin_amdgcn_readfirstlane(threadIdx.x >> 6);
  const int xcd = blockIdx.x & 7, idx = blockIdx.x >> 3, h = xcd * 4 + (idx >> 3), c = idx & 7;
  if (w < 4) attn_helper((ldsp)smem, ws, c, h, w, lane, wg, wu, wd);
  else attn_body((ldsp)smem, ws, c, h, w - 4, lane);
}

constexpr int N80_NS = 4, N80_STG = 18432, N80_CTL = N80_NS * N80_STG, N80_RES = N80_CTL + 20480, N80_LDS = N80_RES + 12288;

template <bool DOWN> DI void n80_body(const u16* __restrict__ x, char* __restrict__ ws, u16* __restrict__ out) {
  constexpr int NT = DOWN ? 24 : 10;
  extern __shared__ __attribute__((aligned(16))) char smem[];
  ldsp lds = (ldsp)smem;
  ldsp ctl = lds + N80_CTL;
  const int lane = threadIdx.x & 63, w = __builtin_amdgcn_readfirstlane(threadIdx.x >> 6);
  const unsigned lds0 = (unsigned)(uintptr_t)lds;
  const int xcd = blockIdx.x & 7, idx = blockIdx.x >> 3, mt = (xcd >> 1) * 8 + (idx >> 2), ct = (xcd & 1) * 4 + (idx & 3);
  const int odd = ct & 1, pb = (ct >> 1) * 5 + (odd ? 3 : 0), ps = (ct >> 1) * 5 + 2;
  if (w >= 4) {
    const int lw = w - 4;
    auto extra = [&] {
      set_m0(lds0 + N80_RES + lw * 3072);
      if (DOWN) {
        const char* hs = ws + WS_H + ((mt >> 1) * 160 + (mt & 1) * 2 + (lw >> 1)) * 1024;
        const unsigned vo = (lane >> 5) * 4096 + (((lane >> 4) & 1) * 32 + (lw & 1) * 16 + (lane & 15)) * 16 + 2048;
        dma<0>(hs + pb * 8192 - 2048, vo); dma<1024>(hs + (pb + 1) * 8192 - 3072, vo); dma<2048>(hs + ps * 8192 - 4096, vo);
      } else {
        const char* xs = (const char*)x + (size_t)(mt * 64 + lw * 16) * 1280 + ct * 160;
        sfor<0, 3>([&](auto ic) {
          constexpr int i = decltype(ic)::value;
          const int ch = i * 64 + lane < 160 ? i * 64 + lane : 159, r = ch / 10;
          dma<i * 1024>(xs - 2048, (unsigned)(r * 1280 + (ch - r * 10) * 16 + 2048 - i * 1024));
        });
      }
    };
    if (w < 6) linear_loader<4, NT, N80_NS, 8192, N80_STG, 0>(ws + (DOWN ? WS_ACT : WS_O) + mt * NT * 8192 + lw * 4096, lds0 + lw * 4096, lane * 16, extra, [] {});
    else linear_loader<5, NT, N80_NS, 10240, N80_STG, 0>(ws + (DOWN ? WS_WD : WS_WO) + ct * NT * 10240 + (w - 6) * 5120, lds0 + 8192 + (w - 6) * 5120, lane * 16, extra, [] {});
    return;
  }
  const int fr = lane & 15, g = lane >> 4, wk = w & 1;
  TS_DECL;
  const int tok = mt * 64 + w * 16 + fr;
  const int hb = WS_H + (((mt >> 1) * 40 + (g >> 1)) * 4 + (mt & 1) * 2 + (w >> 1)) * 1024 + ((g & 1) * 32 + (w & 1) * 16 + fr) * 16;
  f32x4 acc[2][5];
#pragma unroll
  for (int i = 0; i < 2; ++i)
#pragma unroll
    for (int j = 0; j < 5; ++j) { acc[i][j] = (f32x4){0.f, 0.f, 0.f, 0.f}; pin(acc[i][j]); }
  bf16x8 af[2][2], wf[2][5];
  const int aoff = wk * 4096 + w * 1024 + lane * 16, aoff1 = aoff ^ 1024, woff = 8192 + wk * 5120 + lane * 16;
  auto rd1 = [&](ldsp sb, int bs, int n) {
    if (n == 0) af[bs][0] = lds_ld16(sb + aoff);
    else if (n == 2) af[bs][1] = lds_ld16(sb + aoff1);
    else if (n < 7) { const int j = n == 1 ? 0 : n - 2; wf[bs][j] = lds_ld16(sb + woff + j * 1024); }
  };
  bar();
  TS();
#pragma unroll
  for (int n = 0; n < 7; ++n) rd1(lds, 0, n);
  SB0();
  sfor<0, NT>([&](auto tc) {
    constexpr int t = decltype(tc)::value, bs = t & 1;
    if (t + 1 < NT) { bar(); SB0(); }
#pragma unroll
    for (int n = 0; n < 10; ++n) {
      acc[n & 1][n >> 1] = mfma16(wf[bs][n >> 1], af[bs][n & 1], acc[n & 1][n >> 1]);
      if (t + 1 < NT) rd1(lds + ((t + 1) % N80_NS) * N80_STG, bs ^ 1, n);
      SB0();
    }
  });
  TS();
#pragma unroll
  for (int j = 0; j < 5; ++j) *(LAS f32x4*)(ctl + (w * 5 + j) * 1024 + lane * 16) = acc[1][j];
  bar();
#pragma unroll
  for (int j = 0; j < 5; ++j) acc[0][j] += *(const LAS f32x4*)(ctl + ((w ^ 1) * 5 + j) * 1024 + lane * 16);
  TS();
  u32x2 res[5];
  if (DOWN) {
    ldsp rp = lds + N80_RES + w * 3072 + lane * 16;
#pragma unroll
    for (int j = 0; j < 5; ++j) {
      const int e = odd ? (j == 0 ? 5 : j - 1) : j;
      res[j] = *(const LAS u32x2*)(rp + (e >> 1) * 1024 + (e & 1) * 8);
    }
  } else {
#pragma unroll
    for (int j = 0; j < 5; ++j) res[j] = *(const LAS u32x2*)(lds + N80_RES + w * 3072 + fr * 160 + j * 32 + g * 8);
  }
  u32x2 hv[5];
  float s = 0.f;
#pragma unroll
  for (int j = 0; j < 5; ++j) {
    hv[j] = (u32x2){cvtpk(acc[0][j][0] + bflo(res[j][0]), acc[0][j][1] + bfhi(res[j][0])), cvtpk(acc[0][j][2] + bflo(res[j][1]), acc[0][j][3] + bfhi(res[j][1]))};
    s += (bflo(hv[j][0]) + bfhi(hv[j][0])) + (bflo(hv[j][1]) + bfhi(hv[j][1]));
  }
  if (DOWN) {
    char* op = (char*)out + (size_t)tok * 1280 + ct * 160;
#pragma unroll
    for (int jp = 0; jp < 2; ++jp) {
      auto r0 = __builtin_amdgcn_permlane16_swap(hv[2 * jp][0], hv[2 * jp + 1][0], false, false);
      auto r1 = __builtin_amdgcn_permlane16_swap(hv[2 * jp][1], hv[2 * jp + 1][1], false, false);
      *(u32x4*)(op + jp * 64 + (g & 1) * 32 + (g >> 1) * 16) = (u32x4){r0[0], r1[0], r0[1], r1[1]};
    }
    *(u32x2*)(op + 128 + g * 8) = hv[4];
  } else {
    const rsrc_t rw = mk_rsrc(ws, WS_END);
    const u32x2 a0 = odd ? hv[1] : hv[0], a1 = odd ? hv[2] : hv[1], b0 = odd ? hv[3] : hv[2], b1 = odd ? hv[4] : hv[3], s1 = odd ? hv[0] : hv[4];
    st16<AUX3>(rw, hb + pb * 8192, (u32x4){a0[0], a0[1], a1[0], a1[1]});
    st16<AUX3>(rw, hb + (pb + 1) * 8192, (u32x4){b0[0], b0[1], b1[0], b1[1]});
    st8<AUX3>(rw, hb + ps * 8192 + odd * 8, s1);
    const float mean = xsum16_32(s) * (1.f / 80.f);
    float m2 = 0.f;
#pragma unroll
    for (int j = 0; j < 5; ++j) {
      const float d0 = bflo(hv[j][0]) - mean, d1 = bfhi(hv[j][0]) - mean, d2 = bflo(hv[j][1]) - mean, d3 = bfhi(hv[j][1]) - mean;
      m2 += (d0 * d0 + d1 * d1) + (d2 * d2 + d3 * d3);
    }
    m2 = xsum16_32(m2);
    if (g == 0) st8<AUX3>(rw, WS_ST + (tok * 8 + ct) * 8, (u32x2){__float_as_uint(mean), __float_as_uint(m2)});
  }
  TS();
  if (w == 0) { TS_END(DOWN ? 4 : 2); }
}

extern "C" __global__ __launch_bounds__(512, 2) void k_oproj(const u16* __restrict__ x, char* __restrict__ ws) { n80_body<false>(x, ws, nullptr); }

constexpr int K4_NS = 4, K4_STG = 20480, K4_NT = 20, K4_CTL = K4_NS * K4_STG, K4_SD = K4_CTL + 2048, K4_LDS = K4_SD + 8192 + 6144;

extern "C" __global__ __launch_bounds__(512, 2) void k_gateup(char* __restrict__ ws) {
  extern __shared__ __attribute__((aligned(16))) char smem[];
  ldsp lds = (ldsp)smem;
  ldsp ctl = lds + K4_CTL;
  const int lane = threadIdx.x & 63, w = __builtin_amdgcn_readfirstlane(threadIdx.x >> 6);
  const unsigned lds0 = (unsigned)(uintptr_t)lds;
  const int xcd = blockIdx.x & 7, idx = blockIdx.x >> 3, mt = (xcd >> 2) * 8 + (idx >> 2), ft = (xcd & 3) * 4 + (idx & 3);
  if (w >= 4) {
    auto extra = [&] { touch(ws + WS_WD + (xcd & 1) * 4 * 245760 + (idx * 4 + w - 4) * 7680, lane, 60, lds0 + K4_CTL); };
    if (w < 6) linear_loader<4, K4_NT, K4_NS, 8192, K4_STG, 1>(ws + WS_H + mt * K4_NT * 8192 + (w - 4) * 4096, lds0 + (w - 4) * 4096, lane * 16,
                                                               [&] { dma_run<4>(ws + WS_ST + mt * 8192 + (w - 4) * 4096, lds0 + K4_SD + (w - 4) * 4096, lane * 16); }, extra);
    else linear_loader<6, K4_NT, K4_NS, 12288, K4_STG, 1>(ws + WS_WGU + ft * K4_NT * 12288 + (w - 6) * 6144, lds0 + 8192 + (w - 6) * 6144, lane * 16,
                                                          [&] { dma_run<3>(ws + WS_CS + ft * 6144 + (w - 6) * 3072, lds0 + K4_SD + 8192 + (w - 6) * 3072, lane * 16); }, extra);
    return;
  }
  const int r32 = lane & 31, hi = lane >> 5, wm = w >> 1, wn = w & 1;
  TS_DECL;
  f32x16 acc[2][3];
#pragma unroll
  for (int i = 0; i < 2; ++i)
#pragma unroll
    for (int j = 0; j < 3; ++j) { acc[i][j] = (f32x16){}; pin(acc[i][j]); }
  bf16x8 af[2][2], wf[2][3];
  const int aoff = wm * 2048 + lane * 16, woff = 8192 + wn * 3072 + lane * 16;
  auto rda = [&](ldsp sb, int s, int i) { af[s][i] = lds_ld16(sb + aoff + s * 4096 + i * 1024); };
  auto rdw = [&](ldsp sb, int s, int j) { wf[s][j] = lds_ld16(sb + woff + s * 6144 + j * 1024); };
  auto mm = [&](int s, int i, int j) { acc[i][j] = mfma32(wf[s][j], af[s][i], acc[i][j]); };
  bar();
  const int tok = mt * 128 + wm * 64 + r32;
  f32x4 sv[2][2];
  float cv[8];
  const LAS float* cp = (const LAS float*)(lds + K4_SD + 8192) + w * 48 + (lane < 48 ? lane : 47);
  ldsp sp = lds + K4_SD + (wm * 64 + r32) * 64 + hi * 32;
  TS();
  rda(lds, 0, 0); rdw(lds, 0, 0); rda(lds, 0, 1); rdw(lds, 0, 1); rdw(lds, 0, 2); SB0();
  constexpr float GC0 = -2.f * 0.7978845608028654f * 1.4426950408889634f, GC1 = GC0 * 0.044715f;
  float ca[2], cb[2], nmu[2], var[2];
  sfor<0, K4_NT>([&](auto tc) {
    constexpr int t = decltype(tc)::value;
    constexpr bool more = t + 1 < K4_NT;
    ldsp sb = lds + (t % K4_NS) * K4_STG;
    ldsp nb = lds + ((t + 1) % K4_NS) * K4_STG;
    mm(0, 0, 0); rda(sb, 1, 0); rdw(sb, 1, 0); SB0();
    mm(0, 1, 0); rda(sb, 1, 1); rdw(sb, 1, 1); SB0();
    mm(0, 0, 1); rdw(sb, 1, 2); SB0();
    mm(0, 1, 1); SB0();
    mm(0, 0, 2); SB0();
    mm(0, 1, 2); SB0();
    if (more) { bar(); SB0(); }
    mm(1, 0, 0); if (more) { rda(nb, 0, 0); rdw(nb, 0, 0); } SB0();
    mm(1, 1, 0); if (more) { rda(nb, 0, 1); rdw(nb, 0, 1); } SB0();
    mm(1, 0, 1); if (more) rdw(nb, 0, 2); SB0();
    if constexpr (t >= 4 && t <= 7) { cv[2 * t - 8] = cp[(2 * t - 8) * 192]; cv[2 * t - 7] = cp[(2 * t - 7) * 192]; sv[(t - 4) >> 1][t & 1] = *(const LAS f32x4*)(sp + ((t - 4) >> 1) * 2048 + (t & 1) * 16); }
    if constexpr (t == 8) { if (lane < 48) ((LAS float*)(ctl + 1024))[w * 48 + lane] = ((cv[0] + cv[1]) + (cv[2] + cv[3])) + ((cv[4] + cv[5]) + (cv[6] + cv[7])); }
    if constexpr (t == 9 || t == 10) {
      constexpr int i = t - 9;
      const f32x4 s0 = sv[i][0], s1 = sv[i][1];
      const float mu = xsum32((s0[0] + s0[2]) + (s1[0] + s1[2])) * 0.125f;
      const float d0 = s0[0] - mu, d1 = s0[2] - mu, d2 = s1[0] - mu, d3 = s1[2] - mu;
      var[i] = xsum32((s0[1] + s0[3]) + (s1[1] + s1[3]) + 80.f * ((d0 * d0 + d1 * d1) + (d2 * d2 + d3 * d3))) * (1.f / D_) + LN_EPS;
      const float rstd = __builtin_amdgcn_rsqf(var[i]);
      ca[i] = GC0 * rstd; cb[i] = GC1 * rstd * rstd * rstd; nmu[i] = -mu;
    }
    mm(1, 1, 1);
    mm(1, 0, 2);
    mm(1, 1, 2);
    if constexpr (t == 9 || t == 10) { pin(ca[t - 9]); pin(cb[t - 9]); pin(nmu[t - 9]); pin(var[t - 9]); }
    SB0();
  });
  TS();
  const rsrc_t rw = mk_rsrc(ws, WS_END);
  const LAS float* csb = (const LAS float*)(ctl + 1024) + wn * 96 + 4 * hi;
  f32x4 cq[3][4];
#pragma unroll
  for (int j = 0; j < 3; ++j)
#pragma unroll
    for (int b = 0; b < 4; ++b) cq[j][b] = *(const LAS f32x4*)(csb + j * 32 + 8 * b);
#pragma unroll
  for (int i = 0; i < 2; ++i) {
    const int tk = tok + 32 * i;
    const int ob = WS_ACT + (((tk >> 6) * 48 + ft * 3) * 4 + ((tk >> 4) & 3)) * 1024 + (hi * 16 + (tk & 15)) * 16;
#pragma unroll
    for (int j = 0; j < 3; ++j) {
      unsigned pk[4];
#pragma unroll
      for (int r = 0; r < 8; r += 2) {
        const f32x2 gv = (f32x2){acc[i][j][r], acc[i][j][r + 1]} + nmu[i] * (f32x2){cq[j][r >> 2][r & 3], cq[j][r >> 2][(r & 3) + 1]};
        const f32x2 uv = (f32x2){acc[i][j][8 + r], acc[i][j][9 + r]} + nmu[i] * (f32x2){cq[j][2 + (r >> 2)][r & 3], cq[j][2 + (r >> 2)][(r & 3) + 1]};
        const f32x2 arg = gv * (gv * gv * cb[i] + ca[i]);
        const f32x2 ex = {__builtin_amdgcn_exp2f(arg[0]), __builtin_amdgcn_exp2f(arg[1])};
        const f32x2 den = ex * var[i] + var[i];
        const f32x2 rc = {__builtin_amdgcn_rcpf(den[0]), __builtin_amdgcn_rcpf(den[1])};
        const f32x2 a = gv * uv * rc;
        pk[r >> 1] = cvtpk(a[0], a[1]);
      }
      const int T = wn * 3 + j;
      st16<AUX4>(rw, ob + (T >> 1) * 4096 + (T & 1) * 512, (u32x4){pk[0], pk[1], pk[2], pk[3]});
    }
  }
  TS();
  if (w == 0) { TS_END(3); }
}

extern "C" __global__ __launch_bounds__(512, 2) void k_down(char* __restrict__ ws, u16* __restrict__ out) { n80_body<true>(nullptr, ws, out); }
extern "C" __global__ void k_text_end() {}

extern "C" void kernel_launch(void* const* d_in, const int* in_sizes, int n_in, void* d_out, int out_size, void* d_ws, size_t ws_size,
                              hipStream_t stream) {
  const u16* x = (const u16*)d_in[0];
  const u16* wqkv = (const u16*)d_in[1];
  const u16* wo = (const u16*)d_in[2];
  const u16* wg = (const u16*)d_in[4];
  const u16* wu = (const u16*)d_in[5];
  const u16* wd = (const u16*)d_in[6];
  char* ws = (char*)d_ws;
  u16* out = (u16*)d_out;
  (void)hipFuncSetAttribute((const void*)k_qkv, hipFuncAttributeMaxDynamicSharedMemorySize, K1_LDS);
  (void)hipFuncSetAttribute((const void*)k_attn, hipFuncAttributeMaxDynamicSharedMemorySize, K2_LDS);
  (void)hipFuncSetAttribute((const void*)k_oproj, hipFuncAttributeMaxDynamicSharedMemorySize, N80_LDS);
  (void)hipFuncSetAttribute((const void*)k_down, hipFuncAttributeMaxDynamicSharedMemorySize, N80_LDS);
  (void)hipFuncSetAttribute((const void*)k_gateup, hipFuncAttributeMaxDynamicSharedMemorySize, K4_LDS);
#define REP 1
  for (int r = 0; r < REP; ++r) k_qkv<<<256, 512, K1_LDS, stream>>>(x, wqkv, wo, ws);
  for (int r = 0; r < REP; ++r) k_attn<<<256, 768, K2_LDS, stream>>>(ws, wg, wu, wd);
  for (int r = 0; r < REP; ++r) k_oproj<<<256, 512, N80_LDS, stream>>>(x, ws);
  for (int r = 0; r < REP; ++r) k_gateup<<<256, 512, K4_LDS, stream>>>(ws);
  for (int r = 0; r < REP; ++r) k_down<<<256, 512, N80_LDS, stream>>>(ws, out);
}
```

```cpp
#include <hip/hip_runtime.h>
#include <stdint.h>
#include <type_traits>

#define LAS __attribute__((address_space(3)))
#define DI __device__ __forceinline__
typedef unsigned short u16;
typedef short bf16x8 __attribute__((ext_vector_type(8)));
typedef short bf16x4 __attribute__((ext_vector_type(4)));
typedef __bf16 bf16x2_t __attribute__((ext_vector_type(2)));
typedef float f32x2 __attribute__((ext_vector_type(2)));
typedef float f32x4 __attribute__((ext_vector_type(4)));
typedef float f32x16 __attribute__((ext_vector_type(16)));
typedef unsigned u32x4 __attribute__((ext_vector_type(4)));
typedef unsigned u32x2 __attribute__((ext_vector_type(2)));
typedef LAS char* ldsp;
typedef __amdgpu_buffer_rsrc_t rsrc_t;

constexpr int S_ = 2048, D_ = 640, NH = 32, DF = 1536;
constexpr float LN_EPS = 1e-5f;
constexpr float QSCALE = 0.22360679774997896f * 1.4426950408889634f;
constexpr unsigned ONES2 = 0x3f803f80u;
#define AUX_ST 16

constexpr int V_TILE = 1344;
constexpr int WS_QK = 0;
constexpr int WS_V = WS_QK + 160 * S_ * 16;
constexpr int WS_O = WS_V + NH * 64 * V_TILE;
constexpr int WS_H = WS_O + S_ * D_ * 2;
constexpr int WS_ACT = WS_H + S_ * D_ * 2;
constexpr int WS_ST = WS_ACT + S_ * DF * 2;
constexpr int WS_CS = WS_ST + S_ * 8 * 8;
constexpr int WS_WO = WS_CS + 8 * 3072 * 4;
constexpr int WS_WGU = WS_WO + D_ * D_ * 2;
constexpr int WS_WD = WS_WGU + D_ * DF * 4;
constexpr int WS_END = WS_WD + D_ * DF * 2;

#define TS_DECL
#define TS()
#define TS_END(kid)

template <int I, int N, class F> DI void sfor(F&& f) {
  if constexpr (I < N) { f(std::integral_constant<int, I>{}); sfor<I + 1, N>(f); }
}
#define SB0() __builtin_amdgcn_sched_barrier(0)
DI unsigned cvtpk(float lo, float hi) { f32x2 v = {lo, hi}; return __builtin_bit_cast(unsigned, __builtin_convertvector(v, bf16x2_t)); }
DI float bflo(unsigned u) { return __uint_as_float(u << 16); }
DI float bfhi(unsigned u) { return __uint_as_float(u & 0xffff0000u); }
DI float dot2(unsigned a, unsigned b, float c) {
  return __builtin_amdgcn_fdot2_f32_bf16(__builtin_bit_cast(bf16x2_t, a), __builtin_bit_cast(bf16x2_t, b), c, false);
}
DI f32x4 mfma16(bf16x8 a, bf16x8 b, f32x4 c) { return __builtin_amdgcn_mfma_f32_16x16x32_bf16(a, b, c, 0, 0, 0); }
DI f32x16 mfma32(bf16x8 a, bf16x8 b, f32x16 c) { return __builtin_amdgcn_mfma_f32_32x32x16_bf16(a, b, c, 0, 0, 0); }
DI void set_m0(unsigned v) { asm volatile("s_mov_b32 m0, %0\n\ts_nop 0" ::"s"(v) : "memory"); }
template <int OFF> DI void dma(const void* sbase, unsigned voff) {
  asm volatile("global_load_lds_dwordx4 %0, %1 offset:%2" ::"v"(voff), "s"(sbase), "n"(OFF) : "memory");
}
template <int OFF> DI void dma_nt(const void* sbase, unsigned voff) {
  asm volatile("global_load_lds_dwordx4 %0, %1 offset:%2 nt" ::"v"(voff), "s"(sbase), "n"(OFF) : "memory");
}
template <int N> DI void wait_vm() { asm volatile("s_waitcnt vmcnt(%0)" ::"n"(N) : "memory"); }
template <int N> DI void wait_vm_bar() { asm volatile("s_waitcnt vmcnt(%0) lgkmcnt(0)\n\ts_barrier" ::"n"(N) : "memory"); }
DI void bar() { asm volatile("s_waitcnt lgkmcnt(0)\n\ts_barrier" ::: "memory"); }
template <class T> DI void pin(T& v) { asm volatile("" : "+v"(v)); }
DI bf16x8 lds_ld16(ldsp p) { return *(const LAS bf16x8*)p; }
DI bf16x4 lds_tr(ldsp p) { return __builtin_amdgcn_ds_read_tr16_b64_v4i16((LAS bf16x4*)p); }
DI bf16x8 lds_tr2(ldsp p0, ldsp p1) {
  bf16x4 a = lds_tr(p0), b = lds_tr(p1);
  return __builtin_shufflevector(a, b, 0, 1, 2, 3, 4, 5, 6, 7);
}
DI float xsum32(float v) {
  auto r = __builtin_amdgcn_permlane32_swap(__float_as_uint(v), __float_as_uint(v), false, false);
  return __uint_as_float(r[0]) + __uint_as_float(r[1]);
}
DI float xsum16_32(float v) {
  auto r = __builtin_amdgcn_permlane16_swap(__float_as_uint(v), __float_as_uint(v), false, false);
  return xsum32(__uint_as_float(r[0]) + __uint_as_float(r[1]));
}
DI rsrc_t mk_rsrc(void* p, int bytes) { return __builtin_amdgcn_make_buffer_rsrc(p, 0, bytes, 0x00020000); }
template <int AUX = 16> DI void st16(rsrc_t r, int off, u32x4 v) { __builtin_amdgcn_raw_buffer_store_b128(v, r, off, 0, AUX); }
template <int AUX = 16> DI void st8(rsrc_t r, int off, u32x2 v) { __builtin_amdgcn_raw_buffer_store_b64(v, r, off, 0, AUX); }
#define AUX3 0
#define AUX4 16

template <int P, int NT, int NS, int X, class F, class G0, class G> DI void loader_loop(F&& issue, G0&& early, G&& extra) {
  static_assert(NS >= 4, "three stages in flight + one being read");
  issue(0); issue(1);
  wait_vm_bar<P>();
  early();
  issue(2); issue(3);
  int slot = 4 % NS;
#pragma unroll 1
  for (int t = 1; t < NT - 3; ++t) {
    wait_vm_bar<2 * P>();
    issue(slot);
    slot = slot == NS - 1 ? 0 : slot + 1;
  }
  extra();
  wait_vm_bar<2 * P + X>(); wait_vm_bar<P + X>(); wait_vm_bar<X>();
}
DI void touch(const char* p, int lane, int nlines, unsigned lds_scratch) {
  set_m0(lds_scratch);
  asm volatile("global_load_lds_dword %0, %1" ::"v"((unsigned)((lane < nlines ? lane : nlines - 1) * 128)), "s"(p) : "memory");
}
#define CODE_ADDR(dst, sym) asm volatile("s_getpc_b64 s[40:41]\n\ts_add_u32 s40, s40, " #sym "@rel32@lo+4\n\ts_addc_u32 s41, s41, " #sym "@rel32@hi+12\n\ts_mov_b64 %0, s[40:41]" : "=s"(dst) :: "s40", "s41", "scc")
template <int P> DI void dma_run(const char* src, unsigned dst, unsigned lane16) {
  static_assert(P <= 8, "offset range");
  set_m0(dst + 4096);
  sfor<0, P>([&](auto ic) { dma<decltype(ic)::value * 1024 - 4096>(src + 4096, lane16); });
}
template <int P, int NT, int NS, int SSTR, int LSTR, int X, class G0, class G> DI void linear_loader(const char* src, unsigned dst, unsigned lane16, G0&& early, G&& extra) {
  loader_loop<P, NT, NS, X>([&](int slot) { dma_run<P>(src, dst + slot * LSTR, lane16); src += SSTR; }, early, extra);
}

template <int KIND> DI void pack_issue(const u16* wsrc, const u16* wsrc2, int ks, int cg, unsigned ldsdst, int lane) {
  constexpr int PITCH = KIND == 1 ? DF * 2 : D_ * 2;
  set_m0(ldsdst);
  sfor<0, 4>([&](auto ic) {
    constexpr int g = decltype(ic)::value;
    const int t = (lane >> 5) & 1, q = (lane >> 3) & 3;
    int row;
    if (KIND == 0) row = ks < 16 ? 20 * (2 * ks + (g >> 1)) + 8 * t + 4 * (g & 1) + q : 20 * (8 * (ks - 16) + 2 * g + t) + 16 + q;
    else if (KIND == 1) row = 32 * (ks >> 1) + 16 * t + 4 * (2 * (ks & 1) + (g & 1)) + q;
    else row = 32 * ks + 16 * (g >> 1) + 8 * t + 4 * (g & 1) + q;
    dma_nt<g * 1024>((const char*)(KIND == 1 && g >= 2 ? wsrc2 : wsrc) + cg * 128 - 4096, (unsigned)(row * PITCH + (lane & 7) * 16 + 4096 - g * 1024));
  });
}
template <int KIND> DI void pack_finish(ldsp u, rsrc_t rw, int ks, int cg, int lane, LAS float* cs) {
  const int q = (lane & 15) >> 2, p = lane & 3;
  ldsp a = KIND == 1 ? u + ((lane >> 4) & 1) * 2048 + (8 * (lane >> 5) + q) * 128 + p * 8 : u + (8 * (lane >> 4) + q) * 128 + p * 8;
#pragma unroll
  for (int i = 0; i < 4; ++i) {
    const int nt = 4 * cg + i;
    const u32x4 f = __builtin_bit_cast(u32x4, lds_tr2(a + i * 32, a + i * 32 + 512));
    if (KIND == 1) {
      const int ft = nt / 6, j = nt - ft * 6;
      st16(rw, WS_WGU + ((ft * 40 + ks) * 6 + j) * 1024 + lane * 16, f);
      const unsigned sv = (lane & 15) == ((lane >> 4) & 1) ? ONES2 : 0u;
      const f32x4 c = mfma16(__builtin_bit_cast(bf16x8, (u32x4){sv, sv, sv, sv}), __builtin_bit_cast(bf16x8, f), (f32x4){0.f, 0.f, 0.f, 0.f});
      if (lane < 16) { cs[i * 32 + lane] = c[0]; cs[i * 32 + 16 + lane] = c[1]; }
    } else {
      const int ct = nt / 5, j = nt - ct * 5;
      st16(rw, (KIND == 0 ? WS_WO : WS_WD) + ((ct * (KIND == 0 ? 20 : 48) + ks) * 5 + j) * 1024 + lane * 16, f);
    }
  }
}

#define WD0 8
#define WD1 24
constexpr int K1_NS = 4, K1_STG = 32768, K1_NT = 10, K1_CTL = K1_NS * K1_STG, K1_LDS = K1_CTL + 4096;

template <bool ISV> DI void qkv_compute(ldsp lds, char* __restrict__ ws, int mt, int nt, int w, int lane) {
  const int wm = w >> 1, wn = w & 1, r32 = lane & 31, hi = lane >> 5, q = (lane & 15) >> 2, p = lane & 3, blk = (lane >> 4) & 1;
  TS_DECL;
  ldsp ctl = lds + K1_CTL;
  int aoff[4], woff[2];
#pragma unroll
  for (int s = 0; s < 4; ++s) aoff[s] = (wm * 64 + wn * 32 + r32) * 128 + (((2 * s + hi) ^ ((r32 >> 1) & 7)) * 16);
  const int astep = wn ? -4096 : 4096;
#pragma unroll
  for (int j = 0; j < 2; ++j) woff[j] = 16384 + (8 * hi + q) * 256 + (((wn * 2 + (j ^ wm)) ^ q) * 64) + blk * 32 + p * 8;
  f32x16 acc[2][2];
#pragma unroll
  for (int j = 0; j < 2; ++j)
#pragma unroll
    for (int i = 0; i < 2; ++i) { acc[j][i] = (f32x16){}; pin(acc[j][i]); }
  f32x4 rs = {}, rq = {}, cs = {};
  const unsigned sv = (lane & 15) == blk ? ONES2 : 0u;
  const bf16x8 sel = __builtin_bit_cast(bf16x8, (u32x4){sv, sv, sv, sv});
  bf16x8 xf[2][2], wf[2][2];
  auto rdx = [&](ldsp sb, int s, int i) { xf[s & 1][i] = lds_ld16(sb + aoff[s] + (i ? astep : 0)); };
  auto rdw = [&](ldsp sb, int s, int j) { wf[s & 1][j] = lds_tr2(sb + woff[j] + s * 4096, sb + woff[j] + s * 4096 + 1024); };
  auto mm = [&](int s, int j, int i) { acc[j][i] = ISV ? mfma32(xf[s & 1][i], wf[s & 1][j], acc[j][i]) : mfma32(wf[s & 1][j], xf[s & 1][i], acc[j][i]); };
  auto step = [&](ldsp nb, int s, int sn, bool rd) {
    const bf16x8 xa = xf[s & 1][0];
    const bf16x4 xl = __builtin_shufflevector(xa, xa, 0, 1, 2, 3), xh = __builtin_shufflevector(xa, xa, 4, 5, 6, 7);
    mm(s, 0, 0); if (rd) { rdx(nb, sn, 0); rdw(nb, sn, 0); } SB0();
    rq = __builtin_amdgcn_mfma_f32_4x4x4bf16_1k(xl, xl, rq, 0, 0, 0);
    mm(s, 0, 1); if (rd) { rdx(nb, sn, 1); rdw(nb, sn, 1); } SB0();
    rs = mfma16(sel, xa, rs);
    mm(s, 1, 0); SB0();
    rq = __builtin_amdgcn_mfma_f32_4x4x4bf16_1k(xh, xh, rq, 0, 0, 0);
    mm(s, 1, 1); SB0();
    cs = mfma16(sel, wf[s & 1][0], cs); SB0();
  };
  bar();
  TS();
  rdx(lds, 0, 0); rdw(lds, 0, 0); rdx(lds, 0, 1); rdw(lds, 0, 1); SB0();
  sfor<0, K1_NT>([&](auto tc) {
    constexpr int t = decltype(tc)::value;
    ldsp sb = lds + (t % K1_NS) * K1_STG;
    step(sb, 0, 1, true); step(sb, 1, 2, true); step(sb, 2, 3, true);
    if (t + 1 < K1_NT) { bar(); SB0(); }
    step(lds + ((t + 1) % K1_NS) * K1_STG, 3, 0, t + 1 < K1_NT);
  });
  TS();
  LAS f32x2* rst = (LAS f32x2*)(ctl + 1024);
  LAS float* cst = (LAS float*)(ctl + 2048);
  {
    const float sq = xsum32(p == 0 ? rq[0] : p == 1 ? rq[1] : p == 2 ? rq[2] : rq[3]);
    const float mu = __uint_as_float(__builtin_amdgcn_permlane16_swap(__float_as_uint(rs[0]), __float_as_uint(rs[1]), false, false)[0]) * (1.f / D_), var = fmaxf(sq * (1.f / D_) - mu * mu, 0.f);
    if (hi == 0) rst[wm * 64 + wn * 32 + r32] = (f32x2){mu, __builtin_amdgcn_rsqf(var + LN_EPS)};
    if (lane < 16) { cst[wn * 64 + wm * 32 + lane] = cs[0]; cst[wn * 64 + wm * 32 + 16 + lane] = cs[1]; }
  }
  bar();
  TS();
  const rsrc_t rw = mk_rsrc(ws, WS_END);
  if constexpr (!ISV) {
    const float sc = nt < 5 ? QSCALE : 1.f;
#pragma unroll
    for (int i = 0; i < 2; ++i) {
      const int ml = wm * 64 + (i ^ wn) * 32 + r32;
      const f32x2 st = rst[ml];
      const float a = st.y * sc, nm = -st.x * a;
      const int ob = WS_QK + ((nt * 16 + wn * 8 + hi) * S_ + mt * 128 + ml) * 16;
#pragma unroll
      for (int j = 0; j < 2; ++j)
#pragma unroll
        for (int bp = 0; bp < 2; ++bp) {
          unsigned pk[2][2];
#pragma unroll
          for (int e = 0; e < 2; ++e) {
            const int b = 2 * bp + e;
            const f32x4 c4 = *(const LAS f32x4*)(cst + wn * 64 + (j ^ wm) * 32 + 8 * b + 4 * hi);
            const f32x2 v0 = (f32x2){acc[j][i][4 * b], acc[j][i][4 * b + 1]} * a + nm * (f32x2){c4[0], c4[1]};
            const f32x2 v1 = (f32x2){acc[j][i][4 * b + 2], acc[j][i][4 * b + 3]} * a + nm * (f32x2){c4[2], c4[3]};
            pk[e][0] = cvtpk(v0[0], v0[1]); pk[e][1] = cvtpk(v1[0], v1[1]);
          }
          auto r0 = __builtin_amdgcn_permlane32_swap(pk[0][0], pk[1][0], false, false);
          auto r1 = __builtin_amdgcn_permlane32_swap(pk[0][1], pk[1][1], false, false);
          st16(rw, ob + ((j ^ wm) * 4 + bp * 2) * S_ * 16, (u32x4){r0[0], r1[0], r0[1], r1[1]});
        }
    }
  } else {
#pragma unroll
    for (int j = 0; j < 2; ++j) {
      const int nl = wn * 64 + (j ^ wm) * 32 + r32, n = (nt - 10) * 128 + nl, h = n / 20, d = n - h * 20;
      const float c = cst[nl];
      const int ob = WS_V + (h * 64 + mt * 4 + wm * 2) * V_TILE + hi * 336 + d * 16;
#pragma unroll
      for (int i = 0; i < 2; ++i)
#pragma unroll
        for (int s = 0; s < 2; ++s) {
          unsigned pk[4];
#pragma unroll
          for (int e = 0; e < 2; ++e) {
            const int b = 2 * s + e;
            const LAS f32x4* sp = (const LAS f32x4*)(rst + wm * 64 + (i ^ wn) * 32 + 8 * b + 4 * hi);
            const f32x4 s01 = sp[0], s23 = sp[1];
            pk[2 * e] = cvtpk((acc[j][i][4 * b] - s01[0] * c) * s01[1], (acc[j][i][4 * b + 1] - s01[2] * c) * s01[3]);
            pk[2 * e + 1] = cvtpk((acc[j][i][4 * b + 2] - s23[0] * c) * s23[1], (acc[j][i][4 * b + 3] - s23[2] * c) * s23[3]);
          }
          const int o = ob + (i ^ wn) * V_TILE + s * 672;
          st16(rw, o, (u32x4){pk[0], pk[1], pk[2], pk[3]});
          if (d == 19) st16(rw, o + 16, (u32x4){ONES2, ONES2, ONES2, ONES2});
        }
    }
  }
  TS();
  if (w == 0) { TS_END(0); }
}

DI void qkv_loader(ldsp lds, const u16* __restrict__ x, const u16* __restrict__ wqkv, int mt, int nt, int lw, int lane) {
  const bool isx = lw < 2;
  const int half = lw & 1;
  unsigned voff[8];
#pragma unroll
  for (int i = 0; i < 8; ++i) {
    const int r = (half * 8 + i) * 8 + (lane >> 3), c = (lane & 7) ^ ((r >> 1) & 7);
    const int k = (half * 8 + i) * 4 + (lane >> 4), cp = lane & 15;
    voff[i] = (isx ? r * 1280 + c * 16 : k * 3840 + (cp ^ ((k & 3) << 2)) * 16) + 8192 - i * 1024;
  }
  const char* src = (isx ? (const char*)x + (size_t)mt * 128 * 1280 : (const char*)wqkv + nt * 256) - 4096;
  const int sstr = isx ? 128 : 64 * 3840;
  const unsigned dst = (unsigned)(uintptr_t)lds + lw * 8192 + 4096;
  loader_loop<8, K1_NT, K1_NS, 0>([&](int slot) {
    set_m0(dst + slot * K1_STG);
    sfor<0, 8>([&](auto ic) { constexpr int i = decltype(ic)::value; dma<i * 1024 - 4096>(src, voff[i]); });
    src += sstr;
  }, [] {}, [] {});
}

extern "C" __global__ __launch_bounds__(512, 2) void k_qkv(const u16* __restrict__ x, const u16* __restrict__ wqkv, const u16* __restrict__ wo, char* __restrict__ ws,
    const u16* __restrict__ wg, const u16* __restrict__ wu, const u16* __restrict__ wd, const u16* __restrict__ outp) {
  extern __shared__ __attribute__((aligned(16))) char smem[];
  ldsp lds = (ldsp)smem;
  const int lane = threadIdx.x & 63, w = __builtin_amdgcn_readfirstlane(threadIdx.x >> 6);
  const int xcd = blockIdx.x & 7, idx = blockIdx.x >> 3;
  const int nh = xcd & 1, nt = nh * 8 + (idx >> 2), mt = (xcd >> 1) * 4 + (idx & 3);
  if (w == 2) {
    const char *c0, *c1;
    CODE_ADDR(c0, k_qkv); CODE_ADDR(c1, k_text_end);
    const unsigned lim = (unsigned)(c1 - c0), off = (idx * 16 + (lane & 15)) * 128;
    set_m0((unsigned)(uintptr_t)lds + K1_CTL + 3072 + 512);
    asm volatile("global_load_lds_dword %0, %1" ::"v"(off < lim ? off : lim), "s"(c0) : "memory");
  }
  if (w == 3) {
    constexpr int PGS = 65536;
    const int id = idx * 64 + lane;
    constexpr int n0 = WS_END / PGS, n1 = n0 + D_ * DF * 2 / PGS, n2 = n1 + D_ * DF * 2 / PGS, n3 = n2 + D_ * DF * 2 / PGS, n4 = n3 + S_ * D_ * 2 / PGS;
    static_assert(n4 <= 2048, "one lane per page");
    const char* p = id < n0 ? ws + (size_t)id * PGS : id < n1 ? (const char*)wg + (size_t)(id - n0) * PGS : id < n2 ? (const char*)wu + (size_t)(id - n1) * PGS
                  : id < n3 ? (const char*)wd + (size_t)(id - n2) * PGS : id < n4 ? (const char*)outp + (size_t)(id - n3) * PGS : ws;
    set_m0((unsigned)(uintptr_t)lds + K1_CTL + 3072 + 768);
    __builtin_amdgcn_s_sleep(32);
    asm volatile("global_load_lds_dword %0, off" ::"v"(p) : "memory");
  }
  if (w < 2) {
    const int id = blockIdx.x * 64 + lane;
    const bool isx = blockIdx.x < 128;
    const int i2 = id - 8192 < 7680 ? id - 8192 : 7679;
    const unsigned off = isx ? (id >> 2) * 1280 + 256 + w * 512 + (id & 3) * 128 : (128 + w * 256) * 3840 + i2 * 128;
    set_m0((unsigned)(uintptr_t)lds + K1_CTL + 3072 + w * 256);
    const char* src = isx ? (const char*)x : (const char*)wqkv;
    asm volatile("" : "+s"(src));
    if (w == 0) __builtin_amdgcn_s_sleep(WD0); else __builtin_amdgcn_s_sleep(WD1);
    asm volatile("global_load_lds_dword %0, %1" ::"v"(off), "s"(src) : "memory");
  }
  if (nt == 15) {
    const int u0 = mt * 8 + w;
    const rsrc_t rw = mk_rsrc(ws, WS_END);
#pragma unroll
    for (int r = 0; r < 2; ++r) { const int u = u0 + r * 128; if (u < 200) pack_issue<0>(wo, wo, u / 10, u % 10, (unsigned)(uintptr_t)lds + (w * 2 + r) * 4096, lane); }
    wait_vm<0>();
#pragma unroll
    for (int r = 0; r < 2; ++r) { const int u = u0 + r * 128; if (u < 200) pack_finish<0>(lds + (w * 2 + r) * 4096, rw, u / 10, u % 10, lane, nullptr); }
    return;
  }
  if (w >= 4) { qkv_loader(lds, x, wqkv, mt, nt, w - 4, lane); return; }
  if (nt >= 10) qkv_compute<true>(lds, ws, mt, nt, w, lane);
  else qkv_compute<false>(lds, ws, mt, nt, w, lane);
}

#define VLEAD 3
constexpr int K2_V = 24576, K2_PK = K2_V + 16 * V_TILE + 256, K2_CS = K2_PK + 8 * 4096, K2_MSK = K2_CS + 3072, K2_TM = K2_MSK + 2048, K2_LDS = K2_TM + 8192;

DI void attn_helper(ldsp lds, char* __restrict__ ws, int c, int h, int hw, int lane, const u16* __restrict__ wg, const u16* __restrict__ wu, const u16* __restrict__ wd) {
  const unsigned lds0 = (unsigned)(uintptr_t)lds;
  const int pm = 5 * (h >> 1) + 3 * (h & 1), pt = 5 * (h >> 1) + 2;
  const bool EDGE = c == 0;
  {
    const int tok0 = 256 * c - 256 + 128 * hw + ((EDGE && hw < 2) ? 256 : 0);
    const char* kb = ws + WS_QK + (80 * S_ + tok0) * 16;
    set_m0(lds0 + hw * 2048); dma<0>(kb + pm * S_ * 16, lane * 16); dma<1024>(kb + pm * S_ * 16, lane * 16);
    set_m0(lds0 + 8192 + hw * 2048); dma<0>(kb + (pm + 1) * S_ * 16, lane * 16); dma<1024>(kb + (pm + 1) * S_ * 16, lane * 16);
    set_m0(lds0 + 16384 + hw * 2048); dma<0>(kb + pt * S_ * 16, lane * 16); dma<1024>(kb + pt * S_ * 16, lane * 16);
  }
  const int b = blockIdx.x;
  if (lane == 0) *(LAS unsigned*)(lds + K2_CS + 2560 + 16 + hw * 4) = 0u;
  if (hw == 0 && lane == 0) *(LAS unsigned*)(lds + K2_CS + 2560) = 0u;
  if (hw == 1 && lane < 16) *(LAS u32x2*)(lds + K2_TM + lane * 512) = (u32x2){EDGE && lane < 8 ? 0xff80u : 0u, 0u};
  wait_vm<VLEAD>();
  {
    const char* vb = ws + WS_V + (h * 64 + 8 * c - 8) * V_TILE + 4096;
    set_m0(lds0 + K2_V + hw * 5120 + 4096);
    sfor<0, 6>([&](auto ic) {
      constexpr int i = decltype(ic)::value;
      unsigned off = hw * 5120 + lane * 16;
      if (EDGE && off + i * 1024 < 8u * V_TILE) off += 8 * V_TILE;
      dma<i * 1024 - 4096>(vb, off);
    });
    wait_vm_bar<6>();
    wait_vm<0>();
    if (lane == 0) *(volatile LAS unsigned*)(lds + K2_CS + 2560 + 16 + hw * 4) = 1u;
  }
  const rsrc_t rw = mk_rsrc(ws, WS_END);
  const unsigned u0 = lds0 + K2_PK + hw * 4096;
  if (b < 192) {
    const int pcg = b % 24, pks = (b / 24) * 5;
    pack_issue<1>(wg, wu, pks + hw, pcg, u0, lane);
    if (hw == 0) pack_issue<1>(wg, wu, pks + 4, pcg, u0 + 16384, lane);
    if (hw >= 2) touch(ws + WS_WO + (b & 1) * 409600 + ((b >> 3) * 2 + hw - 2) * 6400, lane, 50, lds0 + K2_CS + 2816);
    wait_vm<0>();
    LAS float* ct = (LAS float*)(lds + K2_CS);
    pack_finish<1>(lds + K2_PK + hw * 4096, rw, pks + hw, pcg, lane, ct + hw * 128);
    if (hw == 0) pack_finish<1>(lds + K2_PK + 16384, rw, pks + 4, pcg, lane, ct + 512);
    asm volatile("s_waitcnt lgkmcnt(0)" ::: "memory");
    unsigned old = 0;
    if (lane == 0) old = __hip_atomic_fetch_add((LAS unsigned*)(lds + K2_CS + 2560), 1u, __ATOMIC_RELAXED, __HIP_MEMORY_SCOPE_WORKGROUP);
    if (__builtin_amdgcn_readfirstlane(old) == 3) {
      asm volatile("" ::: "memory");
      const LAS f32x2* c2 = (const LAS f32x2*)ct + lane;
      const f32x2 a = ((c2[0] + c2[64]) + (c2[128] + c2[192])) + c2[256];
      const int e = pcg * 128 + lane * 2, ft = e / 192;
      st8(rw, WS_CS + ((ft * 8 + b / 24) * 192 + e - ft * 192) * 4, __builtin_bit_cast(u32x2, a));
    }
  } else {
    const int bb = b < 252 ? b - 192 : 0, pcg = bb % 10, pks = (bb / 10) * 8 + hw;
    pack_issue<2>(wd, wd, pks, pcg, u0, lane);
    pack_issue<2>(wd, wd, pks + 4, pcg, u0 + 16384, lane);
    if (hw >= 2) touch(ws + WS_WO + (b & 1) * 409600 + ((b >> 3) * 2 + hw - 2) * 6400, lane, 50, lds0 + K2_CS + 2816);
    wait_vm<0>();
    pack_finish<2>(lds + K2_PK + hw * 4096, rw, pks, pcg, lane, nullptr);
    pack_finish<2>(lds + K2_PK + hw * 4096 + 16384, rw, pks + 4, pcg, lane, nullptr);
  }
}

DI void attn_body(ldsp lds, char* __restrict__ ws, int c, int h, int w, int lane) {
  const int r32 = lane & 31, hi = lane >> 5;
  TS_DECL;
  const int pm = 5 * (h >> 1) + 3 * (h & 1), pt = 5 * (h >> 1) + 2;
  const int dq = r32 - 4 * hi;
  const float NEG = -__builtin_inff();
  f32x16 s[8], s8;
#pragma unroll
  for (int r = 0; r < 16; ++r) {
    const bool le = (r & 3) + 8 * (r >> 2) <= dq;
    s8[r] = le ? 0.f : NEG;
    s[0][r] = le ? NEG : 0.f;
  }
  if (w == 0) {
    u32x4 msk[2];
#pragma unroll
    for (int e = 0; e < 8; ++e) msk[e >> 2][e & 3] = (unsigned)((int)__float_as_uint(s8[2 * e]) >> 31) >> 16 | ((unsigned)((int)__float_as_uint(s8[2 * e + 1]) >> 31) << 16);
    *(LAS u32x4*)(lds + K2_MSK + lane * 16) = msk[0]; *(LAS u32x4*)(lds + K2_MSK + 1024 + lane * 16) = msk[1];
  }
  pin(s[0]); pin(s8);
  SB0();
  const char* qb = ws + WS_QK + (256 * c + 32 * w) * 16;
  const bf16x8 qm = *(const bf16x8*)(qb + ((pm + hi) * S_ + r32) * 16);
  u32x2 qtr = *(const u32x2*)(qb + (pt * S_ + r32) * 16 + (h & 1) * 8);
  if (hi) qtr = (u32x2){0x3f80u, 0u};
  bf16x4 qt = __builtin_bit_cast(bf16x4, qtr);
  pin(qt);
  TS();
  bar();
  TS();
  ldsp kp = lds + hi * 8192 + (w * 32 + r32) * 16;
  ldsp ktp = lds + (hi ? K2_TM + w * 512 : 16384 + (w * 32 + r32) * 16 + (h & 1) * 8);
  ldsp vp = lds + K2_V + w * V_TILE + hi * 336 + r32 * 16;
  bf16x8 km[9]; bf16x4 kt[9];
#pragma unroll
  for (int jj = 0; jj < 9; ++jj) { const int j = jj == 0 ? 8 : jj - 1; km[j] = lds_ld16(kp + j * 512); kt[j] = *(const LAS bf16x4*)(ktp + j * 512); }
  SB0();
  auto tail = [&](int j, f32x16 cin) { return __builtin_amdgcn_mfma_f32_32x32x8bf16_1k(kt[j], qt, cin, 0, 0, 0); };
  float mx[4];
  auto maxh = [&](int j, int r0) {
#pragma unroll
    for (int e = 0; e < 4; ++e) mx[e] = __builtin_elementwise_maximum(__builtin_elementwise_maximum(mx[e], s[j][r0 + 2 * e]), s[j][r0 + 2 * e + 1]);
  };
  s8 = mfma32(km[8], qm, s8); s[0] = mfma32(km[0], qm, s[0]); s8 = tail(8, s8);
  s[1] = mfma32(km[1], qm, (f32x16){}); s[0] = tail(0, s[0]);
  s[2] = mfma32(km[2], qm, (f32x16){}); s[1] = tail(1, s[1]); SB0();
  s[3] = mfma32(km[3], qm, (f32x16){});
#pragma unroll
  for (int r = 0; r < 8; ++r) s[0][r] = __builtin_elementwise_maximum(s[0][r], s8[r]);
  SB0();
  s[2] = tail(2, s[2]);
#pragma unroll
  for (int r = 8; r < 16; ++r) s[0][r] = __builtin_elementwise_maximum(s[0][r], s8[r]);
  pin(s[0]);
  SB0();
#pragma unroll
  for (int e = 0; e < 4; ++e) mx[e] = s[0][e];
  s[4] = mfma32(km[4], qm, (f32x16){}); maxh(0, 4); mx[0] = __builtin_elementwise_maximum(__builtin_elementwise_maximum(mx[0], s[0][12]), s[0][13]);
  mx[1] = __builtin_elementwise_maximum(__builtin_elementwise_maximum(mx[1], s[0][14]), s[0][15]); SB0();
  s[3] = tail(3, s[3]); maxh(1, 0); SB0();
  s[5] = mfma32(km[5], qm, (f32x16){}); maxh(1, 8); SB0();
  s[4] = tail(4, s[4]); maxh(2, 0); SB0();
  s[6] = mfma32(km[6], qm, (f32x16){}); maxh(2, 8); SB0();
  s[5] = tail(5, s[5]); maxh(3, 0); SB0();
  s[7] = mfma32(km[7], qm, (f32x16){}); maxh(3, 8); SB0();
  s[6] = tail(6, s[6]); maxh(4, 0); SB0();
  s[7] = tail(7, s[7]); maxh(4, 8);
#pragma unroll
  for (int j = 5; j < 8; ++j) { maxh(j, 0); maxh(j, 8); }
  const float m0 = __builtin_elementwise_maximum(mx[0], mx[1]), m1 = __builtin_elementwise_maximum(mx[2], mx[3]);
  float m = __builtin_elementwise_maximum(m0, m1);
  {
    auto rr = __builtin_amdgcn_permlane32_swap(__float_as_uint(m), __float_as_uint(m), false, false);
    m = __builtin_elementwise_maximum(__uint_as_float(rr[0]), __uint_as_float(rr[1]));
  }
  bf16x4 nm, one4;
  {
    const unsigned u0 = __float_as_uint(m) & 0xffff0000u;
    const float r1 = m - __uint_as_float(u0);
    const unsigned u1 = __float_as_uint(r1) & 0xffff0000u;
    const float r2 = r1 - __uint_as_float(u1);
    const u32x2 v = {hi ? 0u : ((u0 >> 16) | u1), hi ? 0u : (__float_as_uint(r2) >> 16)};
    nm = __builtin_bit_cast(bf16x4, v);
    one4 = __builtin_bit_cast(bf16x4, (u32x2){0xbf80bf80u, 0xbf80bf80u});
  }
  SB0();
  TS();
  f32x16 o = {};
  u32x4 pk[8][2];
  auto sub = [&](int j) { s[j] = __builtin_amdgcn_mfma_f32_32x32x8bf16_1k(one4, nm, s[j], 0, 0, 0); };
  auto ex8 = [&](int j, int ks) {
#pragma unroll
    for (int e = 0; e < 4; ++e) pk[j][ks][e] = cvtpk(__builtin_amdgcn_exp2f(s[j][8 * ks + 2 * e]), __builtin_amdgcn_exp2f(s[j][8 * ks + 2 * e + 1]));
  };
  auto vld = [&](int slot, int ks) { return lds_ld16(vp + slot * V_TILE + ks * 672); };
  auto pv = [&](bf16x8 v, u32x4 p) { o = mfma32(v, __builtin_bit_cast(bf16x8, p), o); };
  sub(7); sub(6); sub(5);
  __builtin_amdgcn_s_setprio(0);
  ex8(7, 0); ex8(7, 1);
  const u32x4 vrdy = *(volatile LAS u32x4*)(lds + K2_CS + 2560 + 16);
  ex8(6, 0); ex8(6, 1);
  pin(pk[7][0]); pin(pk[7][1]); pin(pk[6][0]); pin(pk[6][1]);
  if (__builtin_expect(__builtin_amdgcn_readfirstlane((vrdy[0] & vrdy[1]) & (vrdy[2] & vrdy[3])) == 0, 0)) {
    for (int sp = 0; sp < (1 << 20); ++sp) {
      const u32x4 v = *(volatile LAS u32x4*)(lds + K2_CS + 2560 + 16);
      if (__builtin_amdgcn_readfirstlane((v[0] & v[1]) & (v[2] & v[3]))) break;
      __builtin_amdgcn_s_sleep(1);
    }
  }
  asm volatile("" ::: "memory");
  bf16x8 vf[9][2];
  vf[7][0] = vld(7, 0); vf[7][1] = vld(7, 1);
  SB0();
  u32x4 mk[2];
#pragma unroll
  for (int j = 5; j >= 0; --j) {
    vf[j + 1][0] = vld(j + 1, 0); vf[j + 1][1] = vld(j + 1, 1);
    if (j == 0) { vf[0][0] = vld(0, 0); vf[8][0] = vld(8, 0); vf[0][1] = vld(0, 1); vf[8][1] = vld(8, 1); }
    if (j == 1) { mk[0] = *(const LAS u32x4*)(lds + K2_MSK + lane * 16); mk[1] = *(const LAS u32x4*)(lds + K2_MSK + 1024 + lane * 16); }
    if (j > 0) sub(j - 1);
    SB0();
    ex8(j, 0); pv(vf[j + 2][0], pk[j + 2][0]); SB0();
    ex8(j, 1); pv(vf[j + 2][1], pk[j + 2][1]); SB0();
  }
  pv(vf[1][0], pk[1][0]); pv(vf[1][1], pk[1][1]);
#pragma unroll
  for (int ks = 0; ks < 2; ++ks) {
    const u32x4 p0 = pk[0][ks] & mk[ks], p8 = pk[0][ks] ^ p0;
    pv(vf[0][ks], p0); pv(vf[8][ks], p8);
  }
  TS();
  const float l = __uint_as_float(__builtin_amdgcn_permlane32_swap(__float_as_uint(o[8]), __float_as_uint(o[8]), false, false)[1]);
  const float il = __builtin_amdgcn_rcpf(l);
  const int tok = 256 * c + 32 * w + r32;
  const rsrc_t rw = mk_rsrc(ws, WS_END);
  const int ob = WS_O + (tok >> 6) * 20 * 4096 + ((tok >> 4) & 3) * 1024 + (tok & 15) * 16;
  const u32x4 w0 = {cvtpk(o[0] * il, o[1] * il), cvtpk(o[2] * il, o[3] * il), cvtpk(o[4] * il, o[5] * il), cvtpk(o[6] * il, o[7] * il)};
  st16(rw, ob + (h >> 1) * 4096 + (2 * (h & 1) + hi) * 256, w0);
  if (hi == 0) st8(rw, ob + (16 + (h >> 3)) * 4096 + ((h >> 1) & 3) * 256 + (h & 1) * 8, (u32x2){cvtpk(o[8] * il, o[9] * il), cvtpk(o[10] * il, o[11] * il)});
  TS();
  if (w == 7 || w == 0) { TS_END(w == 7 ? 1 : 5); }
}

extern "C" __global__ __launch_bounds__(768, 3) void k_attn(char* __restrict__ ws, const u16* __restrict__ wg, const u16* __restrict__ wu, const u16* __restrict__ wd) {
  extern __shared__ __attribute__((aligned(16))) char smem[];
  const int lane = threadIdx.x & 63, w = __builtin_amdgcn_readfirstlane(threadIdx.x >> 6);
  const int xcd = blockIdx.x & 7, idx = blockIdx.x >> 3, h = xcd * 4 + (idx >> 3), c = idx & 7;
  if (w < 4) attn_helper((ldsp)smem, ws, c, h, w, lane, wg, wu, wd);
  else attn_body((ldsp)smem, ws, c, h, w - 4, lane);
}

constexpr int N80_NS = 4, N80_STG = 18432, N80_CTL = N80_NS * N80_STG, N80_RES = N80_CTL + 20480, N80_LDS = N80_RES + 12288;

template <bool DOWN> DI void n80_body(const u16* __restrict__ x, char* __restrict__ ws, u16* __restrict__ out) {
  constexpr int NT = DOWN ? 24 : 10;
  extern __shared__ __attribute__((aligned(16))) char smem[];
  ldsp lds = (ldsp)smem;
  ldsp ctl = lds + N80_CTL;
  const int lane = threadIdx.x & 63, w = __builtin_amdgcn_readfirstlane(threadIdx.x >> 6);
  const unsigned lds0 = (unsigned)(uintptr_t)lds;
  const int xcd = blockIdx.x & 7, idx = blockIdx.x >> 3, mt = (xcd >> 1) * 8 + (idx >> 2), ct = (xcd & 1) * 4 + (idx & 3);
  const int odd = ct & 1, pb = (ct >> 1) * 5 + (odd ? 3 : 0), ps = (ct >> 1) * 5 + 2;
  if (w >= 4) {
    const int lw = w - 4;
    auto extra = [&] {
      set_m0(lds0 + N80_RES + lw * 3072);
      if (DOWN) {
        const char* hs = ws + WS_H + ((mt >> 1) * 160 + (mt & 1) * 2 + (lw >> 1)) * 1024;
        const unsigned vo = (lane >> 5) * 4096 + (((lane >> 4) & 1) * 32 + (lw & 1) * 16 + (lane & 15)) * 16 + 2048;
        dma<0>(hs + pb * 8192 - 2048, vo); dma<1024>(hs + (pb + 1) * 8192 - 3072, vo); dma<2048>(hs + ps * 8192 - 4096, vo);
      } else {
        const char* xs = (const char*)x + (size_t)(mt * 64 + lw * 16) * 1280 + ct * 160;
        sfor<0, 3>([&](auto ic) {
          constexpr int i = decltype(ic)::value;
          const int ch = i * 64 + lane < 160 ? i * 64 + lane : 159, r = ch / 10;
          dma<i * 1024>(xs - 2048, (unsigned)(r * 1280 + (ch - r * 10) * 16 + 2048 - i * 1024));
        });
      }
    };
    if (w < 6) linear_loader<4, NT, N80_NS, 8192, N80_STG, 0>(ws + (DOWN ? WS_ACT : WS_O) + mt * NT * 8192 + lw * 4096, lds0 + lw * 4096, lane * 16, extra, [] {});
    else linear_loader<5, NT, N80_NS, 10240, N80_STG, 0>(ws + (DOWN ? WS_WD : WS_WO) + ct * NT * 10240 + (w - 6) * 5120, lds0 + 8192 + (w - 6) * 5120, lane * 16, extra, [] {});
    return;
  }
  const int fr = lane & 15, g = lane >> 4, wk = w & 1;
  TS_DECL;
  const int tok = mt * 64 + w * 16 + fr;
  const int hb = WS_H + (((mt >> 1) * 40 + (g >> 1)) * 4 + (mt & 1) * 2 + (w >> 1)) * 1024 + ((g & 1) * 32 + (w & 1) * 16 + fr) * 16;
  f32x4 acc[2][5];
#pragma unroll
  for (int i = 0; i < 2; ++i)
#pragma unroll
    for (int j = 0; j < 5; ++j) { acc[i][j] = (f32x4){0.f, 0.f, 0.f, 0.f}; pin(acc[i][j]); }
  bf16x8 af[2][2], wf[2][5];
  const int aoff = wk * 4096 + w * 1024 + lane * 16, aoff1 = aoff ^ 1024, woff = 8192 + wk * 5120 + lane * 16;
  auto rd1 = [&](ldsp sb, int bs, int n) {
    if (n == 0) af[bs][0] = lds_ld16(sb + aoff);
    else if (n == 2) af[bs][1] = lds_ld16(sb + aoff1);
    else if (n < 7) { const int j = n == 1 ? 0 : n - 2; wf[bs][j] = lds_ld16(sb + woff + j * 1024); }
  };
  bar();
  TS();
#pragma unroll
  for (int n = 0; n < 7; ++n) rd1(lds, 0, n);
  SB0();
  sfor<0, NT>([&](auto tc) {
    constexpr int t = decltype(tc)::value, bs = t & 1;
    if (t + 1 < NT) { bar(); SB0(); }
#pragma unroll
    for (int n = 0; n < 10; ++n) {
      acc[n & 1][n >> 1] = mfma16(wf[bs][n >> 1], af[bs][n & 1], acc[n & 1][n >> 1]);
      if (t + 1 < NT) rd1(lds + ((t + 1) % N80_NS) * N80_STG, bs ^ 1, n);
      SB0();
    }
  });
  TS();
#pragma unroll
  for (int j = 0; j < 5; ++j) *(LAS f32x4*)(ctl + (w * 5 + j) * 1024 + lane * 16) = acc[1][j];
  bar();
#pragma unroll
  for (int j = 0; j < 5; ++j) acc[0][j] += *(const LAS f32x4*)(ctl + ((w ^ 1) * 5 + j) * 1024 + lane * 16);
  TS();
  u32x2 res[5];
  if (DOWN) {
    ldsp rp = lds + N80_RES + w * 3072 + lane * 16;
#pragma unroll
    for (int j = 0; j < 5; ++j) {
      const int e = odd ? (j == 0 ? 5 : j - 1) : j;
      res[j] = *(const LAS u32x2*)(rp + (e >> 1) * 1024 + (e & 1) * 8);
    }
  } else {
#pragma unroll
    for (int j = 0; j < 5; ++j) res[j] = *(const LAS u32x2*)(lds + N80_RES + w * 3072 + fr * 160 + j * 32 + g * 8);
  }
  u32x2 hv[5];
  float s = 0.f;
#pragma unroll
  for (int j = 0; j < 5; ++j) {
    hv[j] = (u32x2){cvtpk(acc[0][j][0] + bflo(res[j][0]), acc[0][j][1] + bfhi(res[j][0])), cvtpk(acc[0][j][2] + bflo(res[j][1]), acc[0][j][3] + bfhi(res[j][1]))};
    s += (bflo(hv[j][0]) + bfhi(hv[j][0])) + (bflo(hv[j][1]) + bfhi(hv[j][1]));
  }
  if (DOWN) {
    char* op = (char*)out + (size_t)tok * 1280 + ct * 160;
#pragma unroll
    for (int jp = 0; jp < 2; ++jp) {
      auto r0 = __builtin_amdgcn_permlane16_swap(hv[2 * jp][0], hv[2 * jp + 1][0], false, false);
      auto r1 = __builtin_amdgcn_permlane16_swap(hv[2 * jp][1], hv[2 * jp + 1][1], false, false);
      *(u32x4*)(op + jp * 64 + (g & 1) * 32 + (g >> 1) * 16) = (u32x4){r0[0], r1[0], r0[1], r1[1]};
    }
    *(u32x2*)(op + 128 + g * 8) = hv[4];
  } else {
    const rsrc_t rw = mk_rsrc(ws, WS_END);
    const u32x2 a0 = odd ? hv[1] : hv[0], a1 = odd ? hv[2] : hv[1], b0 = odd ? hv[3] : hv[2], b1 = odd ? hv[4] : hv[3], s1 = odd ? hv[0] : hv[4];
    st16<AUX3>(rw, hb + pb * 8192, (u32x4){a0[0], a0[1], a1[0], a1[1]});
    st16<AUX3>(rw, hb + (pb + 1) * 8192, (u32x4){b0[0], b0[1], b1[0], b1[1]});
    st8<AUX3>(rw, hb + ps * 8192 + odd * 8, s1);
    const float mean = xsum16_32(s) * (1.f / 80.f);
    float m2 = 0.f;
#pragma unroll
    for (int j = 0; j < 5; ++j) {
      const float d0 = bflo(hv[j][0]) - mean, d1 = bfhi(hv[j][0]) - mean, d2 = bflo(hv[j][1]) - mean, d3 = bfhi(hv[j][1]) - mean;
      m2 += (d0 * d0 + d1 * d1) + (d2 * d2 + d3 * d3);
    }
    m2 = xsum16_32(m2);
    if (g == 0) st8<AUX3>(rw, WS_ST + (tok * 8 + ct) * 8, (u32x2){__float_as_uint(mean), __float_as_uint(m2)});
  }
  TS();
  if (w == 0) { TS_END(DOWN ? 4 : 2); }
}

extern "C" __global__ __launch_bounds__(512, 2) void k_oproj(const u16* __restrict__ x, char* __restrict__ ws) { n80_body<false>(x, ws, nullptr); }

constexpr int K4_NS = 4, K4_STG = 20480, K4_NT = 20, K4_CTL = K4_NS * K4_STG, K4_SD = K4_CTL + 2048, K4_LDS = K4_SD + 8192 + 6144;

extern "C" __global__ __launch_bounds__(512, 2) void k_gateup(char* __restrict__ ws) {
  extern __shared__ __attribute__((aligned(16))) char smem[];
  ldsp lds = (ldsp)smem;
  ldsp ctl = lds + K4_CTL;
  const int lane = threadIdx.x & 63, w = __builtin_amdgcn_readfirstlane(threadIdx.x >> 6);
  const unsigned lds0 = (unsigned)(uintptr_t)lds;
  const int xcd = blockIdx.x & 7, idx = blockIdx.x >> 3, mt = (xcd >> 2) * 8 + (idx >> 2), ft = (xcd & 3) * 4 + (idx & 3);
  if (w >= 4) {
    auto extra = [&] { touch(ws + WS_WD + (xcd & 1) * 4 * 245760 + (idx * 4 + w - 4) * 7680, lane, 60, lds0 + K4_CTL); };
    if (w < 6) linear_loader<4, K4_NT, K4_NS, 8192, K4_STG, 1>(ws + WS_H + mt * K4_NT * 8192 + (w - 4) * 4096, lds0 + (w - 4) * 4096, lane * 16,
                                                               [&] { dma_run<4>(ws + WS_ST + mt * 8192 + (w - 4) * 4096, lds0 + K4_SD + (w - 4) * 4096, lane * 16); }, extra);
    else linear_loader<6, K4_NT, K4_NS, 12288, K4_STG, 1>(ws + WS_WGU + ft * K4_NT * 12288 + (w - 6) * 6144, lds0 + 8192 + (w - 6) * 6144, lane * 16,
                                                          [&] { dma_run<3>(ws + WS_CS + ft * 6144 + (w - 6) * 3072, lds0 + K4_SD + 8192 + (w - 6) * 3072, lane * 16); }, extra);
    return;
  }
  const int r32 = lane & 31, hi = lane >> 5, wm = w >> 1, wn = w & 1;
  TS_DECL;
  f32x16 acc[2][3];
#pragma unroll
  for (int i = 0; i < 2; ++i)
#pragma unroll
    for (int j = 0; j < 3; ++j) { acc[i][j] = (f32x16){}; pin(acc[i][j]); }
  bf16x8 af[2][2], wf[2][3];
  const int aoff = wm * 2048 + lane * 16, woff = 8192 + wn * 3072 + lane * 16;
  auto rda = [&](ldsp sb, int s, int i) { af[s][i] = lds_ld16(sb + aoff + s * 4096 + i * 1024); };
  auto rdw = [&](ldsp sb, int s, int j) { wf[s][j] = lds_ld16(sb + woff + s * 6144 + j * 1024); };
  auto mm = [&](int s, int i, int j) { acc[i][j] = mfma32(wf[s][j], af[s][i], acc[i][j]); };
  bar();
  const int tok = mt * 128 + wm * 64 + r32;
  f32x4 sv[2][2];
  float cv[8];
  const LAS float* cp = (const LAS float*)(lds + K4_SD + 8192) + w * 48 + (lane < 48 ? lane : 47);
  ldsp sp = lds + K4_SD + (wm * 64 + r32) * 64 + hi * 32;
  TS();
  rda(lds, 0, 0); rdw(lds, 0, 0); rda(lds, 0, 1); rdw(lds, 0, 1); rdw(lds, 0, 2); SB0();
  constexpr float GC0 = -2.f * 0.7978845608028654f * 1.4426950408889634f, GC1 = GC0 * 0.044715f;
  float ca[2], cb[2], nmu[2], var[2];
  sfor<0, K4_NT>([&](auto tc) {
    constexpr int t = decltype(tc)::value;
    constexpr bool more = t + 1 < K4_NT;
    ldsp sb = lds + (t % K4_NS) * K4_STG;
    ldsp nb = lds + ((t + 1) % K4_NS) * K4_STG;
    mm(0, 0, 0); rda(sb, 1, 0); rdw(sb, 1, 0); SB0();
    mm(0, 1, 0); rda(sb, 1, 1); rdw(sb, 1, 1); SB0();
    mm(0, 0, 1); rdw(sb, 1, 2); SB0();
    mm(0, 1, 1); SB0();
    mm(0, 0, 2); SB0();
    mm(0, 1, 2); SB0();
    if (more) { bar(); SB0(); }
    mm(1, 0, 0); if (more) { rda(nb, 0, 0); rdw(nb, 0, 0); } SB0();
    mm(1, 1, 0); if (more) { rda(nb, 0, 1); rdw(nb, 0, 1); } SB0();
    mm(1, 0, 1); if (more) rdw(nb, 0, 2); SB0();
    if constexpr (t >= 4 && t <= 7) { cv[2 * t - 8] = cp[(2 * t - 8) * 192]; cv[2 * t - 7] = cp[(2 * t - 7) * 192]; sv[(t - 4) >> 1][t & 1] = *(const LAS f32x4*)(sp + ((t - 4) >> 1) * 2048 + (t & 1) * 16); }
    if constexpr (t == 8) { if (lane < 48) ((LAS float*)(ctl + 1024))[w * 48 + lane] = ((cv[0] + cv[1]) + (cv[2] + cv[3])) + ((cv[4] + cv[5]) + (cv[6] + cv[7])); }
    if constexpr (t == 9 || t == 10) {
      constexpr int i = t - 9;
      const f32x4 s0 = sv[i][0], s1 = sv[i][1];
      const float mu = xsum32((s0[0] + s0[2]) + (s1[0] + s1[2])) * 0.125f;
      const float d0 = s0[0] - mu, d1 = s0[2] - mu, d2 = s1[0] - mu, d3 = s1[2] - mu;
      var[i] = xsum32((s0[1] + s0[3]) + (s1[1] + s1[3]) + 80.f * ((d0 * d0 + d1 * d1) + (d2 * d2 + d3 * d3))) * (1.f / D_) + LN_EPS;
      const float rstd = __builtin_amdgcn_rsqf(var[i]);
      ca[i] = GC0 * rstd; cb[i] = GC1 * rstd * rstd * rstd; nmu[i] = -mu;
    }
    mm(1, 1, 1);
    mm(1, 0, 2);
    mm(1, 1, 2);
    if constexpr (t == 9 || t == 10) { pin(ca[t - 9]); pin(cb[t - 9]); pin(nmu[t - 9]); pin(var[t - 9]); }
    SB0();
  });
  TS();
  const rsrc_t rw = mk_rsrc(ws, WS_END);
  const LAS float* csb = (const LAS float*)(ctl + 1024) + wn * 96 + 4 * hi;
  f32x4 cq[3][4];
#pragma unroll
  for (int j = 0; j < 3; ++j)
#pragma unroll
    for (int b = 0; b < 4; ++b) cq[j][b] = *(const LAS f32x4*)(csb + j * 32 + 8 * b);
#pragma unroll
  for (int i = 0; i < 2; ++i) {
    const int tk = tok + 32 * i;
    const int ob = WS_ACT + (((tk >> 6) * 48 + ft * 3) * 4 + ((tk >> 4) & 3)) * 1024 + (hi * 16 + (tk & 15)) * 16;
#pragma unroll
    for (int j = 0; j < 3; ++j) {
      unsigned pk[4];
#pragma unroll
      for (int r = 0; r < 8; r += 2) {
        const f32x2 gv = (f32x2){acc[i][j][r], acc[i][j][r + 1]} + nmu[i] * (f32x2){cq[j][r >> 2][r & 3], cq[j][r >> 2][(r & 3) + 1]};
        const f32x2 uv = (f32x2){acc[i][j][8 + r], acc[i][j][9 + r]} + nmu[i] * (f32x2){cq[j][2 + (r >> 2)][r & 3], cq[j][2 + (r >> 2)][(r & 3) + 1]};
        const f32x2 arg = gv * (gv * gv * cb[i] + ca[i]);
        const f32x2 ex = {__builtin_amdgcn_exp2f(arg[0]), __builtin_amdgcn_exp2f(arg[1])};
        const f32x2 den = ex * var[i] + var[i];
        const f32x2 rc = {__builtin_amdgcn_rcpf(den[0]), __builtin_amdgcn_rcpf(den[1])};
        const f32x2 a = gv * uv * rc;
        pk[r >> 1] = cvtpk(a[0], a[1]);
      }
      const int T = wn * 3 + j;
      st16<AUX4>(rw, ob + (T >> 1) * 4096 + (T & 1) * 512, (u32x4){pk[0], pk[1], pk[2], pk[3]});
    }
  }
  TS();
  if (w == 0) { TS_END(3); }
}

extern "C" __global__ __launch_bounds__(512, 2) void k_down(char* __restrict__ ws, u16* __restrict__ out) { n80_body<true>(nullptr, ws, out); }
extern "C" __global__ void k_text_end() {}

extern "C" void kernel_launch(void* const* d_in, const int* in_sizes, int n_in, void* d_out, int out_size, void* d_ws, size_t ws_size,
                              hipStream_t stream) {
  const u16* x = (const u16*)d_in[0];
  const u16* wqkv = (const u16*)d_in[1];
  const u16* wo = (const u16*)d_in[2];
  const u16* wg = (const u16*)d_in[4];
  const u16* wu = (const u16*)d_in[5];
  const u16* wd = (const u16*)d_in[6];
  char* ws = (char*)d_ws;
  u16* out = (u16*)d_out;
  (void)hipFuncSetAttribute((const void*)k_qkv, hipFuncAttributeMaxDynamicSharedMemorySize, K1_LDS);
  (void)hipFuncSetAttribute((const void*)k_attn, hipFuncAttributeMaxDynamicSharedMemorySize, K2_LDS);
  (void)hipFuncSetAttribute((const void*)k_oproj, hipFuncAttributeMaxDynamicSharedMemorySize, N80_LDS);
  (void)hipFuncSetAttribute((const void*)k_down, hipFuncAttributeMaxDynamicSharedMemorySize, N80_LDS);
  (void)hipFuncSetAttribute((const void*)k_gateup, hipFuncAttributeMaxDynamicSharedMemorySize, K4_LDS);
#define REP 1
  for (int r = 0; r < REP; ++r) k_qkv<<<256, 512, K1_LDS, stream>>>(x, wqkv, wo, ws, wg, wu, wd, out);
  for (int r = 0; r < REP; ++r) k_attn<<<256, 768, K2_LDS, stream>>>(ws, wg, wu, wd);
  for (int r = 0; r < REP; ++r) k_oproj<<<256, 512, N80_LDS, stream>>>(x, ws);
  for (int r = 0; r < REP; ++r) k_gateup<<<256, 512, K4_LDS, stream>>>(ws);
  for (int r = 0; r < REP; ++r) k_down<<<256, 512, N80_LDS, stream>>>(ws, out);
}
```

```cpp
#include <hip/hip_runtime.h>
#include <stdint.h>
#include <type_traits>

#define LAS __attribute__((address_space(3)))
#define DI __device__ __forceinline__
typedef unsigned short u16;
typedef short bf16x8 __attribute__((ext_vector_type(8)));
typedef short bf16x4 __attribute__((ext_vector_type(4)));
typedef __bf16 bf16x2_t __attribute__((ext_vector_type(2)));
typedef float f32x2 __attribute__((ext_vector_type(2)));
typedef float f32x4 __attribute__((ext_vector_type(4)));
typedef float f32x16 __attribute__((ext_vector_type(16)));
typedef unsigned u32x4 __attribute__((ext_vector_type(4)));
typedef unsigned u32x2 __attribute__((ext_vector_type(2)));
typedef LAS char* ldsp;
typedef __amdgpu_buffer_rsrc_t rsrc_t;

constexpr int S_ = 2048, D_ = 640, NH = 32, DF = 1536;
constexpr float LN_EPS = 1e-5f;
constexpr float QSCALE = 0.22360679774997896f * 1.4426950408889634f;
constexpr unsigned ONES2 = 0x3f803f80u;
#define AUX_ST 16

constexpr int WS_QK = 0;
constexpr int WS_O = WS_QK + 240 * S_ * 16;
constexpr int WS_H = WS_O + S_ * D_ * 2;
constexpr int WS_ACT = WS_H + S_ * D_ * 2;
constexpr int WS_ST = WS_ACT + S_ * DF * 2;
constexpr int WS_CS = WS_ST + S_ * 8 * 8;
constexpr int WS_WO = WS_CS + 16 * 8192;
constexpr int WS_WGU = WS_WO + D_ * D_ * 2;
constexpr int WS_WD = WS_WGU + D_ * DF * 4;
constexpr int WS_CNT = WS_WD + D_ * DF * 2;
constexpr int WS_END = WS_CNT + 512 * 256;

#define TS_DECL
#define TS()
#define TS_END(kid)

template <int I, int N, class F> DI void sfor(F&& f) {
  if constexpr (I < N) { f(std::integral_constant<int, I>{}); sfor<I + 1, N>(f); }
}
#define SB0() __builtin_amdgcn_sched_barrier(0)
DI unsigned cvtpk(float lo, float hi) { f32x2 v = {lo, hi}; return __builtin_bit_cast(unsigned, __builtin_convertvector(v, bf16x2_t)); }
DI float bflo(unsigned u) { return __uint_as_float(u << 16); }
DI float bfhi(unsigned u) { return __uint_as_float(u & 0xffff0000u); }
DI float dot2(unsigned a, unsigned b, float c) {
  return __builtin_amdgcn_fdot2_f32_bf16(__builtin_bit_cast(bf16x2_t, a), __builtin_bit_cast(bf16x2_t, b), c, false);
}
DI f32x4 mfma16(bf16x8 a, bf16x8 b, f32x4 c) { return __builtin_amdgcn_mfma_f32_16x16x32_bf16(a, b, c, 0, 0, 0); }
DI f32x16 mfma32(bf16x8 a, bf16x8 b, f32x16 c) { return __builtin_amdgcn_mfma_f32_32x32x16_bf16(a, b, c, 0, 0, 0); }
DI void set_m0(unsigned v) { asm volatile("s_mov_b32 m0, %0\n\ts_nop 0" ::"s"(v) : "memory"); }
template <int OFF> DI void dma(const void* sbase, unsigned voff) {
  asm volatile("global_load_lds_dwordx4 %0, %1 offset:%2" ::"v"(voff), "s"(sbase), "n"(OFF) : "memory");
}
template <int OFF> DI void dma_nt(const void* sbase, unsigned voff) {
  asm volatile("global_load_lds_dwordx4 %0, %1 offset:%2 nt" ::"v"(voff), "s"(sbase), "n"(OFF) : "memory");
}
template <int OFF> DI void gld16(u32x4& d, const void* sbase, unsigned voff) {
  asm volatile("global_load_dwordx4 %0, %1, %2 offset:%3" : "=v"(d) : "v"(voff), "s"(sbase), "n"(OFF) : "memory");
}
template <int N> DI void wait_vm() { asm volatile("s_waitcnt vmcnt(%0)" ::"n"(N) : "memory"); }
template <int N> DI void wait_vm_bar() { asm volatile("s_waitcnt vmcnt(%0) lgkmcnt(0)\n\ts_barrier" ::"n"(N) : "memory"); }
DI void bar() { asm volatile("s_waitcnt lgkmcnt(0)\n\ts_barrier" ::: "memory"); }
template <class T> DI void pin(T& v) { asm volatile("" : "+v"(v)); }
DI bf16x8 lds_ld16(ldsp p) { return *(const LAS bf16x8*)p; }
DI bf16x4 lds_tr(ldsp p) { return __builtin_amdgcn_ds_read_tr16_b64_v4i16((LAS bf16x4*)p); }
DI bf16x8 lds_tr2(ldsp p0, ldsp p1) {
  bf16x4 a = lds_tr(p0), b = lds_tr(p1);
  return __builtin_shufflevector(a, b, 0, 1, 2, 3, 4, 5, 6, 7);
}
DI float xsum32(float v) {
  auto r = __builtin_amdgcn_permlane32_swap(__float_as_uint(v), __float_as_uint(v), false, false);
  return __uint_as_float(r[0]) + __uint_as_float(r[1]);
}
DI float xsum16_32(float v) {
  auto r = __builtin_amdgcn_permlane16_swap(__float_as_uint(v), __float_as_uint(v), false, false);
  return xsum32(__uint_as_float(r[0]) + __uint_as_float(r[1]));
}
DI rsrc_t mk_rsrc(void* p, int bytes) { return __builtin_amdgcn_make_buffer_rsrc(p, 0, bytes, 0x00020000); }
template <int AUX = 16> DI void st16(rsrc_t r, int off, u32x4 v) { __builtin_amdgcn_raw_buffer_store_b128(v, r, off, 0, AUX); }
template <int AUX = 16> DI void st8(rsrc_t r, int off, u32x2 v) { __builtin_amdgcn_raw_buffer_store_b64(v, r, off, 0, AUX); }
#define LDAUX 16
#define WIN5 8
#define AUX3 0
#define AUX4 16

template <int P, int NT, int NS, int X, class F, class G0, class G> DI void loader_loop(F&& issue, G0&& early, G&& extra) {
  static_assert(NS >= 4, "three stages in flight + one being read");
  issue(0); issue(1);
  wait_vm_bar<P>();
  early();
  issue(2); issue(3);
  int slot = 4 % NS;
#pragma unroll 1
  for (int t = 1; t < NT - 3; ++t) {
    wait_vm_bar<2 * P>();
    issue(slot);
    slot = slot == NS - 1 ? 0 : slot + 1;
  }
  extra();
  wait_vm_bar<2 * P + X>(); wait_vm_bar<P + X>(); wait_vm_bar<X>();
}
DI void touch(const char* p, int lane, int nlines, unsigned lds_scratch) {
  set_m0(lds_scratch);
  asm volatile("global_load_lds_dword %0, %1" ::"v"((unsigned)((lane < nlines ? lane : nlines - 1) * 128)), "s"(p) : "memory");
}
#define CODE_ADDR(dst, sym) asm volatile("s_getpc_b64 s[40:41]\n\ts_add_u32 s40, s40, " #sym "@rel32@lo+4\n\ts_addc_u32 s41, s41, " #sym "@rel32@hi+12\n\ts_mov_b64 %0, s[40:41]" : "=s"(dst) :: "s40", "s41", "scc")
template <int P> DI void dma_run(const char* src, unsigned dst, unsigned lane16) {
  static_assert(P <= 8, "offset range");
  set_m0(dst + 4096);
  sfor<0, P>([&](auto ic) { dma<decltype(ic)::value * 1024 - 4096>(src + 4096, lane16); });
}
template <int P, int NT, int NS, int SSTR, int LSTR, int X, class G0, class G> DI void linear_loader(const char* src, unsigned dst, unsigned lane16, G0&& early, G&& extra) {
  loader_loop<P, NT, NS, X>([&](int slot) { dma_run<P>(src, dst + slot * LSTR, lane16); src += SSTR; }, early, extra);
}

template <int N> DI void dma_long(const char* src, unsigned dst, unsigned lane16) {
  sfor<0, N / 5>([&](auto ic) { constexpr int i = decltype(ic)::value; dma_run<5>(src + i * 5120, dst + i * 5120, lane16); });
}
DI unsigned lds_add(ldsp p, int lane) {
  unsigned old = 0;
  if (lane == 0) old = __hip_atomic_fetch_add((LAS unsigned*)p, 1u, __ATOMIC_RELAXED, __HIP_MEMORY_SCOPE_WORKGROUP);
  return __builtin_amdgcn_readfirstlane(old);
}
DI void lds_wait(ldsp p, unsigned want) {
  for (int sp = 0; sp < (1 << 22); ++sp) {
    if ((unsigned)__builtin_amdgcn_readfirstlane(*(volatile LAS unsigned*)p) >= want) break;
    __builtin_amdgcn_s_sleep(1);
  }
  asm volatile("" ::: "memory");
}
DI void cnt_add(char* ws, int idx) { __hip_atomic_fetch_add((unsigned*)(ws + WS_CNT) + idx * 64, 1u, __ATOMIC_RELAXED, __HIP_MEMORY_SCOPE_AGENT); }
DI void cnt_wait(char* ws, int idx, unsigned want) {
  unsigned* p = (unsigned*)(ws + WS_CNT) + idx * 64;
  for (int sp = 0; sp < (1 << 18); ++sp) {
    if ((unsigned)__builtin_amdgcn_readfirstlane(__hip_atomic_load(p, __ATOMIC_RELAXED, __HIP_MEMORY_SCOPE_AGENT)) >= want) break;
    __builtin_amdgcn_s_sleep(2);
  }
  __builtin_amdgcn_fence(__ATOMIC_ACQUIRE, "wavefront");
}
DI bf16x8 ld16_sc1(rsrc_t r, int voff, int soff) { return __builtin_bit_cast(bf16x8, __builtin_amdgcn_raw_buffer_load_b128(r, voff, soff, LDAUX)); }

template <int KIND> DI void pack_issue(const u16* wsrc, const u16* wsrc2, int ks, int cg, unsigned ldsdst, int lane) {
  constexpr int PITCH = KIND == 1 ? DF * 2 : D_ * 2;
  set_m0(ldsdst);
  sfor<0, 4>([&](auto ic) {
    constexpr int g = decltype(ic)::value;
    const int t = (lane >> 5) & 1, q = (lane >> 3) & 3;
    int row;
    if (KIND == 0) row = ks < 16 ? 20 * (2 * ks + (g >> 1)) + 4 * (g >> 1) + 8 * t + 4 * (g & 1) + q : 20 * (8 * (ks - 16) + 2 * g + t) + 16 * (1 - t) + q;
    else if (KIND == 1) row = 32 * (ks >> 1) + 16 * t + 4 * (2 * (ks & 1) + (g & 1)) + q;
    else row = 32 * ks + 16 * (g >> 1) + 8 * t + 4 * (g & 1) + q;
    dma_nt<g * 1024>((const char*)(KIND == 1 && g >= 2 ? wsrc2 : wsrc) + cg * 128 - 4096, (unsigned)(row * PITCH + (lane & 7) * 16 + 4096 - g * 1024));
  });
}
template <int KIND> DI void pack_finish(ldsp u, rsrc_t rw, int ks, int cg, int lane, LAS float* cs) {
  const int q = (lane & 15) >> 2, p = lane & 3;
  ldsp a = KIND == 1 ? u + ((lane >> 4) & 1) * 2048 + (8 * (lane >> 5) + q) * 128 + p * 8 : u + (8 * (lane >> 4) + q) * 128 + p * 8;
#pragma unroll
  for (int i = 0; i < 4; ++i) {
    const int nt = 4 * cg + i;
    const u32x4 f = __builtin_bit_cast(u32x4, lds_tr2(a + i * 32, a + i * 32 + 512));
    if (KIND == 1) {
      const int ft = nt / 6, j = nt - ft * 6;
      st16(rw, WS_WGU + ((ft * 40 + ks) * 6 + j) * 1024 + lane * 16, f);
      const unsigned sv = (lane & 15) == ((lane >> 4) & 1) ? ONES2 : 0u;
      const f32x4 c = mfma16(__builtin_bit_cast(bf16x8, (u32x4){sv, sv, sv, sv}), __builtin_bit_cast(bf16x8, f), (f32x4){0.f, 0.f, 0.f, 0.f});
      if (lane < 16) { cs[i * 32 + lane] = c[0]; cs[i * 32 + 16 + lane] = c[1]; }
    } else {
      const int ct = nt / 5, j = nt - ct * 5;
      st16(rw, (KIND == 0 ? WS_WO : WS_WD) + ((ct * (KIND == 0 ? 20 : 48) + ks) * 5 + j) * 1024 + lane * 16, f);
    }
  }
}

#define STATMODE 1
constexpr int NWD_A = 352;
#define WD0 8
#define WD1 24
constexpr int K1_NS = 4, K1_STG = 32768, K1_NT = 10, K1_CTL = K1_NS * K1_STG, K1_LDS = K1_CTL + 4096;

DI void qkv_compute(ldsp lds, char* __restrict__ ws, int mt, int nt, int w, int lane) {
  const int wm = w >> 1, wn = w & 1, r32 = lane & 31, hi = lane >> 5, q = (lane & 15) >> 2, p = lane & 3, blk = (lane >> 4) & 1;
  TS_DECL;
  ldsp ctl = lds + K1_CTL;
  int aoff[4], woff[2];
#pragma unroll
  for (int s = 0; s < 4; ++s) aoff[s] = (wm * 64 + r32) * 128 + (((2 * s + hi) ^ ((r32 >> 1) & 7)) * 16);
#pragma unroll
  for (int j = 0; j < 2; ++j) woff[j] = 16384 + (8 * hi + q) * 256 + (((wn * 2 + (j ^ wm)) ^ q) * 64) + blk * 32 + p * 8;
  f32x16 acc[2][2];
#pragma unroll
  for (int j = 0; j < 2; ++j)
#pragma unroll
    for (int i = 0; i < 2; ++i) { acc[j][i] = (f32x16){}; pin(acc[j][i]); }
  f32x4 cs = {};
  const unsigned sv = (lane & 15) == blk ? ONES2 : 0u;
  const bf16x8 sel = __builtin_bit_cast(bf16x8, (u32x4){sv, sv, sv, sv});
  bf16x8 xf[2][2], wf[2][2];
  auto rdx = [&](ldsp sb, int s, int i) { xf[s & 1][i] = lds_ld16(sb + aoff[s] + i * 4096); };
  auto rdw = [&](ldsp sb, int s, int j) { wf[s & 1][j] = lds_tr2(sb + woff[j] + s * 4096, sb + woff[j] + s * 4096 + 1024); };
  auto mm = [&](int s, int j, int i) { acc[j][i] = mfma32(wf[s & 1][j], xf[s & 1][i], acc[j][i]); };
  auto step = [&](ldsp nb, int s, int sn, bool rd) {
    cs = mfma16(sel, wf[s & 1][0], cs);
    mm(s, 0, 0); if (rd) { rdx(nb, sn, 0); rdw(nb, sn, 0); } SB0();
    mm(s, 0, 1); if (rd) { rdx(nb, sn, 1); rdw(nb, sn, 1); } SB0();
    mm(s, 1, 0); SB0();
    mm(s, 1, 1); SB0();
  };
  bar();
  TS();
  ldsp sb = lds;
  rdx(sb, 0, 0); rdw(sb, 0, 0); rdx(sb, 0, 1); rdw(sb, 0, 1); SB0();
  int slot = 0;
#pragma unroll 1
  for (int t = 0; t < K1_NT - 1; ++t) {
    step(sb, 0, 1, true); step(sb, 1, 2, true); step(sb, 2, 3, true);
    slot = slot == K1_NS - 1 ? 0 : slot + 1;
    sb = lds + slot * K1_STG;
    bar(); SB0();
    step(sb, 3, 0, true);
  }
  step(sb, 0, 1, true); step(sb, 1, 2, true); step(sb, 2, 3, true);
  const LAS f32x2* rst = (const LAS f32x2*)(ctl + 1024);
  LAS float* cst = (LAS float*)(ctl + 2048);
  cs = mfma16(sel, wf[1][0], cs);
  mm(3, 0, 0); mm(3, 0, 1); SB0();
  if (lane < 16) { cst[wn * 64 + wm * 32 + lane] = cs[0]; cst[wn * 64 + wm * 32 + 16 + lane] = cs[1]; }
  mm(3, 1, 0); mm(3, 1, 1); SB0();
  TS();
  bar();
  TS();
  const rsrc_t rw = mk_rsrc(ws, WS_END);
  const float sc = nt < 5 ? QSCALE : 1.f;
  f32x4 c4[2][4];
#pragma unroll
  for (int j = 0; j < 2; ++j)
#pragma unroll
    for (int b = 0; b < 4; ++b) c4[j][b] = *(const LAS f32x4*)(cst + wn * 64 + (j ^ wm) * 32 + 8 * b + 4 * hi);
#pragma unroll
  for (int i = 0; i < 2; ++i) {
    const int ml = wm * 64 + i * 32 + r32;
    const f32x2 st = rst[ml];
    const float a = st.y * sc, nm = -st.x * a;
    const int ob = WS_QK + ((nt * 16 + wn * 8 + hi) * S_ + mt * 128 + ml) * 16;
#pragma unroll
    for (int j = 0; j < 2; ++j)
#pragma unroll
      for (int bp = 0; bp < 2; ++bp) {
        unsigned pk[2][2];
#pragma unroll
        for (int e = 0; e < 2; ++e) {
          const int b = 2 * bp + e;
          const f32x2 v0 = (f32x2){acc[j][i][4 * b], acc[j][i][4 * b + 1]} * a + nm * (f32x2){c4[j][b][0], c4[j][b][1]};
          const f32x2 v1 = (f32x2){acc[j][i][4 * b + 2], acc[j][i][4 * b + 3]} * a + nm * (f32x2){c4[j][b][2], c4[j][b][3]};
          pk[e][0] = cvtpk(v0[0], v0[1]); pk[e][1] = cvtpk(v1[0], v1[1]);
        }
        auto r0 = __builtin_amdgcn_permlane32_swap(pk[0][0], pk[1][0], false, false);
        auto r1 = __builtin_amdgcn_permlane32_swap(pk[0][1], pk[1][1], false, false);
        st16(rw, ob + ((j ^ wm) * 4 + bp * 2) * S_ * 16, (u32x4){r0[0], r1[0], r0[1], r1[1]});
      }
  }
  TS();
  if (w == 0) { TS_END(0); }
}

DI void qkv_stats(ldsp lds, int sw, int lane) {
  const int r32 = lane & 31, hi = lane >> 5;
  const int off = (sw * 32 + r32) * 128, x = (r32 >> 1) & 7;
  f32x2 s0 = {0.f, 0.f}, s1 = {0.f, 0.f}, q0 = {0.f, 0.f}, q1 = {0.f, 0.f};
  int slot = 0;
#pragma unroll 1
  for (int t = 0; t < K1_NT; ++t) {
    bar();
    ldsp sb = lds + slot * K1_STG + off;
    slot = slot == K1_NS - 1 ? 0 : slot + 1;
    u32x4 v[4];
#pragma unroll
    for (int c = 0; c < 4; ++c) v[c] = *(const LAS u32x4*)(sb + (((2 * c + hi) ^ x) * 16));
#pragma unroll
    for (int c = 0; c < 4; ++c)
#pragma unroll
      for (int e = 0; e < 4; e += 2) {
        const float a0 = bflo(v[c][e]), a1 = bfhi(v[c][e]), b0 = bflo(v[c][e + 1]), b1 = bfhi(v[c][e + 1]);
        asm("v_add_f32 %0, %0, %1" : "+v"(s0.x) : "v"(a0)); asm("v_add_f32 %0, %0, %1" : "+v"(s0.y) : "v"(a1));
        asm("v_add_f32 %0, %0, %1" : "+v"(s1.x) : "v"(b0)); asm("v_add_f32 %0, %0, %1" : "+v"(s1.y) : "v"(b1));
        asm("v_fmac_f32 %0, %1, %1" : "+v"(q0.x) : "v"(a0)); asm("v_fmac_f32 %0, %1, %1" : "+v"(q0.y) : "v"(a1));
        asm("v_fmac_f32 %0, %1, %1" : "+v"(q1.x) : "v"(b0)); asm("v_fmac_f32 %0, %1, %1" : "+v"(q1.y) : "v"(b1));
      }
  }
  const float mu = xsum32((s0[0] + s0[1]) + (s1[0] + s1[1])) * (1.f / D_);
  const float var = fmaxf(xsum32((q0[0] + q0[1]) + (q1[0] + q1[1])) * (1.f / D_) - mu * mu, 0.f);
  if (hi == 0) ((LAS f32x2*)(lds + K1_CTL + 1024))[sw * 32 + r32] = (f32x2){mu, __builtin_amdgcn_rsqf(var + LN_EPS)};
  asm volatile("s_waitcnt lgkmcnt(0)" ::: "memory");
}

DI void qkv_loader(ldsp lds, const u16* __restrict__ x, const u16* __restrict__ wqkv, int mt, int nt, int lw, int lane) {
  const bool isx = lw < 2;
  const int half = lw & 1;
  unsigned voff[8];
#pragma unroll
  for (int i = 0; i < 8; ++i) {
    const int r = (half * 8 + i) * 8 + (lane >> 3), c = (lane & 7) ^ ((r >> 1) & 7);
    const int k = (half * 8 + i) * 4 + (lane >> 4), cp = lane & 15;
    voff[i] = (isx ? r * 1280 + c * 16 : k * 3840 + (cp ^ ((k & 3) << 2)) * 16) + 8192 - i * 1024;
  }
  const char* src = (isx ? (const char*)x + (size_t)mt * 128 * 1280 : (const char*)wqkv + nt * 256) - 4096;
  const int sstr = isx ? 128 : 64 * 3840;
  const unsigned dst = (unsigned)(uintptr_t)lds + lw * 8192 + 4096;
  loader_loop<8, K1_NT, K1_NS, 0>([&](int slot) {
    set_m0(dst + slot * K1_STG);
    sfor<0, 8>([&](auto ic) { constexpr int i = decltype(ic)::value; dma<i * 1024 - 4096>(src, voff[i]); });
    src += sstr;
  }, [] {}, [] {});
}

extern "C" __global__ __launch_bounds__(768, 3) void k_qkv(const u16* __restrict__ x, const u16* __restrict__ wqkv, const u16* __restrict__ wo, const u16* __restrict__ wd, char* __restrict__ ws) {
  extern __shared__ __attribute__((aligned(16))) char smem[];
  ldsp lds = (ldsp)smem;
  const int lane = threadIdx.x & 63, w = __builtin_amdgcn_readfirstlane(threadIdx.x >> 6);
  const int xcd = blockIdx.x & 7, idx = blockIdx.x >> 3;
  const int nt = (xcd & 1) * 8 + (idx >> 2), mt = (xcd >> 1) * 4 + (idx & 3);
  if (w < 4 && nt < 15) { qkv_loader(lds, x, wqkv, mt, nt, w, lane); return; }
  if (w == 11 && lane < 2) ((unsigned*)(ws + WS_CNT))[(blockIdx.x * 2 + lane) * 64] = 0u;
  if (w == 10) {
    const char *c0, *c1;
    CODE_ADDR(c0, k_qkv); CODE_ADDR(c1, k_text_end);
    const unsigned lim = c1 > c0 ? (unsigned)(c1 - c0) : 0u, off = (idx * 16 + (lane & 15)) * 128;
    set_m0((unsigned)(uintptr_t)lds + K1_CTL + 3072 + 512);
    asm volatile("global_load_lds_dword %0, %1" ::"v"(off < lim ? off : lim), "s"(c0) : "memory");
  }
  if (w == 8 || w == 9) {
    const int id = blockIdx.x * 64 + lane, u = w - 8;
    const bool isx = blockIdx.x < 128;
    const int i2 = id - 8192 < 7680 ? id - 8192 : 7679;
    const unsigned off = isx ? (id >> 2) * 1280 + 256 + u * 512 + (id & 3) * 128 : (128 + u * 256) * 3840 + i2 * 128;
    set_m0((unsigned)(uintptr_t)lds + K1_CTL + 3072 + u * 256);
    const char* src = isx ? (const char*)x : (const char*)wqkv;
    asm volatile("" : "+s"(src));
    if (u == 0) __builtin_amdgcn_s_sleep(WD0); else __builtin_amdgcn_s_sleep(WD1);
    asm volatile("global_load_lds_dword %0, %1" ::"v"(off), "s"(src) : "memory");
  }
  if (nt == 15) {
    const rsrc_t rw = mk_rsrc(ws, WS_END);
    constexpr int NU = (200 + NWD_A + 191) / 192;
    auto iss = [&](int r) {
      const int u = min(mt * 12 + w + r * 192, 200 + NWD_A - 1), v = u - 200;
      if (u < 200) pack_issue<0>(wo, wo, u / 10, u % 10, (unsigned)(uintptr_t)lds + (w * 2 + (r & 1)) * 4096, lane);
      else pack_issue<2>(wd, wd, v / 10, v % 10, (unsigned)(uintptr_t)lds + (w * 2 + (r & 1)) * 4096, lane);
    };
    iss(0); iss(1);
#pragma unroll 1
    for (int r = 0; r < NU; ++r) {
      if (r == 0) wait_vm<4>(); else if (r + 1 < NU) wait_vm<8>(); else wait_vm<4>();
      const int u = min(mt * 12 + w + r * 192, 200 + NWD_A - 1), v = u - 200;
      if (u < 200) pack_finish<0>(lds + (w * 2 + (r & 1)) * 4096, rw, u / 10, u % 10, lane, nullptr);
      else pack_finish<2>(lds + (w * 2 + (r & 1)) * 4096, rw, v / 10, v % 10, lane, nullptr);
      asm volatile("s_waitcnt lgkmcnt(0)" ::: "memory");
      if (r + 2 < NU) iss(r + 2);
    }
    return;
  }
  if (w >= 8) qkv_stats(lds, w - 8, lane);
  else qkv_compute(lds, ws, mt, nt, w - 4, lane);
}

#define VLEAD 3
constexpr int K2_K = 102400, K2_V = K2_K + 24576, K2_VP = 8192 + 64, K2_CS = K2_V + 4 * K2_VP, K2_MSK = K2_CS + 2560, K2_FL = K2_MSK + 832, K2_LDS = K2_FL + 64 + 256;
static_assert(K2_LDS <= 163840 && K2_FL % 16 == 0, "LDS map");

DI void n80_epilogue_h(f32x4 (&acc)[5], rsrc_t rw, int mt, int ct, int w, int lane) {
  const int fr = lane & 15, g = lane >> 4, tok = mt * 64 + w * 16 + fr;
  const int odd = ct & 1, pb = (ct >> 1) * 5 + (odd ? 3 : 0), ps = (ct >> 1) * 5 + 2;
  const int hb = WS_H + (((mt >> 1) * 40 + (g >> 1)) * 4 + (mt & 1) * 2 + (w >> 1)) * 1024 + ((g & 1) * 32 + (w & 1) * 16 + fr) * 16;
  u32x2 hv[5];
  float s = 0.f;
#pragma unroll
  for (int j = 0; j < 5; ++j) hv[j] = (u32x2){cvtpk(acc[j][0], acc[j][1]), cvtpk(acc[j][2], acc[j][3])};
  const u32x2 a0 = odd ? hv[1] : hv[0], a1 = odd ? hv[2] : hv[1], b0 = odd ? hv[3] : hv[2], b1 = odd ? hv[4] : hv[3], s1 = odd ? hv[0] : hv[4];
  st16<AUX3>(rw, hb + pb * 8192, (u32x4){a0[0], a0[1], a1[0], a1[1]});
  st16<AUX3>(rw, hb + (pb + 1) * 8192, (u32x4){b0[0], b0[1], b1[0], b1[1]});
  st8<AUX3>(rw, hb + ps * 8192 + odd * 8, s1);
  f32x4 v[5];
#pragma unroll
  for (int j = 0; j < 5; ++j) { v[j] = (f32x4){bflo(hv[j][0]), bfhi(hv[j][0]), bflo(hv[j][1]), bfhi(hv[j][1])}; s += (v[j][0] + v[j][1]) + (v[j][2] + v[j][3]); }
  const float mean = xsum16_32(s) * (1.f / 80.f);
  float m2 = 0.f;
#pragma unroll
  for (int j = 0; j < 5; ++j) { const f32x4 d = v[j] - mean; m2 += (d[0] * d[0] + d[1] * d[1]) + (d[2] * d[2] + d[3] * d[3]); }
  m2 = xsum16_32(m2);
  if (g == 0) st8<AUX3>(rw, WS_ST + (tok * 8 + ct) * 8, (u32x2){__float_as_uint(mean), __float_as_uint(m2)});
}

DI void attn_helper(ldsp lds, char* __restrict__ ws, int c, int h, int hw, int lane, const u16* __restrict__ x, const u16* __restrict__ wg, const u16* __restrict__ wu, const u16* __restrict__ wd) {
  const unsigned lds0 = (unsigned)(uintptr_t)lds;
  const int pm = 5 * (h >> 1) + 3 * (h & 1), pt = 5 * (h >> 1) + 2;
  const bool EDGE = c == 0;
  const int tok0 = 256 * c - 256 + 128 * hw + ((EDGE && hw < 2) ? 256 : 0);
  const char* kb = ws + WS_QK + (80 * S_ + tok0) * 16;
  {
    set_m0(lds0 + K2_K + hw * 2048); dma<0>(kb + pm * S_ * 16, lane * 16); dma<1024>(kb + pm * S_ * 16, lane * 16);
    set_m0(lds0 + K2_K + 8192 + hw * 2048); dma<0>(kb + (pm + 1) * S_ * 16, lane * 16); dma<1024>(kb + (pm + 1) * S_ * 16, lane * 16);
    set_m0(lds0 + K2_K + 16384 + hw * 2048); dma<0>(kb + pt * S_ * 16, lane * 16); dma<1024>(kb + pt * S_ * 16, lane * 16);
  }
  TS_DECL;
  const int b = blockIdx.x, xcd = b & 7, idx = b >> 3;
  if (hw == 0 && lane < 8) *(LAS unsigned*)(lds + K2_FL + lane * 4) = 0u;
  {
    const unsigned tm = EDGE && hw < 2 ? 0xff80u : 0u;
    *(LAS u32x4*)(lds + K2_V + 3 * K2_VP + hw * 2048 + lane * 16) = (u32x4){tm, 0u, ONES2, ONES2};
    *(LAS u32x4*)(lds + K2_V + 3 * K2_VP + hw * 2048 + 1024 + lane * 16) = (u32x4){tm, 0u, ONES2, ONES2};
  }
  if (hw == 1 && lane < 52) {
    const int d = lane - 20;
    u32x4 mk;
#pragma unroll
    for (int e = 0; e < 4; ++e) { const int k0 = ((2 * e) & 3) + 8 * (e >> 1); mk[e] = (k0 > d ? 0xffffu : 0u) | (k0 + 1 > d ? 0xffff0000u : 0u); }
    *(LAS u32x4*)(lds + K2_MSK + lane * 16) = mk;
  }
  wait_vm<VLEAD>();
  {
    const char* vb = kb + 80 * S_ * 16;
    set_m0(lds0 + K2_V + hw * 2048); dma<0>(vb + pm * S_ * 16, lane * 16); dma<1024>(vb + pm * S_ * 16, lane * 16);
    set_m0(lds0 + K2_V + K2_VP + hw * 2048); dma<0>(vb + (pm + 1) * S_ * 16, lane * 16); dma<1024>(vb + (pm + 1) * S_ * 16, lane * 16);
    set_m0(lds0 + K2_V + 2 * K2_VP + hw * 2048); dma<0>(vb + pt * S_ * 16, lane * 16); dma<1024>(vb + pt * S_ * 16, lane * 16);
  }
  const rsrc_t rw = mk_rsrc(ws, WS_END);
  const int mt = (xcd >> 1) * 8 + (idx >> 2), ct = (xcd & 1) * 4 + (idx & 3);
  const int q0 = hw * 25600;
  const int bb = b, pcg = bb % 24, pks = (bb / 24) * 4 + hw, v0 = NWD_A + (b - 240) * 8 + hw * 2;
  wait_vm_bar<6>();
  if (b < 240) { pack_issue<1>(wg, wu, pks, pcg, lds0 + q0, lane); wait_vm<4>(); }
  else { pack_issue<2>(wd, wd, v0 / 10, v0 % 10, lds0 + q0, lane); pack_issue<2>(wd, wd, v0 / 10, v0 % 10 + 1, lds0 + q0 + 4096, lane); wait_vm<8>(); }
  if (lane == 0) *(volatile LAS unsigned*)(lds + K2_FL + 16 + hw * 4) = 1u;
  TS();
  u32x4 xr[2]; u32x2 xs;
  {
    const char* xp = (const char*)x + (size_t)(mt * 64 + hw * 16) * 1280 + ct * 160;
    const int g = lane >> 4;
    const unsigned o16 = (lane & 15) * 1280 + (g & 1) * 32 + (g >> 1) * 16, o8 = (lane & 15) * 1280 + g * 8;
    asm volatile("global_load_dwordx4 %0, %1, %2" : "=v"(xr[0]) : "v"(o16), "s"(xp) : "memory");
    asm volatile("global_load_dwordx4 %0, %1, %2 offset:64" : "=v"(xr[1]) : "v"(o16), "s"(xp) : "memory");
    asm volatile("global_load_dwordx2 %0, %1, %2 offset:128" : "=v"(xs) : "v"(o8), "s"(xp) : "memory");
  }
  dma_long<15>(ws + WS_WO + ct * 102400 + q0 + 10240, lds0 + q0 + 10240, lane * 16);
  wait_vm<15>();
  pin(xr[0]); pin(xr[1]); pin(xs);
  TS();
  if (b >= 240) {
    pack_finish<2>(lds + q0, rw, v0 / 10, v0 % 10, lane, nullptr); pack_finish<2>(lds + q0 + 4096, rw, v0 / 10, v0 % 10 + 1, lane, nullptr);
    asm volatile("s_waitcnt lgkmcnt(0)" ::: "memory");
  } else {
    LAS float* ct4 = (LAS float*)(lds + K2_CS);
    pack_finish<1>(lds + q0, rw, pks, pcg, lane, ct4 + hw * 128);
    asm volatile("s_waitcnt lgkmcnt(0)" ::: "memory");
    if (lds_add(lds + K2_FL, lane) == 3) {
      asm volatile("" ::: "memory");
      const LAS f32x2* c2 = (const LAS f32x2*)ct4 + lane;
      const f32x2 a = (c2[0] + c2[64]) + (c2[128] + c2[192]);
      const int e = pcg * 128 + lane * 2, ft = e / 192;
      st8(rw, WS_CS + ft * 8192 + ((bb / 24) * 192 + e - ft * 192) * 4, __builtin_bit_cast(u32x2, a));
    }
  }
  TS();
  dma_long<10>(ws + WS_WO + ct * 102400 + q0, lds0 + q0, lane * 16);
  if (hw == 3) {
    const char *c0, *c1;
    CODE_ADDR(c0, k_mlp); CODE_ADDR(c1, k_text_end);
    const unsigned lim = c1 > c0 ? (unsigned)(c1 - c0) : 0u, off = (idx * 8 + (lane & 7)) * 128;
    set_m0(lds0 + K2_FL + 64);
    asm volatile("global_load_lds_dword %0, %1" ::"v"(off < lim ? off : lim), "s"(c0) : "memory");
  }
  f32x4 acc[5];
#pragma unroll
  for (int jp = 0; jp < 2; ++jp) {
    auto r0 = __builtin_amdgcn_permlane16_swap(xr[jp][0], xr[jp][2], false, false);
    auto r1 = __builtin_amdgcn_permlane16_swap(xr[jp][1], xr[jp][3], false, false);
    acc[2 * jp] = (f32x4){bflo(r0[0]), bfhi(r0[0]), bflo(r1[0]), bfhi(r1[0])};
    acc[2 * jp + 1] = (f32x4){bflo(r0[1]), bfhi(r0[1]), bflo(r1[1]), bfhi(r1[1])};
  }
  acc[4] = (f32x4){bflo(xs[0]), bfhi(xs[0]), bflo(xs[1]), bfhi(xs[1])};
#pragma unroll
  for (int j = 0; j < 5; ++j) pin(acc[j]);
  wait_vm<0>();
  lds_add(lds + K2_FL + 8, lane);
  lds_wait(lds + K2_FL + 8, 4);
  TS();
  if (hw == 0) { cnt_wait(ws, mt * 8 + ct, 32); if (lane == 0) *(volatile LAS unsigned*)(lds + K2_FL + 12) = 1u; }
  else lds_wait(lds + K2_FL + 12, 1);
  TS();
  bf16x8 af[10][2], wf[2][5];
  f32x4 ac1[5];
#pragma unroll
  for (int j = 0; j < 5; ++j) ac1[j] = (f32x4){0.f, 0.f, 0.f, 0.f};
  const int ao = WS_O + mt * 81920 + (hw & 1) * 4096 + hw * 1024 + lane * 16;
#pragma unroll
  for (int t = 0; t < 10; ++t) { af[t][0] = ld16_sc1(rw, ao, t * 8192); af[t][1] = ld16_sc1(rw, ao ^ 1024, t * 8192); }
  SB0();
  ldsp wp = lds + (hw & 1) * 5120 + lane * 16;
#pragma unroll
  for (int j = 0; j < 5; ++j) wf[0][j] = lds_ld16(wp + j * 1024);
#pragma unroll
  for (int t = 0; t < 10; ++t) {
#pragma unroll
    for (int j = 0; j < 5; ++j) {
      if (t + 1 < 10) wf[(t + 1) & 1][j] = lds_ld16(wp + (t + 1) * 10240 + j * 1024);
      ac1[j] = mfma16(wf[t & 1][j], af[t][1], ac1[j]);
    }
#pragma unroll
    for (int j = 0; j < 5; ++j) acc[j] = mfma16(wf[t & 1][j], af[t][0], acc[j]);
    SB0();
  }
  lds_wait(lds + K2_FL + 4, 8);
#pragma unroll
  for (int j = 0; j < 5; ++j) *(LAS f32x4*)(lds + K2_K + (hw * 5 + j) * 1024 + lane * 16) = ac1[j];
  bar();
#pragma unroll
  for (int j = 0; j < 5; ++j) acc[j] += *(const LAS f32x4*)(lds + K2_K + ((hw ^ 1) * 5 + j) * 1024 + lane * 16);
  TS();
  n80_epilogue_h(acc, rw, mt, ct, hw, lane);
  TS();
  if (hw == 1) { TS_END(2); }
}

DI void attn_body(ldsp lds, char* __restrict__ ws, int c, int h, int w, int lane) {
  const int r32 = lane & 31, hi = lane >> 5;
  TS_DECL;
  const int pm = 5 * (h >> 1) + 3 * (h & 1), pt = 5 * (h >> 1) + 2;
  const int dq = r32 - 4 * hi;
  const float NEG = -__builtin_inff();
  f32x16 s[8], s8;
#pragma unroll
  for (int r = 0; r < 16; ++r) {
    const bool le = (r & 3) + 8 * (r >> 2) <= dq;
    s8[r] = le ? 0.f : NEG;
    s[0][r] = le ? NEG : 0.f;
  }
  pin(s[0]); pin(s8);
  SB0();
  const char* qb = ws + WS_QK + (256 * c + 32 * w) * 16;
  const bf16x8 qm = *(const bf16x8*)(qb + ((pm + hi) * S_ + r32) * 16);
  u32x2 qtr = *(const u32x2*)(qb + (pt * S_ + r32) * 16 + (h & 1) * 8);
  if (hi) qtr = (u32x2){0x3f80u, 0u};
  bf16x4 qt = __builtin_bit_cast(bf16x4, qtr);
  pin(qt);
  TS();
  bar();
  TS();
  ldsp kp = lds + K2_K + hi * 8192 + (w * 32 + r32) * 16;
  ldsp ktp = lds + (hi ? K2_V + 3 * K2_VP + w * 512 : K2_K + 16384 + (w * 32 + r32) * 16 + (h & 1) * 8);
  const int vq = (lane & 15) >> 2, vpp = lane & 3, vblk = (lane >> 4) & 1;
  ldsp vp = lds + K2_V + (w * 32 + 4 * hi + vq) * 16 + (vblk == 0 ? (vpp >> 1) * K2_VP + (vpp & 1) * 8 : vpp == 0 ? 2 * K2_VP + (h & 1) * 8 : 3 * K2_VP + 8);
  bf16x8 km[9]; bf16x4 kt[9];
#pragma unroll
  for (int jj = 0; jj < 9; ++jj) { const int j = jj == 0 ? 8 : jj - 1; km[j] = lds_ld16(kp + j * 512); kt[j] = *(const LAS bf16x4*)(ktp + j * 512); }
  SB0();
  auto tail = [&](int j, f32x16 cin) { return __builtin_amdgcn_mfma_f32_32x32x8bf16_1k(kt[j], qt, cin, 0, 0, 0); };
  float mx[4];
  auto maxh = [&](int j, int r0) {
#pragma unroll
    for (int e = 0; e < 4; ++e) mx[e] = __builtin_elementwise_maximum(__builtin_elementwise_maximum(mx[e], s[j][r0 + 2 * e]), s[j][r0 + 2 * e + 1]);
  };
  s8 = mfma32(km[8], qm, s8); s[0] = mfma32(km[0], qm, s[0]); s8 = tail(8, s8);
  s[1] = mfma32(km[1], qm, (f32x16){}); s[0] = tail(0, s[0]);
  s[2] = mfma32(km[2], qm, (f32x16){}); s[1] = tail(1, s[1]); SB0();
  s[3] = mfma32(km[3], qm, (f32x16){});
#pragma unroll
  for (int r = 0; r < 8; ++r) s[0][r] = __builtin_elementwise_maximum(s[0][r], s8[r]);
  SB0();
  s[2] = tail(2, s[2]);
#pragma unroll
  for (int r = 8; r < 16; ++r) s[0][r] = __builtin_elementwise_maximum(s[0][r], s8[r]);
  pin(s[0]);
  SB0();
#pragma unroll
  for (int e = 0; e < 4; ++e) mx[e] = s[0][e];
  s[4] = mfma32(km[4], qm, (f32x16){}); maxh(0, 4); mx[0] = __builtin_elementwise_maximum(__builtin_elementwise_maximum(mx[0], s[0][12]), s[0][13]);
  mx[1] = __builtin_elementwise_maximum(__builtin_elementwise_maximum(mx[1], s[0][14]), s[0][15]); SB0();
  s[3] = tail(3, s[3]); maxh(1, 0); SB0();
  s[5] = mfma32(km[5], qm, (f32x16){}); maxh(1, 8); SB0();
  s[4] = tail(4, s[4]); maxh(2, 0); SB0();
  s[6] = mfma32(km[6], qm, (f32x16){}); maxh(2, 8); SB0();
  s[5] = tail(5, s[5]); maxh(3, 0); SB0();
  s[7] = mfma32(km[7], qm, (f32x16){}); maxh(3, 8); SB0();
  s[6] = tail(6, s[6]); maxh(4, 0); SB0();
  s[7] = tail(7, s[7]); maxh(4, 8);
#pragma unroll
  for (int j = 5; j < 8; ++j) { maxh(j, 0); maxh(j, 8); }
  const float m0 = __builtin_elementwise_maximum(mx[0], mx[1]), m1 = __builtin_elementwise_maximum(mx[2], mx[3]);
  float m = __builtin_elementwise_maximum(m0, m1);
  {
    auto rr = __builtin_amdgcn_permlane32_swap(__float_as_uint(m), __float_as_uint(m), false, false);
    m = __builtin_elementwise_maximum(__uint_as_float(rr[0]), __uint_as_float(rr[1]));
  }
  bf16x4 nm, one4;
  {
    const unsigned u0 = __float_as_uint(m) & 0xffff0000u;
    const float r1 = m - __uint_as_float(u0);
    const unsigned u1 = __float_as_uint(r1) & 0xffff0000u;
    const float r2 = r1 - __uint_as_float(u1);
    const u32x2 v = {hi ? 0u : ((u0 >> 16) | u1), hi ? 0u : (__float_as_uint(r2) >> 16)};
    nm = __builtin_bit_cast(bf16x4, v);
    one4 = __builtin_bit_cast(bf16x4, (u32x2){0xbf80bf80u, 0xbf80bf80u});
  }
  SB0();
  TS();
  f32x16 o = {};
  u32x4 pk[8][2];
  auto sub = [&](int j) { s[j] = __builtin_amdgcn_mfma_f32_32x32x8bf16_1k(one4, nm, s[j], 0, 0, 0); };
  auto ex8 = [&](int j, int ks) {
#pragma unroll
    for (int e = 0; e < 4; ++e) pk[j][ks][e] = cvtpk(__builtin_amdgcn_exp2f(s[j][8 * ks + 2 * e]), __builtin_amdgcn_exp2f(s[j][8 * ks + 2 * e + 1]));
  };
  auto vld = [&](int slot, int ks) { return lds_tr2(vp + slot * 512 + ks * 256, vp + slot * 512 + ks * 256 + 128); };
  auto pv = [&](bf16x8 v, u32x4 p) { o = mfma32(v, __builtin_bit_cast(bf16x8, p), o); };
  sub(7); sub(6); sub(5);
  __builtin_amdgcn_s_setprio(0);
  ex8(7, 0); ex8(7, 1);
  const u32x4 vrdy = *(volatile LAS u32x4*)(lds + K2_FL + 16);
  ex8(6, 0); ex8(6, 1);
  pin(pk[7][0]); pin(pk[7][1]); pin(pk[6][0]); pin(pk[6][1]);
  if (__builtin_expect(__builtin_amdgcn_readfirstlane((vrdy[0] & vrdy[1]) & (vrdy[2] & vrdy[3])) == 0, 0)) {
    for (int sp = 0; sp < (1 << 20); ++sp) {
      const u32x4 v = *(volatile LAS u32x4*)(lds + K2_FL + 16);
      if (__builtin_amdgcn_readfirstlane((v[0] & v[1]) & (v[2] & v[3]))) break;
      __builtin_amdgcn_s_sleep(1);
    }
  }
  asm volatile("" ::: "memory");
  bf16x8 vf[9][2];
  vf[7][0] = vld(7, 0); vf[7][1] = vld(7, 1);
  SB0();
  u32x4 mk[2];
#pragma unroll
  for (int j = 5; j >= 0; --j) {
    vf[j + 1][0] = vld(j + 1, 0); vf[j + 1][1] = vld(j + 1, 1);
    if (j == 0) { vf[0][0] = vld(0, 0); vf[8][0] = vld(8, 0); vf[0][1] = vld(0, 1); vf[8][1] = vld(8, 1); }
    if (j == 1) { mk[0] = *(const LAS u32x4*)(lds + K2_MSK + (dq + 20) * 16); mk[1] = *(const LAS u32x4*)(lds + K2_MSK + (dq + 4) * 16); }
    if (j > 0) sub(j - 1);
    SB0();
    ex8(j, 0); pv(vf[j + 2][0], pk[j + 2][0]); SB0();
    ex8(j, 1); pv(vf[j + 2][1], pk[j + 2][1]); SB0();
  }
  pv(vf[1][0], pk[1][0]); pv(vf[1][1], pk[1][1]);
#pragma unroll
  for (int ks = 0; ks < 2; ++ks) {
    const u32x4 p0 = pk[0][ks] & mk[ks], p8 = pk[0][ks] ^ p0;
    pv(vf[0][ks], p0); pv(vf[8][ks], p8);
  }
  TS();
  const float l = __uint_as_float(__builtin_amdgcn_permlane32_swap(__float_as_uint(o[8]), __float_as_uint(o[8]), false, false)[1]);
  const float il = __builtin_amdgcn_rcpf(l);
  const int tok = 256 * c + 32 * w + r32;
  const rsrc_t rw = mk_rsrc(ws, WS_END);
  const int ob = WS_O + (tok >> 6) * 20 * 4096 + ((tok >> 4) & 3) * 1024 + (tok & 15) * 16;
  const u32x4 w0 = {cvtpk(o[0] * il, o[1] * il), cvtpk(o[2] * il, o[3] * il), cvtpk(o[4] * il, o[5] * il), cvtpk(o[6] * il, o[7] * il)};
  st16(rw, ob + (h >> 1) * 4096 + (2 * (h & 1) + hi) * 256, w0);
  if (hi == 0) st8(rw, ob + (16 + (h >> 3)) * 4096 + ((h >> 1) & 3) * 256 + (h & 1) * 8, (u32x2){cvtpk(o[8] * il, o[9] * il), cvtpk(o[10] * il, o[11] * il)});
  TS();
  wait_vm<0>();
  if (lds_add(lds + K2_FL + 4, lane) == 7 && lane < 32) cnt_add(ws, c * 32 + lane);
  if (w == 7 || w == 0) { TS_END(w == 7 ? 1 : 5); }
}

extern "C" __global__ __launch_bounds__(768, 3) void k_attn(char* __restrict__ ws, const u16* __restrict__ x, const u16* __restrict__ wg, const u16* __restrict__ wu, const u16* __restrict__ wd) {
  extern __shared__ __attribute__((aligned(16))) char smem[];
  const int lane = threadIdx.x & 63, w = __builtin_amdgcn_readfirstlane(threadIdx.x >> 6);
  const int xcd = blockIdx.x & 7, idx = blockIdx.x >> 3, h = xcd * 4 + (idx >> 3), c = idx & 7;
  if (w < 4) attn_helper((ldsp)smem, ws, c, h, w, lane, x, wg, wu, wd);
  else attn_body((ldsp)smem, ws, c, h, w - 4, lane);
}

constexpr int K4_NS = 4, K4_STG = 20480, K4_NT = 20, K4_RING = 61440, K4_CTL = K4_RING + K4_NS * K4_STG, K4_SD = K4_CTL + 2048, K4_LDS = K4_SD + 8192 + 8192;

extern "C" __global__ __launch_bounds__(512, 2) void k_mlp(char* __restrict__ ws, u16* __restrict__ out) {
  extern __shared__ __attribute__((aligned(16))) char smem[];
  ldsp lds = (ldsp)smem + K4_RING;
  ldsp ctl = (ldsp)smem + K4_CTL;
  const int lane = threadIdx.x & 63, w = __builtin_amdgcn_readfirstlane(threadIdx.x >> 6);
  const unsigned lds00 = (unsigned)(uintptr_t)(ldsp)smem, lds0 = lds00 + K4_RING;
  const int xcd = blockIdx.x & 7, idx = blockIdx.x >> 3, mt = (xcd >> 2) * 8 + (idx >> 2), ft = (xcd & 3) * 4 + (idx & 3);
  const int mt5 = (xcd >> 1) * 8 + (idx >> 2), ct = (xcd & 1) * 4 + (idx & 3);
  const int odd = ct & 1, pb = (ct >> 1) * 5 + (odd ? 3 : 0), ps = (ct >> 1) * 5 + 2;
  if (w >= 4) {
    const int lw = w - 4;
    const char* wd = ws + WS_WD + ct * 245760;
    {
      const bool isa = w < 6;
      const char* src = isa ? ws + WS_H + mt * K4_NT * 8192 + lw * 4096 : ws + WS_WGU + ft * K4_NT * 12288 + (w - 6) * 6144;
      const unsigned dst = isa ? lds0 + lw * 4096 : lds0 + 8192 + (w - 6) * 6144;
      auto one = [] {};
      constexpr int XP = 0;
      if (isa) loader_loop<4 + XP, K4_NT, K4_NS, 0>([&](int slot) { dma_run<4>(src, dst + slot * K4_STG, lane * 16); src += 8192; one(); },
                                               [&] { dma_run<4>(ws + WS_ST + mt * 8192 + lw * 4096, lds00 + K4_SD + lw * 4096, lane * 16); }, [] {});
      else loader_loop<6 + XP, K4_NT, K4_NS, 0>([&](int slot) { dma_run<6>(src, dst + slot * K4_STG, lane * 16); src += 12288; one(); },
                                           [&] { dma_run<4>(ws + WS_CS + ft * 8192 + (w - 6) * 4096, lds00 + K4_SD + 8192 + (w - 6) * 4096, lane * 16); }, [] {});
    }
    dma_long<15>(wd + lw * 15360, lds00 + lw * 15360, lane * 16);
    {
      set_m0(lds00 + K4_SD + lw * 3072);
      const char* hs = ws + WS_H + ((mt5 >> 1) * 160 + (mt5 & 1) * 2 + (lw >> 1)) * 1024;
      const unsigned vo = (lane >> 5) * 4096 + (((lane >> 4) & 1) * 32 + (lw & 1) * 16 + (lane & 15)) * 16 + 2048;
      dma<0>(hs + pb * 8192 - 2048, vo); dma<1024>(hs + (pb + 1) * 8192 - 3072, vo); dma<2048>(hs + ps * 8192 - 4096, vo);
    }
    lds_wait(ctl, 4);
    dma_long<20>(wd + 61440 + lw * 20480, lds0 + lw * 20480, lane * 16);
    wait_vm_bar<0>();
    bar();
    dma_long<15>(wd + 143360 + lw * 15360, lds00 + lw * 15360, lane * 16);
    wait_vm_bar<0>();
    dma_long<10>(wd + 204800 + lw * 10240, lds0 + lw * 10240, lane * 16);
    wait_vm_bar<0>();
    return;
  }
  if (threadIdx.x < 4) ((LAS unsigned*)ctl)[threadIdx.x] = 0u;
  const int r32 = lane & 31, hi = lane >> 5, wm = w >> 1, wn = w & 1;
  TS_DECL;
  f32x16 acc[2][3];
#pragma unroll
  for (int i = 0; i < 2; ++i)
#pragma unroll
    for (int j = 0; j < 3; ++j) { acc[i][j] = (f32x16){}; pin(acc[i][j]); }
  bf16x8 af[2][2], wf[2][3];
  const int aoff = wm * 2048 + lane * 16, woff = 8192 + wn * 3072 + lane * 16;
  auto rda = [&](ldsp sb, int s, int i) { af[s][i] = lds_ld16(sb + aoff + s * 4096 + i * 1024); };
  auto rdw = [&](ldsp sb, int s, int j) { wf[s][j] = lds_ld16(sb + woff + s * 6144 + j * 1024); };
  auto mm = [&](int s, int i, int j) { acc[i][j] = mfma32(wf[s][j], af[s][i], acc[i][j]); };
  bar();
  const int tok = mt * 128 + wm * 64 + r32;
  f32x4 sv[2][2];
  float cv[10];
  const LAS float* cp = (const LAS float*)((ldsp)smem + K4_SD + 8192) + w * 48 + (lane < 48 ? lane : 47);
  ldsp sp = (ldsp)smem + K4_SD + (wm * 64 + r32) * 64 + hi * 32;
  TS();
  rda(lds, 0, 0); rdw(lds, 0, 0); rda(lds, 0, 1); rdw(lds, 0, 1); rdw(lds, 0, 2); SB0();
  constexpr float GC0 = -2.f * 0.7978845608028654f * 1.4426950408889634f, GC1 = GC0 * 0.044715f;
  float ca[2], cb[2], nmu[2], var[2];
  sfor<0, K4_NT>([&](auto tc) {
    constexpr int t = decltype(tc)::value;
    constexpr bool more = t + 1 < K4_NT;
    ldsp sb = lds + (t % K4_NS) * K4_STG;
    ldsp nb = lds + ((t + 1) % K4_NS) * K4_STG;
    mm(0, 0, 0); rda(sb, 1, 0); rdw(sb, 1, 0); SB0();
    mm(0, 1, 0); rda(sb, 1, 1); rdw(sb, 1, 1); SB0();
    mm(0, 0, 1); rdw(sb, 1, 2); SB0();
    mm(0, 1, 1); SB0();
    mm(0, 0, 2); SB0();
    mm(0, 1, 2); SB0();
    if (more) { bar(); SB0(); }
    else { asm volatile("s_waitcnt lgkmcnt(0)" ::: "memory"); lds_add(ctl, lane); SB0(); }
    mm(1, 0, 0); if (more) { rda(nb, 0, 0); rdw(nb, 0, 0); } SB0();
    mm(1, 1, 0); if (more) { rda(nb, 0, 1); rdw(nb, 0, 1); } SB0();
    mm(1, 0, 1); if (more) rdw(nb, 0, 2); SB0();
    if constexpr (t == 3) { cv[8] = cp[8 * 192]; cv[9] = cp[9 * 192]; }
    if constexpr (t >= 4 && t <= 7) { cv[2 * t - 8] = cp[(2 * t - 8) * 192]; cv[2 * t - 7] = cp[(2 * t - 7) * 192]; sv[(t - 4) >> 1][t & 1] = *(const LAS f32x4*)(sp + ((t - 4) >> 1) * 2048 + (t & 1) * 16); }
    if constexpr (t == 8) { if (lane < 48) ((LAS float*)(ctl + 1024))[w * 48 + lane] = (((cv[0] + cv[1]) + (cv[2] + cv[3])) + ((cv[4] + cv[5]) + (cv[6] + cv[7]))) + (cv[8] + cv[9]); }
    if constexpr (t == 9 || t == 10) {
      constexpr int i = t - 9;
      const f32x4 s0 = sv[i][0], s1 = sv[i][1];
      const float mu = xsum32((s0[0] + s0[2]) + (s1[0] + s1[2])) * 0.125f;
      const float d0 = s0[0] - mu, d1 = s0[2] - mu, d2 = s1[0] - mu, d3 = s1[2] - mu;
      var[i] = xsum32((s0[1] + s0[3]) + (s1[1] + s1[3]) + 80.f * ((d0 * d0 + d1 * d1) + (d2 * d2 + d3 * d3))) * (1.f / D_) + LN_EPS;
      const float rstd = __builtin_amdgcn_rsqf(var[i]);
      ca[i] = GC0 * rstd; cb[i] = GC1 * rstd * rstd * rstd; nmu[i] = -mu;
    }
    mm(1, 1, 1);
    mm(1, 0, 2);
    mm(1, 1, 2);
    if constexpr (t == 9 || t == 10) { pin(ca[t - 9]); pin(cb[t - 9]); pin(nmu[t - 9]); pin(var[t - 9]); }
    SB0();
  });
  TS();
  const rsrc_t rw = mk_rsrc(ws, WS_END);
  const LAS float* csb = (const LAS float*)(ctl + 1024) + wn * 96 + 4 * hi;
  f32x4 cq[3][4];
#pragma unroll
  for (int j = 0; j < 3; ++j)
#pragma unroll
    for (int b = 0; b < 4; ++b) cq[j][b] = *(const LAS f32x4*)(csb + j * 32 + 8 * b);
#pragma unroll
  for (int i = 0; i < 2; ++i) {
    const int tk = tok + 32 * i;
    const int ob = WS_ACT + (((tk >> 6) * 48 + ft * 3) * 4 + ((tk >> 4) & 3)) * 1024 + (hi * 16 + (tk & 15)) * 16;
#pragma unroll
    for (int j = 0; j < 3; ++j) {
      unsigned pk[4];
#pragma unroll
      for (int r = 0; r < 8; r += 2) {
        const f32x2 gv = (f32x2){acc[i][j][r], acc[i][j][r + 1]} + nmu[i] * (f32x2){cq[j][r >> 2][r & 3], cq[j][r >> 2][(r & 3) + 1]};
        const f32x2 uv = (f32x2){acc[i][j][8 + r], acc[i][j][9 + r]} + nmu[i] * (f32x2){cq[j][2 + (r >> 2)][r & 3], cq[j][2 + (r >> 2)][(r & 3) + 1]};
        const f32x2 arg = gv * (gv * gv * cb[i] + ca[i]);
        const f32x2 ex = {__builtin_amdgcn_exp2f(arg[0]), __builtin_amdgcn_exp2f(arg[1])};
        const f32x2 den = ex * var[i] + var[i];
        const f32x2 rc = {__builtin_amdgcn_rcpf(den[0]), __builtin_amdgcn_rcpf(den[1])};
        const f32x2 a = gv * uv * rc;
        pk[r >> 1] = cvtpk(a[0], a[1]);
      }
      const int T = wn * 3 + j;
      st16<AUX4>(rw, ob + (T >> 1) * 4096 + (T & 1) * 512, (u32x4){pk[0], pk[1], pk[2], pk[3]});
    }
  }
  TS();
  wait_vm<0>();
  if (lds_add(ctl + 4 + wm * 4, lane) == 1 && lane < 8) cnt_add(ws, 256 + (mt * 2 + wm) * 8 + lane);
  TS();
  if (w == 0) { cnt_wait(ws, 256 + mt5 * 8 + ct, 16); if (lane == 0) *(volatile LAS unsigned*)(ctl + 12) = 1u; }
  else lds_wait(ctl + 12, 1);
  TS();
  constexpr int WIN = WIN5;
  bf16x8 a5[WIN][2], w5[2][5];
  const int ao = WS_ACT + mt5 * 196608 + (w & 1) * 4096 + w * 1024 + lane * 16;
#pragma unroll
  for (int t = 0; t < WIN; ++t) { a5[t][0] = ld16_sc1(rw, ao, t * 8192); a5[t][1] = ld16_sc1(rw, ao ^ 1024, t * 8192); }
  SB0();
  bar();
  TS();
  f32x4 ac5[5];
  {
    ldsp rp = (ldsp)smem + K4_SD + w * 3072 + lane * 16;
#pragma unroll
    for (int j = 0; j < 5; ++j) {
      const int e = odd ? (j == 0 ? 5 : j - 1) : j;
      const u32x2 r = *(const LAS u32x2*)(rp + (e >> 1) * 1024 + (e & 1) * 8);
      ac5[j] = (f32x4){bflo(r[0]), bfhi(r[0]), bflo(r[1]), bfhi(r[1])};
    }
  }
  f32x4 ad5[5];
#pragma unroll
  for (int j = 0; j < 5; ++j) ad5[j] = (f32x4){0.f, 0.f, 0.f, 0.f};
  ldsp wp = (ldsp)smem + (w & 1) * 5120 + lane * 16;
#pragma unroll
  for (int j = 0; j < 5; ++j) w5[0][j] = lds_ld16(wp + j * 1024);
  sfor<0, 24>([&](auto tc) {
    constexpr int t = decltype(tc)::value, tn = t + 1;
    if constexpr (tn == 6 || tn == 14 || tn == 20) { bar(); SB0(); TS(); }
#pragma unroll
    for (int j = 0; j < 5; ++j) {
      if constexpr (tn < 24) w5[tn & 1][j] = lds_ld16(wp + (tn % 14) * 10240 + j * 1024);
      ad5[j] = mfma16(w5[t & 1][j], a5[t % WIN][1], ad5[j]);
    }
#pragma unroll
    for (int j = 0; j < 5; ++j) ac5[j] = mfma16(w5[t & 1][j], a5[t % WIN][0], ac5[j]);
    if constexpr (t + WIN < 24) { a5[t % WIN][0] = ld16_sc1(rw, ao, (t + WIN) * 8192); a5[t % WIN][1] = ld16_sc1(rw, ao ^ 1024, (t + WIN) * 8192); }
    SB0();
  });
  TS();
#pragma unroll
  for (int j = 0; j < 5; ++j) *(LAS f32x4*)((ldsp)smem + 102400 + (w * 5 + j) * 1024 + lane * 16) = ad5[j];
  bar();
#pragma unroll
  for (int j = 0; j < 5; ++j) ac5[j] += *(const LAS f32x4*)((ldsp)smem + 102400 + ((w ^ 1) * 5 + j) * 1024 + lane * 16);
  TS();
  {
    const int g = lane >> 4;
    u32x2 hv[5];
#pragma unroll
    for (int j = 0; j < 5; ++j) hv[j] = (u32x2){cvtpk(ac5[j][0], ac5[j][1]), cvtpk(ac5[j][2], ac5[j][3])};
    char* op = (char*)out + (size_t)(mt5 * 64 + w * 16 + (lane & 15)) * 1280 + ct * 160;
#pragma unroll
    for (int jp = 0; jp < 2; ++jp) {
      auto r0 = __builtin_amdgcn_permlane16_swap(hv[2 * jp][0], hv[2 * jp + 1][0], false, false);
      auto r1 = __builtin_amdgcn_permlane16_swap(hv[2 * jp][1], hv[2 * jp + 1][1], false, false);
      *(u32x4*)(op + jp * 64 + (g & 1) * 32 + (g >> 1) * 16) = (u32x4){r0[0], r1[0], r0[1], r1[1]};
    }
    *(u32x2*)(op + 128 + g * 8) = hv[4];
  }
  TS();
  if (w == 0) { TS_END(3); }
}

extern "C" __global__ void k_text_end() {}

extern "C" void kernel_launch(void* const* d_in, const int* in_sizes, int n_in, void* d_out, int out_size, void* d_ws, size_t ws_size,
                              hipStream_t stream) {
  const u16* x = (const u16*)d_in[0];
  const u16* wqkv = (const u16*)d_in[1];
  const u16* wo = (const u16*)d_in[2];
  const u16* wg = (const u16*)d_in[4];
  const u16* wu = (const u16*)d_in[5];
  const u16* wd = (const u16*)d_in[6];
  char* ws = (char*)d_ws;
  u16* out = (u16*)d_out;
  (void)hipFuncSetAttribute((const void*)k_qkv, hipFuncAttributeMaxDynamicSharedMemorySize, K1_LDS);
  (void)hipFuncSetAttribute((const void*)k_attn, hipFuncAttributeMaxDynamicSharedMemorySize, K2_LDS);
  (void)hipFuncSetAttribute((const void*)k_mlp, hipFuncAttributeMaxDynamicSharedMemorySize, K4_LDS);
  k_qkv<<<256, 768, K1_LDS, stream>>>(x, wqkv, wo, wd, ws);
  k_attn<<<256, 768, K2_LDS, stream>>>(ws, x, wg, wu, wd);
  k_mlp<<<256, 512, K4_LDS, stream>>>(ws, out);
}
```

```cpp
#include <hip/hip_runtime.h>
#include <stdint.h>
#include <type_traits>

#define LAS __attribute__((address_space(3)))
#define DI __device__ __forceinline__
typedef unsigned short u16;
typedef short bf16x8 __attribute__((ext_vector_type(8)));
typedef short bf16x4 __attribute__((ext_vector_type(4)));
typedef __bf16 bf16x2_t __attribute__((ext_vector_type(2)));
typedef float f32x2 __attribute__((ext_vector_type(2)));
typedef float f32x4 __attribute__((ext_vector_type(4)));
typedef float f32x16 __attribute__((ext_vector_type(16)));
typedef unsigned u32x4 __attribute__((ext_vector_type(4)));
typedef unsigned u32x2 __attribute__((ext_vector_type(2)));
typedef LAS char* ldsp;
typedef __amdgpu_buffer_rsrc_t rsrc_t;

constexpr int S_ = 2048, D_ = 640, NH = 32, DF = 1536;
constexpr float LN_EPS = 1e-5f;
constexpr float QSCALE = 0.22360679774997896f * 1.4426950408889634f;
constexpr unsigned ONES2 = 0x3f803f80u;
#define AUX_ST 16

constexpr int PLANE = S_ * 16;
constexpr int WS_QK = 0;
constexpr int WS_ACT = WS_QK + 80 * PLANE;
constexpr int WS_H = WS_ACT + S_ * DF * 2;
constexpr int WS_WO = WS_H + S_ * D_ * 2;
constexpr int WS_ST = WS_WO + D_ * D_ * 2;
constexpr int WS_CS = WS_ST + S_ * 8 * 8;
constexpr int WS_WGU = WS_CS + 16 * 8192;
constexpr int WS_WD = WS_WGU + D_ * DF * 4;
static_assert(WS_WO >= 0 && WS_QK % 4096 == 0, "workspace map");
constexpr int WS_CNT = WS_WD + D_ * DF * 2;
#define CSTR 64
constexpr int WS_END = WS_CNT + 512 * CSTR * 4;

#define TS_DECL
#define TS()
#define TS_END(kid)

template <int I, int N, class F> DI void sfor(F&& f) {
  if constexpr (I < N) { f(std::integral_constant<int, I>{}); sfor<I + 1, N>(f); }
}
#define SB0() __builtin_amdgcn_sched_barrier(0)
DI unsigned cvtpk(float lo, float hi) { f32x2 v = {lo, hi}; return __builtin_bit_cast(unsigned, __builtin_convertvector(v, bf16x2_t)); }
DI float bflo(unsigned u) { return __uint_as_float(u << 16); }
DI float bfhi(unsigned u) { return __uint_as_float(u & 0xffff0000u); }
DI float dot2(unsigned a, unsigned b, float c) {
  return __builtin_amdgcn_fdot2_f32_bf16(__builtin_bit_cast(bf16x2_t, a), __builtin_bit_cast(bf16x2_t, b), c, false);
}
DI f32x4 mfma16(bf16x8 a, bf16x8 b, f32x4 c) { return __builtin_amdgcn_mfma_f32_16x16x32_bf16(a, b, c, 0, 0, 0); }
DI f32x16 mfma32(bf16x8 a, bf16x8 b, f32x16 c) { return __builtin_amdgcn_mfma_f32_32x32x16_bf16(a, b, c, 0, 0, 0); }
DI void set_m0(unsigned v) { asm volatile("s_mov_b32 m0, %0\n\ts_nop 0" ::"s"(v) : "memory"); }
template <int OFF> DI void dma(const void* sbase, unsigned voff) {
  asm volatile("global_load_lds_dwordx4 %0, %1 offset:%2" ::"v"(voff), "s"(sbase), "n"(OFF) : "memory");
}
template <int OFF> DI void dma_nt(const void* sbase, unsigned voff) {
  asm volatile("global_load_lds_dwordx4 %0, %1 offset:%2 nt" ::"v"(voff), "s"(sbase), "n"(OFF) : "memory");
}
template <int OFF> DI void gld16(u32x4& d, const void* sbase, unsigned voff) {
  asm volatile("global_load_dwordx4 %0, %1, %2 offset:%3" : "=v"(d) : "v"(voff), "s"(sbase), "n"(OFF) : "memory");
}
template <int N> DI void wait_vm() { asm volatile("s_waitcnt vmcnt(%0)" ::"n"(N) : "memory"); }
template <int N> DI void wait_vm_bar() { asm volatile("s_waitcnt vmcnt(%0) lgkmcnt(0)\n\ts_barrier" ::"n"(N) : "memory"); }
DI void bar() { asm volatile("s_waitcnt lgkmcnt(0)\n\ts_barrier" ::: "memory"); }
template <class T> DI void pin(T& v) { asm volatile("" : "+v"(v)); }
DI bf16x8 lds_ld16(ldsp p) { return *(const LAS bf16x8*)p; }
DI bf16x4 lds_tr(ldsp p) { return __builtin_amdgcn_ds_read_tr16_b64_v4i16((LAS bf16x4*)p); }
DI bf16x8 lds_tr2(ldsp p0, ldsp p1) {
  bf16x4 a = lds_tr(p0), b = lds_tr(p1);
  return __builtin_shufflevector(a, b, 0, 1, 2, 3, 4, 5, 6, 7);
}
DI float xsum32(float v) {
  auto r = __builtin_amdgcn_permlane32_swap(__float_as_uint(v), __float_as_uint(v), false, false);
  return __uint_as_float(r[0]) + __uint_as_float(r[1]);
}
DI float xsum16_32(float v) {
  auto r = __builtin_amdgcn_permlane16_swap(__float_as_uint(v), __float_as_uint(v), false, false);
  return xsum32(__uint_as_float(r[0]) + __uint_as_float(r[1]));
}
DI rsrc_t mk_rsrc(void* p, int bytes) { return __builtin_amdgcn_make_buffer_rsrc(p, 0, bytes, 0x00020000); }
template <int AUX = 16> DI void st16(rsrc_t r, int off, u32x4 v) { __builtin_amdgcn_raw_buffer_store_b128(v, r, off, 0, AUX); }
template <int AUX = 16> DI void st8(rsrc_t r, int off, u32x2 v) { __builtin_amdgcn_raw_buffer_store_b64(v, r, off, 0, AUX); }
#define CSH 2
#define PDLY 0
#define PSLP 2
#define REL0 2
#define REL1 6
#define REL2 10
#define REL3 13
#define REL4 16
#define SLP4 1
#define LDAUX 16
#define WIN5 4
#define AUX3 0
#define AUX5 0
#define AUX4 16

template <int P, int NT, int NS, int X, class F, class G0, class G> DI void loader_loop(F&& issue, G0&& early, G&& extra) {
  static_assert(NS >= 4, "three stages in flight + one being read");
  issue(0); issue(1);
  wait_vm_bar<P>();
  early();
  issue(2); issue(3);
  int slot = 4 % NS;
#pragma unroll 1
  for (int t = 1; t < NT - 3; ++t) {
    wait_vm_bar<2 * P>();
    issue(slot);
    slot = slot == NS - 1 ? 0 : slot + 1;
  }
  extra();
  wait_vm_bar<2 * P + X>(); wait_vm_bar<P + X>(); wait_vm_bar<X>();
}
DI void touch(const char* p, int lane, int nlines, unsigned lds_scratch) {
  set_m0(lds_scratch);
  asm volatile("global_load_lds_dword %0, %1" ::"v"((unsigned)((lane < nlines ? lane : nlines - 1) * 128)), "s"(p) : "memory");
}
#define CODE_ADDR(dst, sym) asm volatile("s_getpc_b64 s[40:41]\n\ts_add_u32 s40, s40, " #sym "@rel32@lo+4\n\ts_addc_u32 s41, s41, " #sym "@rel32@hi+12\n\ts_mov_b64 %0, s[40:41]" : "=s"(dst) :: "s40", "s41", "scc")
template <int P> DI void dma_run(const char* src, unsigned dst, unsigned lane16) {
  static_assert(P <= 8, "offset range");
  set_m0(dst + 4096);
  sfor<0, P>([&](auto ic) { dma<decltype(ic)::value * 1024 - 4096>(src + 4096, lane16); });
}
template <int P, int NT, int NS, int SSTR, int LSTR, int X, class G0, class G> DI void linear_loader(const char* src, unsigned dst, unsigned lane16, G0&& early, G&& extra) {
  loader_loop<P, NT, NS, X>([&](int slot) { dma_run<P>(src, dst + slot * LSTR, lane16); src += SSTR; }, early, extra);
}

template <int N> DI void dma_long(const char* src, unsigned dst, unsigned lane16) {
  sfor<0, N / 5>([&](auto ic) { constexpr int i = decltype(ic)::value; dma_run<5>(src + i * 5120, dst + i * 5120, lane16); });
}
DI unsigned lds_add(ldsp p, int lane) {
  unsigned old = 0;
  if (lane == 0) old = __hip_atomic_fetch_add((LAS unsigned*)p, 1u, __ATOMIC_RELAXED, __HIP_MEMORY_SCOPE_WORKGROUP);
  return __builtin_amdgcn_readfirstlane(old);
}
DI void lds_wait(ldsp p, unsigned want) {
  for (int sp = 0; sp < (1 << 22); ++sp) {
    if ((unsigned)__builtin_amdgcn_readfirstlane(*(volatile LAS unsigned*)p) >= want) break;
    __builtin_amdgcn_s_sleep(1);
  }
  asm volatile("" ::: "memory");
}
DI void lds_post(ldsp p, unsigned v, int lane) { if (lane == 0) *(volatile LAS unsigned*)p = v; asm volatile("" ::: "memory"); }
DI void lds_wait4(ldsp p, unsigned want) {
  for (int sp = 0; sp < (1 << 22); ++sp) {
    const u32x4 v = *(volatile LAS u32x4*)p;
    if ((unsigned)__builtin_amdgcn_readfirstlane(min(min(v[0], v[1]), min(v[2], v[3]))) >= want) break;
    __builtin_amdgcn_s_sleep(SLP4);
  }
  asm volatile("" ::: "memory");
}
DI void cnt_add(char* ws, int idx) { __hip_atomic_fetch_add((unsigned*)(ws + WS_CNT) + idx * CSTR, 1u, __ATOMIC_RELAXED, __HIP_MEMORY_SCOPE_AGENT); }
DI void cnt_wait(char* ws, int idx, unsigned want) {
  unsigned* p = (unsigned*)(ws + WS_CNT) + idx * CSTR;
  for (int sp = 0; sp < (1 << 18); ++sp) {
    if ((unsigned)__builtin_amdgcn_readfirstlane(__hip_atomic_load(p, __ATOMIC_RELAXED, __HIP_MEMORY_SCOPE_AGENT)) >= want) break;
    __builtin_amdgcn_s_sleep(PSLP);
  }
  __builtin_amdgcn_fence(__ATOMIC_ACQUIRE, "wavefront");
}
DI bf16x8 ld16_sc1(rsrc_t r, int voff, int soff) { return __builtin_bit_cast(bf16x8, __builtin_amdgcn_raw_buffer_load_b128(r, voff, soff, LDAUX)); }

template <int KIND> DI void pack_issue(const u16* wsrc, const u16* wsrc2, int ks, int cg, unsigned ldsdst, int lane) {
  constexpr int PITCH = KIND == 1 ? DF * 2 : D_ * 2;
  set_m0(ldsdst);
  sfor<0, 4>([&](auto ic) {
    constexpr int g = decltype(ic)::value;
    const int t = (lane >> 5) & 1, q = (lane >> 3) & 3;
    int row;
    if (KIND == 0) { const int P = 4 * ks + g, hp = P / 5, r = P - 5 * hp; row = 40 * hp + q + (r < 2 ? 8 * t + 4 * r : r == 2 ? 16 + 4 * t : 12 + 8 * t + 4 * r); }
    else if (KIND == 1) row = 32 * (ks >> 1) + 16 * t + 4 * (2 * (ks & 1) + (g & 1)) + q;
    else row = 32 * ks + 16 * (g >> 1) + 8 * t + 4 * (g & 1) + q;
    dma_nt<g * 1024>((const char*)(KIND == 1 && g >= 2 ? wsrc2 : wsrc) + cg * 128 - 4096, (unsigned)(row * PITCH + (lane & 7) * 16 + 4096 - g * 1024));
  });
}
template <int KIND> DI void pack_finish(ldsp u, rsrc_t rw, int ks, int cg, int lane, LAS float* cs) {
  const int q = (lane & 15) >> 2, p = lane & 3;
  ldsp a = KIND == 1 ? u + ((lane >> 4) & 1) * 2048 + (8 * (lane >> 5) + q) * 128 + p * 8 : u + (8 * (lane >> 4) + q) * 128 + p * 8;
#pragma unroll
  for (int i = 0; i < 4; ++i) {
    const int nt = 4 * cg + i;
    const u32x4 f = __builtin_bit_cast(u32x4, lds_tr2(a + i * 32, a + i * 32 + 512));
    if (KIND == 1) {
      const int ft = nt / 6, j = nt - ft * 6;
      st16(rw, WS_WGU + ((ft * 40 + ks) * 6 + j) * 1024 + lane * 16, f);
      const unsigned sv = (lane & 15) == ((lane >> 4) & 1) ? ONES2 : 0u;
      const f32x4 c = mfma16(__builtin_bit_cast(bf16x8, (u32x4){sv, sv, sv, sv}), __builtin_bit_cast(bf16x8, f), (f32x4){0.f, 0.f, 0.f, 0.f});
      if (lane < 16) { cs[i * 32 + lane] = c[0]; cs[i * 32 + 16 + lane] = c[1]; }
    } else {
      const int ct = nt / 5, j = nt - ct * 5;
      st16(rw, (KIND == 0 ? WS_WO : WS_WD) + ((ct * (KIND == 0 ? 20 : 48) + ks) * 5 + j) * 1024 + lane * 16, f);
    }
  }
}

#define STATMODE 1
constexpr int NWD_A = 352;
#define WD0 8
#define WD1 24
constexpr int K1_NS = 4, K1_STG = 32768, K1_NT = 10, K1_CTL = K1_NS * K1_STG, K1_LDS = K1_CTL + 4096;

DI void qkv_compute(ldsp lds, char* __restrict__ ws, int mt, int nt, int w, int lane) {
  const int wm = w >> 1, wn = w & 1, r32 = lane & 31, hi = lane >> 5, q = (lane & 15) >> 2, p = lane & 3, blk = (lane >> 4) & 1;
  TS_DECL;
  ldsp ctl = lds + K1_CTL;
  int aoff[4], woff[2];
#pragma unroll
  for (int s = 0; s < 4; ++s) aoff[s] = (wm * 64 + r32) * 128 + (((2 * s + hi) ^ ((r32 >> 1) & 7)) * 16);
#pragma unroll
  for (int j = 0; j < 2; ++j) woff[j] = 16384 + (8 * hi + q) * 256 + (((wn * 2 + (j ^ wm)) ^ q) * 64) + blk * 32 + p * 8;
  f32x16 acc[2][2];
#pragma unroll
  for (int j = 0; j < 2; ++j)
#pragma unroll
    for (int i = 0; i < 2; ++i) { acc[j][i] = (f32x16){}; pin(acc[j][i]); }
  f32x4 cs = {};
  const unsigned sv = (lane & 15) == blk ? ONES2 : 0u;
  const bf16x8 sel = __builtin_bit_cast(bf16x8, (u32x4){sv, sv, sv, sv});
  bf16x8 xf[2][2], wf[2][2];
  auto rdx = [&](ldsp sb, int s, int i) { xf[s & 1][i] = lds_ld16(sb + aoff[s] + i * 4096); };
  auto rdw = [&](ldsp sb, int s, int j) { wf[s & 1][j] = lds_tr2(sb + woff[j] + s * 4096, sb + woff[j] + s * 4096 + 1024); };
  auto mm = [&](int s, int j, int i) { acc[j][i] = mfma32(wf[s & 1][j], xf[s & 1][i], acc[j][i]); };
  auto step = [&](ldsp nb, int s, int sn, bool rd) {
    cs = mfma16(sel, wf[s & 1][0], cs);
    mm(s, 0, 0); if (rd) { rdx(nb, sn, 0); rdw(nb, sn, 0); } SB0();
    mm(s, 0, 1); if (rd) { rdx(nb, sn, 1); rdw(nb, sn, 1); } SB0();
    mm(s, 1, 0); SB0();
    mm(s, 1, 1); SB0();
  };
  bar();
  TS();
  ldsp sb = lds;
  rdx(sb, 0, 0); rdw(sb, 0, 0); rdx(sb, 0, 1); rdw(sb, 0, 1); SB0();
  int slot = 0;
#pragma unroll 1
  for (int t = 0; t < K1_NT - 1; ++t) {
    step(sb, 0, 1, true); step(sb, 1, 2, true); step(sb, 2, 3, true);
    slot = slot == K1_NS - 1 ? 0 : slot + 1;
    sb = lds + slot * K1_STG;
    bar(); SB0();
    step(sb, 3, 0, true);
  }
  step(sb, 0, 1, true); step(sb, 1, 2, true); step(sb, 2, 3, true);
  const LAS f32x2* rst = (const LAS f32x2*)(ctl + 1024);
  LAS float* cst = (LAS float*)(ctl + 2048);
  cs = mfma16(sel, wf[1][0], cs);
  mm(3, 0, 0); mm(3, 0, 1); SB0();
  if (lane < 16) { cst[wn * 64 + wm * 32 + lane] = cs[0]; cst[wn * 64 + wm * 32 + 16 + lane] = cs[1]; }
  mm(3, 1, 0); mm(3, 1, 1); SB0();
  TS();
  bar();
  TS();
  const rsrc_t rw = mk_rsrc(ws, WS_END);
  const float sc = nt < 5 ? QSCALE : 1.f;
  f32x4 c4[2][4];
#pragma unroll
  for (int j = 0; j < 2; ++j)
#pragma unroll
    for (int b = 0; b < 4; ++b) c4[j][b] = *(const LAS f32x4*)(cst + wn * 64 + (j ^ wm) * 32 + 8 * b + 4 * hi);
#pragma unroll
  for (int i = 0; i < 2; ++i) {
    const int ml = wm * 64 + i * 32 + r32;
    const f32x2 st = rst[ml];
    const float a = st.y * sc, nm = -st.x * a;
    const int ob = WS_QK + ((nt * 16 + wn * 8 + hi) * S_ + mt * 128 + ml) * 16;
#pragma unroll
    for (int j = 0; j < 2; ++j)
#pragma unroll
      for (int bp = 0; bp < 2; ++bp) {
        unsigned pk[2][2];
#pragma unroll
        for (int e = 0; e < 2; ++e) {
          const int b = 2 * bp + e;
          const f32x2 v0 = (f32x2){acc[j][i][4 * b], acc[j][i][4 * b + 1]} * a + nm * (f32x2){c4[j][b][0], c4[j][b][1]};
          const f32x2 v1 = (f32x2){acc[j][i][4 * b + 2], acc[j][i][4 * b + 3]} * a + nm * (f32x2){c4[j][b][2], c4[j][b][3]};
          pk[e][0] = cvtpk(v0[0], v0[1]); pk[e][1] = cvtpk(v1[0], v1[1]);
        }
        auto r0 = __builtin_amdgcn_permlane32_swap(pk[0][0], pk[1][0], false, false);
        auto r1 = __builtin_amdgcn_permlane32_swap(pk[0][1], pk[1][1], false, false);
        st16(rw, ob + ((j ^ wm) * 4 + bp * 2) * S_ * 16, (u32x4){r0[0], r1[0], r0[1], r1[1]});
      }
  }
  TS();
  if (w == 0) { TS_END(0); }
}

DI void qkv_stats(ldsp lds, int sw, int lane) {
  const int r32 = lane & 31, hi = lane >> 5;
  const int off = (sw * 32 + r32) * 128, x = (r32 >> 1) & 7;
  f32x2 s0 = {0.f, 0.f}, s1 = {0.f, 0.f}, q0 = {0.f, 0.f}, q1 = {0.f, 0.f};
  int slot = 0;
#pragma unroll 1
  for (int t = 0; t < K1_NT; ++t) {
    bar();
    ldsp sb = lds + slot * K1_STG + off;
    slot = slot == K1_NS - 1 ? 0 : slot + 1;
    u32x4 v[4];
#pragma unroll
    for (int c = 0; c < 4; ++c) v[c] = *(const LAS u32x4*)(sb + (((2 * c + hi) ^ x) * 16));
#pragma unroll
    for (int c = 0; c < 4; ++c)
#pragma unroll
      for (int e = 0; e < 4; e += 2) {
        const float a0 = bflo(v[c][e]), a1 = bfhi(v[c][e]), b0 = bflo(v[c][e + 1]), b1 = bfhi(v[c][e + 1]);
        asm("v_add_f32 %0, %0, %1" : "+v"(s0.x) : "v"(a0)); asm("v_add_f32 %0, %0, %1" : "+v"(s0.y) : "v"(a1));
        asm("v_add_f32 %0, %0, %1" : "+v"(s1.x) : "v"(b0)); asm("v_add_f32 %0, %0, %1" : "+v"(s1.y) : "v"(b1));
        asm("v_fmac_f32 %0, %1, %1" : "+v"(q0.x) : "v"(a0)); asm("v_fmac_f32 %0, %1, %1" : "+v"(q0.y) : "v"(a1));
        asm("v_fmac_f32 %0, %1, %1" : "+v"(q1.x) : "v"(b0)); asm("v_fmac_f32 %0, %1, %1" : "+v"(q1.y) : "v"(b1));
      }
  }
  const float mu = xsum32((s0[0] + s0[1]) + (s1[0] + s1[1])) * (1.f / D_);
  const float var = fmaxf(xsum32((q0[0] + q0[1]) + (q1[0] + q1[1])) * (1.f / D_) - mu * mu, 0.f);
  if (hi == 0) ((LAS f32x2*)(lds + K1_CTL + 1024))[sw * 32 + r32] = (f32x2){mu, __builtin_amdgcn_rsqf(var + LN_EPS)};
  asm volatile("s_waitcnt lgkmcnt(0)" ::: "memory");
}

DI void qkv_loader(ldsp lds, const u16* __restrict__ x, const u16* __restrict__ wqkv, int mt, int nt, int lw, int lane) {
  const bool isx = lw < 2;
  const int half = lw & 1;
  unsigned voff[8];
#pragma unroll
  for (int i = 0; i < 8; ++i) {
    const int r = (half * 8 + i) * 8 + (lane >> 3), c = (lane & 7) ^ ((r >> 1) & 7);
    const int k = (half * 8 + i) * 4 + (lane >> 4), cp = lane & 15;
    voff[i] = (isx ? r * 1280 + c * 16 : k * 3840 + (cp ^ ((k & 3) << 2)) * 16) + 8192 - i * 1024;
  }
  const char* src = (isx ? (const char*)x + (size_t)mt * 128 * 1280 : (const char*)wqkv + nt * 256) - 4096;
  const int sstr = isx ? 128 : 64 * 3840;
  const unsigned dst = (unsigned)(uintptr_t)lds + lw * 8192 + 4096;
  loader_loop<8, K1_NT, K1_NS, 0>([&](int slot) {
    set_m0(dst + slot * K1_STG);
    sfor<0, 8>([&](auto ic) { constexpr int i = decltype(ic)::value; dma<i * 1024 - 4096>(src, voff[i]); });
    src += sstr;
  }, [] {}, [] {});
}

extern "C" __global__ __launch_bounds__(768, 3) void k_qkv(const u16* __restrict__ x, const u16* __restrict__ wqkv, const u16* __restrict__ wo, const u16* __restrict__ wd, char* __restrict__ ws) {
  extern __shared__ __attribute__((aligned(16))) char smem[];
  ldsp lds = (ldsp)smem;
  const int lane = threadIdx.x & 63, w = __builtin_amdgcn_readfirstlane(threadIdx.x >> 6);
  const int xcd = blockIdx.x & 7, idx = blockIdx.x >> 3;
  const int nt = (xcd & 1) * 8 + (idx >> 2), mt = (xcd >> 1) * 4 + (idx & 3);
  if (w < 4 && nt < 15) { qkv_loader(lds, x, wqkv, mt, nt, w, lane); return; }
  if (w == 11 && lane < 2) ((unsigned*)(ws + WS_CNT))[(blockIdx.x * 2 + lane) * CSTR] = 0u;
  if (w == 10) {
    const char *c0, *c1;
    CODE_ADDR(c0, k_qkv); CODE_ADDR(c1, k_text_end);
    const unsigned lim = c1 > c0 ? (unsigned)(c1 - c0) : 0u, off = (idx * 16 + (lane & 15)) * 128;
    set_m0((unsigned)(uintptr_t)lds + K1_CTL + 3072 + 512);
    asm volatile("global_load_lds_dword %0, %1" ::"v"(off < lim ? off : lim), "s"(c0) : "memory");
  }
  if (w == 8 || w == 9) {
    const int id = blockIdx.x * 64 + lane, u = w - 8;
    const bool isx = blockIdx.x < 128;
    const int i2 = id - 8192 < 7680 ? id - 8192 : 7679;
    const unsigned off = isx ? (id >> 2) * 1280 + 256 + u * 512 + (id & 3) * 128 : (128 + u * 256) * 3840 + i2 * 128;
    set_m0((unsigned)(uintptr_t)lds + K1_CTL + 3072 + u * 256);
    const char* src = isx ? (const char*)x : (const char*)wqkv;
    asm volatile("" : "+s"(src));
    if (u == 0) __builtin_amdgcn_s_sleep(WD0); else __builtin_amdgcn_s_sleep(WD1);
    asm volatile("global_load_lds_dword %0, %1" ::"v"(off), "s"(src) : "memory");
  }
  if (nt == 15) {
    const rsrc_t rw = mk_rsrc(ws, WS_END);
    constexpr int NU = (200 + NWD_A + 191) / 192;
    auto iss = [&](int r) {
      const int u = min(mt * 12 + w + r * 192, 200 + NWD_A - 1), v = u - 200;
      if (u < 200) pack_issue<0>(wo, wo, u / 10, u % 10, (unsigned)(uintptr_t)lds + (w * 2 + (r & 1)) * 4096, lane);
      else pack_issue<2>(wd, wd, v / 10, v % 10, (unsigned)(uintptr_t)lds + (w * 2 + (r & 1)) * 4096, lane);
    };
    iss(0); iss(1);
#pragma unroll 1
    for (int r = 0; r < NU; ++r) {
      if (r == 0) wait_vm<4>(); else if (r + 1 < NU) wait_vm<8>(); else wait_vm<4>();
      const int u = min(mt * 12 + w + r * 192, 200 + NWD_A - 1), v = u - 200;
      if (u < 200) pack_finish<0>(lds + (w * 2 + (r & 1)) * 4096, rw, u / 10, u % 10, lane, nullptr);
      else pack_finish<2>(lds + (w * 2 + (r & 1)) * 4096, rw, v / 10, v % 10, lane, nullptr);
      asm volatile("s_waitcnt lgkmcnt(0)" ::: "memory");
      if (r + 2 < NU) iss(r + 2);
    }
    return;
  }
  if (w >= 8) qkv_stats(lds, w - 8, lane);
  else qkv_compute(lds, ws, mt, nt, w - 4, lane);
}

#define VLEAD 3
constexpr int K2_K = 102400, K2_V = K2_K + 24576, K2_VP = 8192 + 64, K2_CS = K2_V + 4 * K2_VP, K2_MSK = K2_CS + 2560, K2_FL = K2_MSK + 832, K2_LDS = K2_FL + 64 + 256;
static_assert(K2_LDS <= 163840 && K2_FL % 16 == 0, "LDS map");

DI void n80_epilogue_h(f32x4 (&acc)[5], rsrc_t rw, int mt, int ct, int w, int lane) {
  const int fr = lane & 15, g = lane >> 4, tok = mt * 64 + w * 16 + fr;
  const int odd = ct & 1, pb = (ct >> 1) * 5 + (odd ? 3 : 0), ps = (ct >> 1) * 5 + 2;
  const int hb = WS_H + (((mt >> 1) & 1) * 40 + (g >> 1)) * PLANE + (mt >> 2) * 4096 + ((mt & 1) * 2 + (w >> 1)) * 1024 + ((g & 1) * 32 + (w & 1) * 16 + fr) * 16;
  u32x2 hv[5];
  float s = 0.f;
#pragma unroll
  for (int j = 0; j < 5; ++j) hv[j] = (u32x2){cvtpk(acc[j][0], acc[j][1]), cvtpk(acc[j][2], acc[j][3])};
  const u32x2 a0 = odd ? hv[1] : hv[0], a1 = odd ? hv[2] : hv[1], b0 = odd ? hv[3] : hv[2], b1 = odd ? hv[4] : hv[3], s1 = odd ? hv[0] : hv[4];
  st16<AUX3>(rw, hb + pb * 2 * PLANE, (u32x4){a0[0], a0[1], a1[0], a1[1]});
  st16<AUX3>(rw, hb + (pb + 1) * 2 * PLANE, (u32x4){b0[0], b0[1], b1[0], b1[1]});
  st8<AUX3>(rw, hb + ps * 2 * PLANE + odd * 8, s1);
  f32x4 v[5];
#pragma unroll
  for (int j = 0; j < 5; ++j) { v[j] = (f32x4){bflo(hv[j][0]), bfhi(hv[j][0]), bflo(hv[j][1]), bfhi(hv[j][1])}; s += (v[j][0] + v[j][1]) + (v[j][2] + v[j][3]); }
  const float mean = xsum16_32(s) * (1.f / 80.f);
  float m2 = 0.f;
#pragma unroll
  for (int j = 0; j < 5; ++j) { const f32x4 d = v[j] - mean; m2 += (d[0] * d[0] + d[1] * d[1]) + (d[2] * d[2] + d[3] * d[3]); }
  m2 = xsum16_32(m2);
  if (g == 0) st8<AUX3>(rw, WS_ST + (tok * 8 + ct) * 8, (u32x2){__float_as_uint(mean), __float_as_uint(m2)});
}

DI void attn_helper(ldsp lds, char* __restrict__ ws, int c, int h, int hw, int lane, const u16* __restrict__ x, const u16* __restrict__ wg, const u16* __restrict__ wu, const u16* __restrict__ wd) {
  const unsigned lds0 = (unsigned)(uintptr_t)lds;
  const int pm = 5 * (h >> 1) + 3 * (h & 1), pt = 5 * (h >> 1) + 2;
  const bool EDGE = c == 0;
  const int tok0 = 256 * c - 256 + 128 * hw + ((EDGE && hw < 2) ? 256 : 0);
  const char* kb = ws + WS_QK + (80 * S_ + tok0) * 16;
  {
    set_m0(lds0 + K2_K + hw * 2048); dma<0>(kb + pm * S_ * 16, lane * 16); dma<1024>(kb + pm * S_ * 16, lane * 16);
    set_m0(lds0 + K2_K + 8192 + hw * 2048); dma<0>(kb + (pm + 1) * S_ * 16, lane * 16); dma<1024>(kb + (pm + 1) * S_ * 16, lane * 16);
    set_m0(lds0 + K2_K + 16384 + hw * 2048); dma<0>(kb + pt * S_ * 16, lane * 16); dma<1024>(kb + pt * S_ * 16, lane * 16);
  }
  TS_DECL;
  const int b = blockIdx.x, xcd = b & 7, idx = b >> 3;
  if (hw == 0 && lane < 8) *(LAS unsigned*)(lds + K2_FL + lane * 4) = 0u;
  {
    const unsigned tm = EDGE && hw < 2 ? 0xff80u : 0u;
    *(LAS u32x4*)(lds + K2_V + 3 * K2_VP + hw * 2048 + lane * 16) = (u32x4){tm, 0u, ONES2, ONES2};
    *(LAS u32x4*)(lds + K2_V + 3 * K2_VP + hw * 2048 + 1024 + lane * 16) = (u32x4){tm, 0u, ONES2, ONES2};
  }
  if (hw == 1 && lane < 52) {
    const int d = lane - 20;
    u32x4 mk;
#pragma unroll
    for (int e = 0; e < 4; ++e) { const int k0 = ((2 * e) & 3) + 8 * (e >> 1); mk[e] = (k0 > d ? 0xffffu : 0u) | (k0 + 1 > d ? 0xffff0000u : 0u); }
    *(LAS u32x4*)(lds + K2_MSK + lane * 16) = mk;
  }
  wait_vm<VLEAD>();
  {
    const char* vb = kb + 80 * S_ * 16;
    set_m0(lds0 + K2_V + hw * 2048); dma<0>(vb + pm * S_ * 16, lane * 16); dma<1024>(vb + pm * S_ * 16, lane * 16);
    set_m0(lds0 + K2_V + K2_VP + hw * 2048); dma<0>(vb + (pm + 1) * S_ * 16, lane * 16); dma<1024>(vb + (pm + 1) * S_ * 16, lane * 16);
    set_m0(lds0 + K2_V + 2 * K2_VP + hw * 2048); dma<0>(vb + pt * S_ * 16, lane * 16); dma<1024>(vb + pt * S_ * 16, lane * 16);
  }
  const rsrc_t rw = mk_rsrc(ws, WS_END);
  const int mt = xcd * 4 + (idx >> 3), ct = idx & 7;
  const int q0 = hw * 25600;
  const int bb = b, pcg = bb % 24, pks = (bb / 24) * 4 + hw, v0 = NWD_A + (b - 240) * 8 + hw * 2;
  wait_vm_bar<6>();
  if (b < 240) { pack_issue<1>(wg, wu, pks, pcg, lds0 + q0, lane); wait_vm<4>(); }
  else { pack_issue<2>(wd, wd, v0 / 10, v0 % 10, lds0 + q0, lane); pack_issue<2>(wd, wd, v0 / 10, v0 % 10 + 1, lds0 + q0 + 4096, lane); wait_vm<8>(); }
  if (lane == 0) *(volatile LAS unsigned*)(lds + K2_FL + 16 + hw * 4) = 1u;
  TS();
  u32x4 xr[2]; u32x2 xs;
  {
    const char* xp = (const char*)x + (size_t)(mt * 64 + hw * 16) * 1280 + ct * 160;
    const int g = lane >> 4;
    const unsigned o16 = (lane & 15) * 1280 + (g & 1) * 32 + (g >> 1) * 16, o8 = (lane & 15) * 1280 + g * 8;
    asm volatile("global_load_dwordx4 %0, %1, %2" : "=v"(xr[0]) : "v"(o16), "s"(xp) : "memory");
    asm volatile("global_load_dwordx4 %0, %1, %2 offset:64" : "=v"(xr[1]) : "v"(o16), "s"(xp) : "memory");
    asm volatile("global_load_dwordx2 %0, %1, %2 offset:128" : "=v"(xs) : "v"(o8), "s"(xp) : "memory");
  }
  dma_long<15>(ws + WS_WO + ct * 102400 + q0 + 10240, lds0 + q0 + 10240, lane * 16);
  wait_vm<15>();
  pin(xr[0]); pin(xr[1]); pin(xs);
  TS();
  if (b >= 240) {
    pack_finish<2>(lds + q0, rw, v0 / 10, v0 % 10, lane, nullptr); pack_finish<2>(lds + q0 + 4096, rw, v0 / 10, v0 % 10 + 1, lane, nullptr);
    asm volatile("s_waitcnt lgkmcnt(0)" ::: "memory");
  } else {
    LAS float* ct4 = (LAS float*)(lds + K2_CS);
    pack_finish<1>(lds + q0, rw, pks, pcg, lane, ct4 + hw * 128);
    asm volatile("s_waitcnt lgkmcnt(0)" ::: "memory");
    if (lds_add(lds + K2_FL, lane) == 3) {
      asm volatile("" ::: "memory");
      const LAS f32x2* c2 = (const LAS f32x2*)ct4 + lane;
      const f32x2 a = (c2[0] + c2[64]) + (c2[128] + c2[192]);
      const int e = pcg * 128 + lane * 2, ft = e / 192;
      st8(rw, WS_CS + ft * 8192 + ((bb / 24) * 192 + e - ft * 192) * 4, __builtin_bit_cast(u32x2, a));
    }
  }
  TS();
  dma_long<10>(ws + WS_WO + ct * 102400 + q0, lds0 + q0, lane * 16);
  if (hw == 3) {
    const char *c0, *c1;
    CODE_ADDR(c0, k_mlp); CODE_ADDR(c1, k_text_end);
    const unsigned lim = c1 > c0 ? (unsigned)(c1 - c0) : 0u, off = (idx * 8 + (lane & 7)) * 128;
    set_m0(lds0 + K2_FL + 64);
    asm volatile("global_load_lds_dword %0, %1" ::"v"(off < lim ? off : lim), "s"(c0) : "memory");
  }
  f32x4 acc[5];
#pragma unroll
  for (int jp = 0; jp < 2; ++jp) {
    auto r0 = __builtin_amdgcn_permlane16_swap(xr[jp][0], xr[jp][2], false, false);
    auto r1 = __builtin_amdgcn_permlane16_swap(xr[jp][1], xr[jp][3], false, false);
    acc[2 * jp] = (f32x4){bflo(r0[0]), bfhi(r0[0]), bflo(r1[0]), bfhi(r1[0])};
    acc[2 * jp + 1] = (f32x4){bflo(r0[1]), bfhi(r0[1]), bflo(r1[1]), bfhi(r1[1])};
  }
  acc[4] = (f32x4){bflo(xs[0]), bfhi(xs[0]), bflo(xs[1]), bfhi(xs[1])};
#pragma unroll
  for (int j = 0; j < 5; ++j) pin(acc[j]);
  wait_vm<0>();
  lds_add(lds + K2_FL + 8, lane);
  lds_wait(lds + K2_FL + 8, 4);
  TS();
  if (hw == 0) {
    lds_wait(lds + K2_FL + 4, 8);
    __builtin_amdgcn_s_sleep(PDLY);
    cnt_wait(ws, (mt * 8 + ct) >> CSH, 32); if (lane == 0) *(volatile LAS unsigned*)(lds + K2_FL + 12) = 1u; }
  else lds_wait(lds + K2_FL + 12, 1);
  TS();
  bf16x8 af[10][2], wf[2][5];
  f32x4 ac1[5];
#pragma unroll
  for (int j = 0; j < 5; ++j) ac1[j] = (f32x4){0.f, 0.f, 0.f, 0.f};
  const int ao = WS_QK + ((hw & 1) * 4 + (lane >> 4)) * PLANE + (mt * 64 + hw * 16 + (lane & 15)) * 16;
#pragma unroll
  for (int t = 0; t < 10; ++t) { af[t][0] = ld16_sc1(rw, ao, t * 8 * PLANE); af[t][1] = ld16_sc1(rw, ao ^ 256, t * 8 * PLANE); }
  SB0();
  ldsp wp = lds + (hw & 1) * 5120 + lane * 16;
#pragma unroll
  for (int j = 0; j < 5; ++j) wf[0][j] = lds_ld16(wp + j * 1024);
#pragma unroll
  for (int t = 0; t < 10; ++t) {
#pragma unroll
    for (int j = 0; j < 5; ++j) {
      if (t + 1 < 10) wf[(t + 1) & 1][j] = lds_ld16(wp + (t + 1) * 10240 + j * 1024);
      ac1[j] = mfma16(wf[t & 1][j], af[t][1], ac1[j]);
    }
#pragma unroll
    for (int j = 0; j < 5; ++j) acc[j] = mfma16(wf[t & 1][j], af[t][0], acc[j]);
    SB0();
  }
  lds_wait(lds + K2_FL + 4, 8);
#pragma unroll
  for (int j = 0; j < 5; ++j) *(LAS f32x4*)(lds + K2_K + (hw * 5 + j) * 1024 + lane * 16) = ac1[j];
  bar();
#pragma unroll
  for (int j = 0; j < 5; ++j) acc[j] += *(const LAS f32x4*)(lds + K2_K + ((hw ^ 1) * 5 + j) * 1024 + lane * 16);
  TS();
  n80_epilogue_h(acc, rw, mt, ct, hw, lane);
  TS();
  if (hw == 1) { TS_END(2); }
}

DI void attn_body(ldsp lds, char* __restrict__ ws, int c, int h, int w, int lane) {
  const int r32 = lane & 31, hi = lane >> 5;
  TS_DECL;
  const int pm = 5 * (h >> 1) + 3 * (h & 1), pt = 5 * (h >> 1) + 2;
  const int dq = r32 - 4 * hi;
  const float NEG = -__builtin_inff();
  f32x16 s[8], s8;
#pragma unroll
  for (int r = 0; r < 16; ++r) {
    const bool le = (r & 3) + 8 * (r >> 2) <= dq;
    s8[r] = le ? 0.f : NEG;
    s[0][r] = le ? NEG : 0.f;
  }
  pin(s[0]); pin(s8);
  SB0();
  const char* qb = ws + WS_QK + (256 * c + 32 * w) * 16;
  const bf16x8 qm = *(const bf16x8*)(qb + ((pm + hi) * S_ + r32) * 16);
  u32x2 qtr = *(const u32x2*)(qb + (pt * S_ + r32) * 16 + (h & 1) * 8);
  if (hi) qtr = (u32x2){0x3f80u, 0u};
  bf16x4 qt = __builtin_bit_cast(bf16x4, qtr);
  pin(qt);
  TS();
  bar();
  TS();
  ldsp kp = lds + K2_K + hi * 8192 + (w * 32 + r32) * 16;
  ldsp ktp = lds + (hi ? K2_V + 3 * K2_VP + w * 512 : K2_K + 16384 + (w * 32 + r32) * 16 + (h & 1) * 8);
  const int vq = (lane & 15) >> 2, vpp = lane & 3, vblk = (lane >> 4) & 1;
  ldsp vp = lds + K2_V + (w * 32 + 4 * hi + vq) * 16 + (vblk == 0 ? (vpp >> 1) * K2_VP + (vpp & 1) * 8 : vpp == 0 ? 2 * K2_VP + (h & 1) * 8 : 3 * K2_VP + 8);
  bf16x8 km[9]; bf16x4 kt[9];
#pragma unroll
  for (int jj = 0; jj < 9; ++jj) { const int j = jj == 0 ? 8 : jj - 1; km[j] = lds_ld16(kp + j * 512); kt[j] = *(const LAS bf16x4*)(ktp + j * 512); }
  SB0();
  auto tail = [&](int j, f32x16 cin) { return __builtin_amdgcn_mfma_f32_32x32x8bf16_1k(kt[j], qt, cin, 0, 0, 0); };
  float mx[4];
  auto maxh = [&](int j, int r0) {
#pragma unroll
    for (int e = 0; e < 4; ++e) mx[e] = __builtin_elementwise_maximum(__builtin_elementwise_maximum(mx[e], s[j][r0 + 2 * e]), s[j][r0 + 2 * e + 1]);
  };
  s8 = mfma32(km[8], qm, s8); s[0] = mfma32(km[0], qm, s[0]); s8 = tail(8, s8);
  s[1] = mfma32(km[1], qm, (f32x16){}); s[0] = tail(0, s[0]);
  s[2] = mfma32(km[2], qm, (f32x16){}); s[1] = tail(1, s[1]); SB0();
  s[3] = mfma32(km[3], qm, (f32x16){});
#pragma unroll
  for (int r = 0; r < 8; ++r) s[0][r] = __builtin_elementwise_maximum(s[0][r], s8[r]);
  SB0();
  s[2] = tail(2, s[2]);
#pragma unroll
  for (int r = 8; r < 16; ++r) s[0][r] = __builtin_elementwise_maximum(s[0][r], s8[r]);
  pin(s[0]);
  SB0();
#pragma unroll
  for (int e = 0; e < 4; ++e) mx[e] = s[0][e];
  s[4] = mfma32(km[4], qm, (f32x16){}); maxh(0, 4); mx[0] = __builtin_elementwise_maximum(__builtin_elementwise_maximum(mx[0], s[0][12]), s[0][13]);
  mx[1] = __builtin_elementwise_maximum(__builtin_elementwise_maximum(mx[1], s[0][14]), s[0][15]); SB0();
  s[3] = tail(3, s[3]); maxh(1, 0); SB0();
  s[5] = mfma32(km[5], qm, (f32x16){}); maxh(1, 8); SB0();
  s[4] = tail(4, s[4]); maxh(2, 0); SB0();
  s[6] = mfma32(km[6], qm, (f32x16){}); maxh(2, 8); SB0();
  s[5] = tail(5, s[5]); maxh(3, 0); SB0();
  s[7] = mfma32(km[7], qm, (f32x16){}); maxh(3, 8); SB0();
  s[6] = tail(6, s[6]); maxh(4, 0); SB0();
  s[7] = tail(7, s[7]); maxh(4, 8);
#pragma unroll
  for (int j = 5; j < 8; ++j) { maxh(j, 0); maxh(j, 8); }
  const float m0 = __builtin_elementwise_maximum(mx[0], mx[1]), m1 = __builtin_elementwise_maximum(mx[2], mx[3]);
  float m = __builtin_elementwise_maximum(m0, m1);
  {
    auto rr = __builtin_amdgcn_permlane32_swap(__float_as_uint(m), __float_as_uint(m), false, false);
    m = __builtin_elementwise_maximum(__uint_as_float(rr[0]), __uint_as_float(rr[1]));
  }
  bf16x4 nm, one4;
  {
    const unsigned u0 = __float_as_uint(m) & 0xffff0000u;
    const float r1 = m - __uint_as_float(u0);
    const unsigned u1 = __float_as_uint(r1) & 0xffff0000u;
    const float r2 = r1 - __uint_as_float(u1);
    const u32x2 v = {hi ? 0u : ((u0 >> 16) | u1), hi ? 0u : (__float_as_uint(r2) >> 16)};
    nm = __builtin_bit_cast(bf16x4, v);
    one4 = __builtin_bit_cast(bf16x4, (u32x2){0xbf80bf80u, 0xbf80bf80u});
  }
  SB0();
  TS();
  f32x16 o = {};
  u32x4 pk[8][2];
  auto sub = [&](int j) { s[j] = __builtin_amdgcn_mfma_f32_32x32x8bf16_1k(one4, nm, s[j], 0, 0, 0); };
  auto ex8 = [&](int j, int ks) {
#pragma unroll
    for (int e = 0; e < 4; ++e) pk[j][ks][e] = cvtpk(__builtin_amdgcn_exp2f(s[j][8 * ks + 2 * e]), __builtin_amdgcn_exp2f(s[j][8 * ks + 2 * e + 1]));
  };
  auto vld = [&](int slot, int ks) { return lds_tr2(vp + slot * 512 + ks * 256, vp + slot * 512 + ks * 256 + 128); };
  auto pv = [&](bf16x8 v, u32x4 p) { o = mfma32(v, __builtin_bit_cast(bf16x8, p), o); };
  sub(7); sub(6); sub(5);
  __builtin_amdgcn_s_setprio(0);
  ex8(7, 0); ex8(7, 1);
  const u32x4 vrdy = *(volatile LAS u32x4*)(lds + K2_FL + 16);
  ex8(6, 0); ex8(6, 1);
  pin(pk[7][0]); pin(pk[7][1]); pin(pk[6][0]); pin(pk[6][1]);
  if (__builtin_expect(__builtin_amdgcn_readfirstlane((vrdy[0] & vrdy[1]) & (vrdy[2] & vrdy[3])) == 0, 0)) {
    for (int sp = 0; sp < (1 << 20); ++sp) {
      const u32x4 v = *(volatile LAS u32x4*)(lds + K2_FL + 16);
      if (__builtin_amdgcn_readfirstlane((v[0] & v[1]) & (v[2] & v[3]))) break;
      __builtin_amdgcn_s_sleep(1);
    }
  }
  asm volatile("" ::: "memory");
  bf16x8 vf[9][2];
  vf[7][0] = vld(7, 0); vf[7][1] = vld(7, 1);
  SB0();
  u32x4 mk[2];
#pragma unroll
  for (int j = 5; j >= 0; --j) {
    vf[j + 1][0] = vld(j + 1, 0); vf[j + 1][1] = vld(j + 1, 1);
    if (j == 0) { vf[0][0] = vld(0, 0); vf[8][0] = vld(8, 0); vf[0][1] = vld(0, 1); vf[8][1] = vld(8, 1); }
    if (j == 1) { mk[0] = *(const LAS u32x4*)(lds + K2_MSK + (dq + 20) * 16); mk[1] = *(const LAS u32x4*)(lds + K2_MSK + (dq + 4) * 16); }
    if (j > 0) sub(j - 1);
    SB0();
    ex8(j, 0); pv(vf[j + 2][0], pk[j + 2][0]); SB0();
    ex8(j, 1); pv(vf[j + 2][1], pk[j + 2][1]); SB0();
  }
  pv(vf[1][0], pk[1][0]); pv(vf[1][1], pk[1][1]);
#pragma unroll
  for (int ks = 0; ks < 2; ++ks) {
    const u32x4 p0 = pk[0][ks] & mk[ks], p8 = pk[0][ks] ^ p0;
    pv(vf[0][ks], p0); pv(vf[8][ks], p8);
  }
  TS();
  const float l = __uint_as_float(__builtin_amdgcn_permlane32_swap(__float_as_uint(o[8]), __float_as_uint(o[8]), false, false)[1]);
  const float il = __builtin_amdgcn_rcpf(l);
  const int tok = 256 * c + 32 * w + r32;
  const rsrc_t rw = mk_rsrc(ws, WS_END);
  const u32x4 w0 = {cvtpk(o[0] * il, o[1] * il), cvtpk(o[2] * il, o[3] * il), cvtpk(o[4] * il, o[5] * il), cvtpk(o[6] * il, o[7] * il)};
  st16(rw, WS_QK + (pm + hi) * PLANE + tok * 16, w0);
  if (hi == 0) st8(rw, WS_QK + pt * PLANE + tok * 16 + (h & 1) * 8, (u32x2){cvtpk(o[8] * il, o[9] * il), cvtpk(o[10] * il, o[11] * il)});
  TS();
  wait_vm<0>();
  if (lds_add(lds + K2_FL + 4, lane) == 7 && lane < (32 >> CSH)) cnt_add(ws, (c * 32 >> CSH) + lane);
  if (w == 7 || w == 0) { TS_END(w == 7 ? 1 : 5); }
}

extern "C" __global__ __launch_bounds__(768, 3) void k_attn(char* __restrict__ ws, const u16* __restrict__ x, const u16* __restrict__ wg, const u16* __restrict__ wu, const u16* __restrict__ wd) {
  extern __shared__ __attribute__((aligned(16))) char smem[];
  const int lane = threadIdx.x & 63, w = __builtin_amdgcn_readfirstlane(threadIdx.x >> 6);
  const int xcd = blockIdx.x & 7, idx = blockIdx.x >> 3, h = xcd * 4 + (idx >> 3), c = idx & 7;
  if (w < 4) attn_helper((ldsp)smem, ws, c, h, w, lane, x, wg, wu, wd);
  else attn_body((ldsp)smem, ws, c, h, w - 4, lane);
}

constexpr int K4_NS = 4, K4_STG = 20480, K4_NT = 20, K4_RING = 61440, K4_CTL = K4_RING + K4_NS * K4_STG, K4_SD = K4_CTL + 2048, K4_LDS = K4_SD + 8192 + 8192;

extern "C" __global__ __launch_bounds__(512, 2) void k_mlp(char* __restrict__ ws, u16* __restrict__ out) {
  extern __shared__ __attribute__((aligned(16))) char smem[];
  ldsp lds = (ldsp)smem + K4_RING;
  ldsp ctl = (ldsp)smem + K4_CTL;
  const int lane = threadIdx.x & 63, w = __builtin_amdgcn_readfirstlane(threadIdx.x >> 6);
  const unsigned lds00 = (unsigned)(uintptr_t)(ldsp)smem, lds0 = lds00 + K4_RING;
  const int xcd = blockIdx.x & 7, idx = blockIdx.x >> 3, mt = (xcd >> 2) * 8 + (idx >> 2), ft = (xcd & 3) * 4 + (idx & 3);
  const int mt5 = (xcd >> 1) * 8 + (idx >> 2), ct = (xcd & 1) * 4 + (idx & 3);
  const int odd = ct & 1, pb = (ct >> 1) * 5 + (odd ? 3 : 0), ps = (ct >> 1) * 5 + 2;
  if (w >= 4) {
    const int lw = w - 4;
    const char* wd = ws + WS_WD + ct * 245760;
    {
      const bool isa = w < 6;
      const char* src = isa ? ws + WS_H + ((mt & 1) * 40 + lw) * PLANE + (mt >> 1) * 4096 : ws + WS_WGU + ft * K4_NT * 12288 + (w - 6) * 6144;
      const unsigned dst = isa ? lds0 + lw * 4096 : lds0 + 8192 + (w - 6) * 6144;
      auto one = [] {};
      constexpr int XP = 0;
      if (isa) loader_loop<4 + XP, K4_NT, K4_NS, 0>([&](int slot) { dma_run<4>(src, dst + slot * K4_STG, lane * 16); src += 2 * PLANE; one(); },
                                               [&] { dma_run<4>(ws + WS_ST + mt * 8192 + lw * 4096, lds00 + K4_SD + lw * 4096, lane * 16); }, [] {});
      else loader_loop<6 + XP, K4_NT, K4_NS, 0>([&](int slot) { dma_run<6>(src, dst + slot * K4_STG, lane * 16); src += 12288; one(); },
                                           [&] { dma_run<4>(ws + WS_CS + ft * 8192 + (w - 6) * 4096, lds00 + K4_SD + 8192 + (w - 6) * 4096, lane * 16); }, [] {});
    }
    dma_long<15>(wd + lw * 15360, lds00 + lw * 15360, lane * 16);
    {
      set_m0(lds00 + K4_SD + lw * 3072);
      const char* hs = ws + WS_H + ((mt5 >> 1) & 1) * 40 * PLANE + (mt5 >> 2) * 4096 + ((mt5 & 1) * 2 + (lw >> 1)) * 1024;
      const unsigned vo = (lane >> 5) * PLANE + (((lane >> 4) & 1) * 32 + (lw & 1) * 16 + (lane & 15)) * 16 + 2048;
      dma<0>(hs + pb * 2 * PLANE - 2048, vo); dma<1024>(hs + (pb + 1) * 2 * PLANE - 3072, vo); dma<2048>(hs + ps * 2 * PLANE - 4096, vo);
    }
    lds_wait(ctl, 4);
    dma_long<20>(wd + 61440 + lw * 20480, lds0 + lw * 20480, lane * 16);
    wait_vm_bar<0>();
#pragma unroll 1
    for (int g = 0; g < 5; ++g) {
      lds_wait4(ctl + 16, g + 1);
      dma_run<5>(wd + 143360 + g * 20480 + lw * 5120, lds00 + g * 20480 + lw * 5120, lane * 16);
      wait_vm<0>();
      lds_post(ctl + 32 + lw * 4, g + 1, lane);
    }
    return;
  }
  if (threadIdx.x < 12) ((LAS unsigned*)ctl)[threadIdx.x] = 0u;
  const int r32 = lane & 31, hi = lane >> 5, wm = w >> 1, wn = w & 1;
  TS_DECL;
  f32x16 acc[2][3];
#pragma unroll
  for (int i = 0; i < 2; ++i)
#pragma unroll
    for (int j = 0; j < 3; ++j) { acc[i][j] = (f32x16){}; pin(acc[i][j]); }
  bf16x8 af[2][2], wf[2][3];
  const int aoff = wm * 2048 + lane * 16, woff = 8192 + wn * 3072 + lane * 16;
  auto rda = [&](ldsp sb, int s, int i) { af[s][i] = lds_ld16(sb + aoff + s * 4096 + i * 1024); };
  auto rdw = [&](ldsp sb, int s, int j) { wf[s][j] = lds_ld16(sb + woff + s * 6144 + j * 1024); };
  auto mm = [&](int s, int i, int j) { acc[i][j] = mfma32(wf[s][j], af[s][i], acc[i][j]); };
  bar();
  const int tok = mt * 128 + wm * 64 + r32;
  f32x4 sv[2][2];
  float cv[10];
  const LAS float* cp = (const LAS float*)((ldsp)smem + K4_SD + 8192) + w * 48 + (lane < 48 ? lane : 47);
  ldsp sp = (ldsp)smem + K4_SD + (wm * 64 + r32) * 64 + hi * 32;
  TS();
  rda(lds, 0, 0); rdw(lds, 0, 0); rda(lds, 0, 1); rdw(lds, 0, 1); rdw(lds, 0, 2); SB0();
  constexpr float GC0 = -2.f * 0.7978845608028654f * 1.4426950408889634f, GC1 = GC0 * 0.044715f;
  float ca[2], cb[2], nmu[2], var[2];
  sfor<0, K4_NT>([&](auto tc) {
    constexpr int t = decltype(tc)::value;
    constexpr bool more = t + 1 < K4_NT;
    ldsp sb = lds + (t % K4_NS) * K4_STG;
    ldsp nb = lds + ((t + 1) % K4_NS) * K4_STG;
    mm(0, 0, 0); rda(sb, 1, 0); rdw(sb, 1, 0); SB0();
    mm(0, 1, 0); rda(sb, 1, 1); rdw(sb, 1, 1); SB0();
    mm(0, 0, 1); rdw(sb, 1, 2); SB0();
    mm(0, 1, 1); SB0();
    mm(0, 0, 2); SB0();
    mm(0, 1, 2); SB0();
    if (more) { bar(); SB0(); }
    else { asm volatile("s_waitcnt lgkmcnt(0)" ::: "memory"); lds_add(ctl, lane); SB0(); }
    mm(1, 0, 0); if (more) { rda(nb, 0, 0); rdw(nb, 0, 0); } SB0();
    mm(1, 1, 0); if (more) { rda(nb, 0, 1); rdw(nb, 0, 1); } SB0();
    mm(1, 0, 1); if (more) rdw(nb, 0, 2); SB0();
    if constexpr (t == 3) { cv[8] = cp[8 * 192]; cv[9] = cp[9 * 192]; }
    if constexpr (t >= 4 && t <= 7) { cv[2 * t - 8] = cp[(2 * t - 8) * 192]; cv[2 * t - 7] = cp[(2 * t - 7) * 192]; sv[(t - 4) >> 1][t & 1] = *(const LAS f32x4*)(sp + ((t - 4) >> 1) * 2048 + (t & 1) * 16); }
    if constexpr (t == 8) { if (lane < 48) ((LAS float*)(ctl + 1024))[w * 48 + lane] = (((cv[0] + cv[1]) + (cv[2] + cv[3])) + ((cv[4] + cv[5]) + (cv[6] + cv[7]))) + (cv[8] + cv[9]); }
    if constexpr (t == 9 || t == 10) {
      constexpr int i = t - 9;
      const f32x4 s0 = sv[i][0], s1 = sv[i][1];
      const float mu = xsum32((s0[0] + s0[2]) + (s1[0] + s1[2])) * 0.125f;
      const float d0 = s0[0] - mu, d1 = s0[2] - mu, d2 = s1[0] - mu, d3 = s1[2] - mu;
      var[i] = xsum32((s0[1] + s0[3]) + (s1[1] + s1[3]) + 80.f * ((d0 * d0 + d1 * d1) + (d2 * d2 + d3 * d3))) * (1.f / D_) + LN_EPS;
      const float rstd = __builtin_amdgcn_rsqf(var[i]);
      ca[i] = GC0 * rstd; cb[i] = GC1 * rstd * rstd * rstd; nmu[i] = -mu;
    }
    mm(1, 1, 1);
    mm(1, 0, 2);
    mm(1, 1, 2);
    if constexpr (t == 9 || t == 10) { pin(ca[t - 9]); pin(cb[t - 9]); pin(nmu[t - 9]); pin(var[t - 9]); }
    SB0();
  });
  TS();
  const rsrc_t rw = mk_rsrc(ws, WS_END);
  const LAS float* csb = (const LAS float*)(ctl + 1024) + wn * 96 + 4 * hi;
  f32x4 cq[3][4];
#pragma unroll
  for (int j = 0; j < 3; ++j)
#pragma unroll
    for (int b = 0; b < 4; ++b) cq[j][b] = *(const LAS f32x4*)(csb + j * 32 + 8 * b);
#pragma unroll
  for (int i = 0; i < 2; ++i) {
    const int tk = tok + 32 * i;
    const int ob = WS_ACT + (((tk >> 6) * 48 + ft * 3) * 4 + ((tk >> 4) & 3)) * 1024 + (hi * 16 + (tk & 15)) * 16;
#pragma unroll
    for (int j = 0; j < 3; ++j) {
      unsigned pk[4];
#pragma unroll
      for (int r = 0; r < 8; r += 2) {
        const f32x2 gv = (f32x2){acc[i][j][r], acc[i][j][r + 1]} + nmu[i] * (f32x2){cq[j][r >> 2][r & 3], cq[j][r >> 2][(r & 3) + 1]};
        const f32x2 uv = (f32x2){acc[i][j][8 + r], acc[i][j][9 + r]} + nmu[i] * (f32x2){cq[j][2 + (r >> 2)][r & 3], cq[j][2 + (r >> 2)][(r & 3) + 1]};
        const f32x2 arg = gv * (gv * gv * cb[i] + ca[i]);
        const f32x2 ex = {__builtin_amdgcn_exp2f(arg[0]), __builtin_amdgcn_exp2f(arg[1])};
        const f32x2 den = ex * var[i] + var[i];
        const f32x2 rc = {__builtin_amdgcn_rcpf(den[0]), __builtin_amdgcn_rcpf(den[1])};
        const f32x2 a = gv * uv * rc;
        pk[r >> 1] = cvtpk(a[0], a[1]);
      }
      const int T = wn * 3 + j;
      st16<AUX4>(rw, ob + (T >> 1) * 4096 + (T & 1) * 512, (u32x4){pk[0], pk[1], pk[2], pk[3]});
    }
  }
  TS();
  wait_vm<0>();
  if (lds_add(ctl + 4 + wm * 4, lane) == 1 && lane < (8 >> CSH)) cnt_add(ws, 256 + ((mt * 2 + wm) * 8 >> CSH) + lane);
  TS();
  if (w == 0) { __builtin_amdgcn_s_sleep(PDLY); cnt_wait(ws, 256 + ((mt5 * 8 + ct) >> CSH), 16); if (lane == 0) *(volatile LAS unsigned*)(ctl + 12) = 1u; }
  else lds_wait(ctl + 12, 1);
  TS();
  constexpr int WIN = WIN5;
  bf16x8 a5[WIN][2], w5[2][5];
  const int ao = WS_ACT + mt5 * 196608 + (w & 1) * 4096 + w * 1024 + lane * 16;
#pragma unroll
  for (int t = 0; t < WIN; ++t) { a5[t][0] = ld16_sc1(rw, ao, t * 8192); a5[t][1] = ld16_sc1(rw, ao ^ 1024, t * 8192); }
  SB0();
  bar();
  TS();
  f32x4 ac5[5];
  {
    ldsp rp = (ldsp)smem + K4_SD + w * 3072 + lane * 16;
#pragma unroll
    for (int j = 0; j < 5; ++j) {
      const int e = odd ? (j == 0 ? 5 : j - 1) : j;
      const u32x2 r = *(const LAS u32x2*)(rp + (e >> 1) * 1024 + (e & 1) * 8);
      ac5[j] = (f32x4){bflo(r[0]), bfhi(r[0]), bflo(r[1]), bfhi(r[1])};
    }
  }
  f32x4 ad5[5];
#pragma unroll
  for (int j = 0; j < 5; ++j) ad5[j] = (f32x4){0.f, 0.f, 0.f, 0.f};
  ldsp wp = (ldsp)smem + (w & 1) * 5120 + lane * 16;
#pragma unroll
  for (int j = 0; j < 5; ++j) w5[0][j] = lds_ld16(wp + j * 1024);
  sfor<0, 24>([&](auto tc) {
    constexpr int t = decltype(tc)::value, tn = t + 1;
    if constexpr (tn >= 14 && tn < 24 && (tn & 1) == 0) { lds_wait4(ctl + 32, (tn - 14) / 2 + 1); SB0(); }
    if constexpr (tn == REL0 || tn == REL1 || tn == REL2 || tn == REL3 || tn == REL4) {
      asm volatile("" ::: "memory");
      lds_post(ctl + 16 + w * 4, tn == REL0 ? 1 : tn == REL1 ? 2 : tn == REL2 ? 3 : tn == REL3 ? 4 : 5, lane);
    }
#pragma unroll
    for (int j = 0; j < 5; ++j) {
      if constexpr (tn < 24) w5[tn & 1][j] = lds_ld16(wp + (tn % 14) * 10240 + j * 1024);
      ad5[j] = mfma16(w5[t & 1][j], a5[t % WIN][1], ad5[j]);
    }
#pragma unroll
    for (int j = 0; j < 5; ++j) ac5[j] = mfma16(w5[t & 1][j], a5[t % WIN][0], ac5[j]);
    if constexpr (t + WIN < 24) { a5[t % WIN][0] = ld16_sc1(rw, ao, (t + WIN) * 8192); a5[t % WIN][1] = ld16_sc1(rw, ao ^ 1024, (t + WIN) * 8192); }
    SB0();
  });
  TS();
#pragma unroll
  for (int j = 0; j < 5; ++j) *(LAS f32x4*)((ldsp)smem + 102400 + (w * 5 + j) * 1024 + lane * 16) = ad5[j];
  bar();
#pragma unroll
  for (int j = 0; j < 5; ++j) ac5[j] += *(const LAS f32x4*)((ldsp)smem + 102400 + ((w ^ 1) * 5 + j) * 1024 + lane * 16);
  TS();
  {
    const int g = lane >> 4;
    u32x2 hv[5];
#pragma unroll
    for (int j = 0; j < 5; ++j) hv[j] = (u32x2){cvtpk(ac5[j][0], ac5[j][1]), cvtpk(ac5[j][2], ac5[j][3])};
    const rsrc_t ro = mk_rsrc(out, S_ * D_ * 2);
    const int oo = (mt5 * 64 + w * 16 + (lane & 15)) * 1280 + ct * 160;
#pragma unroll
    for (int jp = 0; jp < 2; ++jp) {
      auto r0 = __builtin_amdgcn_permlane16_swap(hv[2 * jp][0], hv[2 * jp + 1][0], false, false);
      auto r1 = __builtin_amdgcn_permlane16_swap(hv[2 * jp][1], hv[2 * jp + 1][1], false, false);
      st16<AUX5>(ro, oo + jp * 64 + (g & 1) * 32 + (g >> 1) * 16, (u32x4){r0[0], r1[0], r0[1], r1[1]});
    }
    st8<AUX5>(ro, oo + 128 + g * 8, hv[4]);
  }
  TS();
  if (w == 0) { TS_END(3); }
}

extern "C" __global__ void k_text_end() {}

extern "C" void kernel_launch(void* const* d_in, const int* in_sizes, int n_in, void* d_out, int out_size, void* d_ws, size_t ws_size,
                              hipStream_t stream) {
  const u16* x = (const u16*)d_in[0];
  const u16* wqkv = (const u16*)d_in[1];
  const u16* wo = (const u16*)d_in[2];
  const u16* wg = (const u16*)d_in[4];
  const u16* wu = (const u16*)d_in[5];
  const u16* wd = (const u16*)d_in[6];
  char* ws = (char*)d_ws;
  u16* out = (u16*)d_out;
  (void)hipFuncSetAttribute((const void*)k_qkv, hipFuncAttributeMaxDynamicSharedMemorySize, K1_LDS);
  (void)hipFuncSetAttribute((const void*)k_attn, hipFuncAttributeMaxDynamicSharedMemorySize, K2_LDS);
  (void)hipFuncSetAttribute((const void*)k_mlp, hipFuncAttributeMaxDynamicSharedMemorySize, K4_LDS);
  k_qkv<<<256, 768, K1_LDS, stream>>>(x, wqkv, wo, wd, ws);
  k_attn<<<256, 768, K2_LDS, stream>>>(ws, x, wg, wu, wd);
  k_mlp<<<256, 512, K4_LDS, stream>>>(ws, out);
}
```

```cpp
#define NOWARM
#include <hip/hip_runtime.h>
#include <stdint.h>
#include <type_traits>

#define LAS __attribute__((address_space(3)))
#define DI __device__ __forceinline__
typedef unsigned short u16;
typedef short bf16x8 __attribute__((ext_vector_type(8)));
typedef short bf16x4 __attribute__((ext_vector_type(4)));
typedef __bf16 bf16x2_t __attribute__((ext_vector_type(2)));
typedef float f32x2 __attribute__((ext_vector_type(2)));
typedef float f32x4 __attribute__((ext_vector_type(4)));
typedef float f32x16 __attribute__((ext_vector_type(16)));
typedef unsigned u32x4 __attribute__((ext_vector_type(4)));
typedef unsigned u32x2 __attribute__((ext_vector_type(2)));
typedef LAS char* ldsp;
typedef __amdgpu_buffer_rsrc_t rsrc_t;

constexpr int S_ = 2048, D_ = 640, NH = 32, DF = 1536;
constexpr float LN_EPS = 1e-5f;
constexpr float QSCALE = 0.22360679774997896f * 1.4426950408889634f;
constexpr unsigned ONES2 = 0x3f803f80u;
#define AUX_ST 16

constexpr int PLANE = S_ * 16;
constexpr int WS_QK = 0;
constexpr int WS_ACT = WS_QK + 80 * PLANE;
constexpr int WS_H = WS_ACT + S_ * DF * 2;
constexpr int WS_WO = WS_H + S_ * D_ * 2;
constexpr int WS_ST = WS_WO + D_ * D_ * 2;
constexpr int WS_CS = WS_ST + S_ * 8 * 8;
constexpr int WS_WGU = WS_CS + 16 * 8192;
constexpr int WS_WD = WS_WGU + D_ * DF * 4;
static_assert(WS_WO >= 0 && WS_QK % 4096 == 0, "workspace map");
constexpr int WS_CNT = WS_WD + D_ * DF * 2;
#define CSTR 64
constexpr int WS_END = WS_CNT + 512 * CSTR * 4;

#define TS_DECL
#define TS()
#define TS_END(kid)

template <int I, int N, class F> DI void sfor(F&& f) {
  if constexpr (I < N) { f(std::integral_constant<int, I>{}); sfor<I + 1, N>(f); }
}
#define SB0() __builtin_amdgcn_sched_barrier(0)
DI unsigned cvtpk(float lo, float hi) { f32x2 v = {lo, hi}; return __builtin_bit_cast(unsigned, __builtin_convertvector(v, bf16x2_t)); }
DI float bflo(unsigned u) { return __uint_as_float(u << 16); }
DI float bfhi(unsigned u) { return __uint_as_float(u & 0xffff0000u); }
DI float dot2(unsigned a, unsigned b, float c) {
  return __builtin_amdgcn_fdot2_f32_bf16(__builtin_bit_cast(bf16x2_t, a), __builtin_bit_cast(bf16x2_t, b), c, false);
}
DI f32x4 mfma16(bf16x8 a, bf16x8 b, f32x4 c) { return __builtin_amdgcn_mfma_f32_16x16x32_bf16(a, b, c, 0, 0, 0); }
DI f32x16 mfma32(bf16x8 a, bf16x8 b, f32x16 c) { return __builtin_amdgcn_mfma_f32_32x32x16_bf16(a, b, c, 0, 0, 0); }
DI void set_m0(unsigned v) { asm volatile("s_mov_b32 m0, %0\n\ts_nop 0" ::"s"(v) : "memory"); }
template <int OFF> DI void dma(const void* sbase, unsigned voff) {
  asm volatile("global_load_lds_dwordx4 %0, %1 offset:%2" ::"v"(voff), "s"(sbase), "n"(OFF) : "memory");
}
template <int OFF> DI void dma_nt(const void* sbase, unsigned voff) {
  asm volatile("global_load_lds_dwordx4 %0, %1 offset:%2 nt" ::"v"(voff), "s"(sbase), "n"(OFF) : "memory");
}
template <int OFF> DI void gld16(u32x4& d, const void* sbase, unsigned voff) {
  asm volatile("global_load_dwordx4 %0, %1, %2 offset:%3" : "=v"(d) : "v"(voff), "s"(sbase), "n"(OFF) : "memory");
}
template <int N> DI void wait_vm() { asm volatile("s_waitcnt vmcnt(%0)" ::"n"(N) : "memory"); }
template <int N> DI void wait_vm_bar() { asm volatile("s_waitcnt vmcnt(%0) lgkmcnt(0)\n\ts_barrier" ::"n"(N) : "memory"); }
DI void bar() { asm volatile("s_waitcnt lgkmcnt(0)\n\ts_barrier" ::: "memory"); }
template <class T> DI void pin(T& v) { asm volatile("" : "+v"(v)); }
DI bf16x8 lds_ld16(ldsp p) { return *(const LAS bf16x8*)p; }
DI bf16x4 lds_tr(ldsp p) { return __builtin_amdgcn_ds_read_tr16_b64_v4i16((LAS bf16x4*)p); }
DI bf16x8 lds_tr2(ldsp p0, ldsp p1) {
  bf16x4 a = lds_tr(p0), b = lds_tr(p1);
  return __builtin_shufflevector(a, b, 0, 1, 2, 3, 4, 5, 6, 7);
}
DI float xsum32(float v) {
  auto r = __builtin_amdgcn_permlane32_swap(__float_as_uint(v), __float_as_uint(v), false, false);
  return __uint_as_float(r[0]) + __uint_as_float(r[1]);
}
DI float xsum16_32(float v) {
  auto r = __builtin_amdgcn_permlane16_swap(__float_as_uint(v), __float_as_uint(v), false, false);
  return xsum32(__uint_as_float(r[0]) + __uint_as_float(r[1]));
}
DI rsrc_t mk_rsrc(void* p, int bytes) { return __builtin_amdgcn_make_buffer_rsrc(p, 0, bytes, 0x00020000); }
template <int AUX = 16> DI void st16(rsrc_t r, int off, u32x4 v) { __builtin_amdgcn_raw_buffer_store_b128(v, r, off, 0, AUX); }
template <int AUX = 16> DI void st8(rsrc_t r, int off, u32x2 v) { __builtin_amdgcn_raw_buffer_store_b64(v, r, off, 0, AUX); }
#define CSH 2
#define PDLY 0
#define PSLP 2
#define REL0 2
#define REL1 6
#define REL2 10
#define REL3 13
#define REL4 16
#define SLP4 1
#define LDAUX 16
#define WIN5 4
#define AUX3 0
#define AUX5 0
#define AUX4 16

template <int P, int NT, int NS, int X, class F, class G0, class G> DI void loader_loop(F&& issue, G0&& early, G&& extra) {
  static_assert(NS >= 4, "three stages in flight + one being read");
  issue(0); issue(1);
  wait_vm_bar<P>();
  early();
  issue(2); issue(3);
  int slot = 4 % NS;
#pragma unroll 1
  for (int t = 1; t < NT - 3; ++t) {
    wait_vm_bar<2 * P>();
    issue(slot);
    slot = slot == NS - 1 ? 0 : slot + 1;
  }
  extra();
  wait_vm_bar<2 * P + X>(); wait_vm_bar<P + X>(); wait_vm_bar<X>();
}
DI void touch(const char* p, int lane, int nlines, unsigned lds_scratch) {
  set_m0(lds_scratch);
  asm volatile("global_load_lds_dword %0, %1" ::"v"((unsigned)((lane < nlines ? lane : nlines - 1) * 128)), "s"(p) : "memory");
}
#define CODE_ADDR(dst, sym) asm volatile("s_getpc_b64 s[40:41]\n\ts_add_u32 s40, s40, " #sym "@rel32@lo+4\n\ts_addc_u32 s41, s41, " #sym "@rel32@hi+12\n\ts_mov_b64 %0, s[40:41]" : "=s"(dst) :: "s40", "s41", "scc")
template <int P> DI void dma_run(const char* src, unsigned dst, unsigned lane16) {
  static_assert(P <= 8, "offset range");
  set_m0(dst + 4096);
  sfor<0, P>([&](auto ic) { dma<decltype(ic)::value * 1024 - 4096>(src + 4096, lane16); });
}
template <int P, int NT, int NS, int SSTR, int LSTR, int X, class G0, class G> DI void linear_loader(const char* src, unsigned dst, unsigned lane16, G0&& early, G&& extra) {
  loader_loop<P, NT, NS, X>([&](int slot) { dma_run<P>(src, dst + slot * LSTR, lane16); src += SSTR; }, early, extra);
}

template <int N> DI void dma_long(const char* src, unsigned dst, unsigned lane16) {
  sfor<0, N / 5>([&](auto ic) { constexpr int i = decltype(ic)::value; dma_run<5>(src + i * 5120, dst + i * 5120, lane16); });
}
DI unsigned lds_add(ldsp p, int lane) {
  unsigned old = 0;
  if (lane == 0) old = __hip_atomic_fetch_add((LAS unsigned*)p, 1u, __ATOMIC_RELAXED, __HIP_MEMORY_SCOPE_WORKGROUP);
  return __builtin_amdgcn_readfirstlane(old);
}
DI void lds_wait(ldsp p, unsigned want) {
  for (int sp = 0; sp < (1 << 22); ++sp) {
    if ((unsigned)__builtin_amdgcn_readfirstlane(*(volatile LAS unsigned*)p) >= want) break;
    __builtin_amdgcn_s_sleep(1);
  }
  asm volatile("" ::: "memory");
}
DI void lds_post(ldsp p, unsigned v, int lane) { if (lane == 0) *(volatile LAS unsigned*)p = v; asm volatile("" ::: "memory"); }
DI void lds_wait4(ldsp p, unsigned want) {
  for (int sp = 0; sp < (1 << 22); ++sp) {
    const u32x4 v = *(volatile LAS u32x4*)p;
    if ((unsigned)__builtin_amdgcn_readfirstlane(min(min(v[0], v[1]), min(v[2], v[3]))) >= want) break;
    __builtin_amdgcn_s_sleep(SLP4);
  }
  asm volatile("" ::: "memory");
}
DI void cnt_add(char* ws, int idx) { __hip_atomic_fetch_add((unsigned*)(ws + WS_CNT) + idx * CSTR, 1u, __ATOMIC_RELAXED, __HIP_MEMORY_SCOPE_AGENT); }
DI void cnt_wait(char* ws, int idx, unsigned want) {
  unsigned* p = (unsigned*)(ws + WS_CNT) + idx * CSTR;
  for (int sp = 0; sp < (1 << 18); ++sp) {
    if ((unsigned)__builtin_amdgcn_readfirstlane(__hip_atomic_load(p, __ATOMIC_RELAXED, __HIP_MEMORY_SCOPE_AGENT)) >= want) break;
    __builtin_amdgcn_s_sleep(PSLP);
  }
  __builtin_amdgcn_fence(__ATOMIC_ACQUIRE, "wavefront");
}
DI bf16x8 ld16_sc1(rsrc_t r, int voff, int soff) { return __builtin_bit_cast(bf16x8, __builtin_amdgcn_raw_buffer_load_b128(r, voff, soff, LDAUX)); }

template <int KIND> DI void pack_issue(const u16* wsrc, const u16* wsrc2, int ks, int cg, unsigned ldsdst, int lane) {
  constexpr int PITCH = KIND == 1 ? DF * 2 : D_ * 2;
  set_m0(ldsdst);
  sfor<0, 4>([&](auto ic) {
    constexpr int g = decltype(ic)::value;
    const int t = (lane >> 5) & 1, q = (lane >> 3) & 3;
    int row;
    if (KIND == 0) { const int P = 4 * ks + g, hp = P / 5, r = P - 5 * hp; row = 40 * hp + q + (r < 2 ? 8 * t + 4 * r : r == 2 ? 16 + 4 * t : 12 + 8 * t + 4 * r); }
    else if (KIND == 1) row = 32 * (ks >> 1) + 16 * t + 4 * (2 * (ks & 1) + (g & 1)) + q;
    else row = 32 * ks + 16 * (g >> 1) + 8 * t + 4 * (g & 1) + q;
    dma_nt<g * 1024>((const char*)(KIND == 1 && g >= 2 ? wsrc2 : wsrc) + cg * 128 - 4096, (unsigned)(row * PITCH + (lane & 7) * 16 + 4096 - g * 1024));
  });
}
template <int KIND> DI void pack_finish(ldsp u, rsrc_t rw, int ks, int cg, int lane, LAS float* cs) {
  const int q = (lane & 15) >> 2, p = lane & 3;
  ldsp a = KIND == 1 ? u + ((lane >> 4) & 1) * 2048 + (8 * (lane >> 5) + q) * 128 + p * 8 : u + (8 * (lane >> 4) + q) * 128 + p * 8;
#pragma unroll
  for (int i = 0; i < 4; ++i) {
    const int nt = 4 * cg + i;
    const u32x4 f = __builtin_bit_cast(u32x4, lds_tr2(a + i * 32, a + i * 32 + 512));
    if (KIND == 1) {
      const int ft = nt / 6, j = nt - ft * 6;
      st16(rw, WS_WGU + ((ft * 40 + ks) * 6 + j) * 1024 + lane * 16, f);
      const unsigned sv = (lane & 15) == ((lane >> 4) & 1) ? ONES2 : 0u;
      const f32x4 c = mfma16(__builtin_bit_cast(bf16x8, (u32x4){sv, sv, sv, sv}), __builtin_bit_cast(bf16x8, f), (f32x4){0.f, 0.f, 0.f, 0.f});
      if (lane < 16) { cs[i * 32 + lane] = c[0]; cs[i * 32 + 16 + lane] = c[1]; }
    } else {
      const int ct = nt / 5, j = nt - ct * 5;
      st16(rw, (KIND == 0 ? WS_WO : WS_WD) + ((ct * (KIND == 0 ? 20 : 48) + ks) * 5 + j) * 1024 + lane * 16, f);
    }
  }
}

#define STATMODE 1
constexpr int NWD_A = 352;
#define WD0 8
#define WD1 24
constexpr int K1_NS = 4, K1_STG = 32768, K1_NT = 10, K1_CTL = K1_NS * K1_STG, K1_LDS = K1_CTL + 4096;

DI void qkv_compute(ldsp lds, char* __restrict__ ws, int mt, int nt, int w, int lane) {
  const int wm = w >> 1, wn = w & 1, r32 = lane & 31, hi = lane >> 5, q = (lane & 15) >> 2, p = lane & 3, blk = (lane >> 4) & 1;
  TS_DECL;
  ldsp ctl = lds + K1_CTL;
  int aoff[4], woff[2];
#pragma unroll
  for (int s = 0; s < 4; ++s) aoff[s] = (wm * 64 + r32) * 128 + (((2 * s + hi) ^ ((r32 >> 1) & 7)) * 16);
#pragma unroll
  for (int j = 0; j < 2; ++j) woff[j] = 16384 + (8 * hi + q) * 256 + (((wn * 2 + (j ^ wm)) ^ q) * 64) + blk * 32 + p * 8;
  f32x16 acc[2][2];
#pragma unroll
  for (int j = 0; j < 2; ++j)
#pragma unroll
    for (int i = 0; i < 2; ++i) { acc[j][i] = (f32x16){}; pin(acc[j][i]); }
  f32x4 cs = {};
  const unsigned sv = (lane & 15) == blk ? ONES2 : 0u;
  const bf16x8 sel = __builtin_bit_cast(bf16x8, (u32x4){sv, sv, sv, sv});
  bf16x8 xf[2][2], wf[2][2];
  auto rdx = [&](ldsp sb, int s, int i) { xf[s & 1][i] = lds_ld16(sb + aoff[s] + i * 4096); };
  auto rdw = [&](ldsp sb, int s, int j) { wf[s & 1][j] = lds_tr2(sb + woff[j] + s * 4096, sb + woff[j] + s * 4096 + 1024); };
  auto mm = [&](int s, int j, int i) { acc[j][i] = mfma32(wf[s & 1][j], xf[s & 1][i], acc[j][i]); };
  auto step = [&](ldsp nb, int s, int sn, bool rd) {
    cs = mfma16(sel, wf[s & 1][0], cs);
    mm(s, 0, 0); if (rd) { rdx(nb, sn, 0); rdw(nb, sn, 0); } SB0();
    mm(s, 0, 1); if (rd) { rdx(nb, sn, 1); rdw(nb, sn, 1); } SB0();
    mm(s, 1, 0); SB0();
    mm(s, 1, 1); SB0();
  };
  bar();
  TS();
  ldsp sb = lds;
  rdx(sb, 0, 0); rdw(sb, 0, 0); rdx(sb, 0, 1); rdw(sb, 0, 1); SB0();
  int slot = 0;
#pragma unroll 1
  for (int t = 0; t < K1_NT - 1; ++t) {
    step(sb, 0, 1, true); step(sb, 1, 2, true); step(sb, 2, 3, true);
    slot = slot == K1_NS - 1 ? 0 : slot + 1;
    sb = lds + slot * K1_STG;
    bar(); SB0();
    step(sb, 3, 0, true);
  }
  step(sb, 0, 1, true); step(sb, 1, 2, true); step(sb, 2, 3, true);
  const LAS f32x2* rst = (const LAS f32x2*)(ctl + 1024);
  LAS float* cst = (LAS float*)(ctl + 2048);
  cs = mfma16(sel, wf[1][0], cs);
  mm(3, 0, 0); mm(3, 0, 1); SB0();
  if (lane < 16) { cst[wn * 64 + wm * 32 + lane] = cs[0]; cst[wn * 64 + wm * 32 + 16 + lane] = cs[1]; }
  mm(3, 1, 0); mm(3, 1, 1); SB0();
  TS();
  bar();
  TS();
  const rsrc_t rw = mk_rsrc(ws, WS_END);
  const float sc = nt < 5 ? QSCALE : 1.f;
  f32x4 c4[2][4];
#pragma unroll
  for (int j = 0; j < 2; ++j)
#pragma unroll
    for (int b = 0; b < 4; ++b) c4[j][b] = *(const LAS f32x4*)(cst + wn * 64 + (j ^ wm) * 32 + 8 * b + 4 * hi);
#pragma unroll
  for (int i = 0; i < 2; ++i) {
    const int ml = wm * 64 + i * 32 + r32;
    const f32x2 st = rst[ml];
    const float a = st.y * sc, nm = -st.x * a;
    const int ob = WS_QK + ((nt * 16 + wn * 8 + hi) * S_ + mt * 128 + ml) * 16;
#pragma unroll
    for (int j = 0; j < 2; ++j)
#pragma unroll
      for (int bp = 0; bp < 2; ++bp) {
        unsigned pk[2][2];
#pragma unroll
        for (int e = 0; e < 2; ++e) {
          const int b = 2 * bp + e;
          const f32x2 v0 = (f32x2){acc[j][i][4 * b], acc[j][i][4 * b + 1]} * a + nm * (f32x2){c4[j][b][0], c4[j][b][1]};
          const f32x2 v1 = (f32x2){acc[j][i][4 * b + 2], acc[j][i][4 * b + 3]} * a + nm * (f32x2){c4[j][b][2], c4[j][b][3]};
          pk[e][0] = cvtpk(v0[0], v0[1]); pk[e][1] = cvtpk(v1[0], v1[1]);
        }
        auto r0 = __builtin_amdgcn_permlane32_swap(pk[0][0], pk[1][0], false, false);
        auto r1 = __builtin_amdgcn_permlane32_swap(pk[0][1], pk[1][1], false, false);
        st16(rw, ob + ((j ^ wm) * 4 + bp * 2) * S_ * 16, (u32x4){r0[0], r1[0], r0[1], r1[1]});
      }
  }
  TS();
  if (w == 0) { TS_END(0); }
}

DI void qkv_stats(ldsp lds, int sw, int lane) {
  const int r32 = lane & 31, hi = lane >> 5;
  const int off = (sw * 32 + r32) * 128, x = (r32 >> 1) & 7;
  f32x2 s0 = {0.f, 0.f}, s1 = {0.f, 0.f}, q0 = {0.f, 0.f}, q1 = {0.f, 0.f};
  int slot = 0;
#pragma unroll 1
  for (int t = 0; t < K1_NT; ++t) {
    bar();
    ldsp sb = lds + slot * K1_STG + off;
    slot = slot == K1_NS - 1 ? 0 : slot + 1;
    u32x4 v[4];
#pragma unroll
    for (int c = 0; c < 4; ++c) v[c] = *(const LAS u32x4*)(sb + (((2 * c + hi) ^ x) * 16));
#pragma unroll
    for (int c = 0; c < 4; ++c)
#pragma unroll
      for (int e = 0; e < 4; e += 2) {
        const float a0 = bflo(v[c][e]), a1 = bfhi(v[c][e]), b0 = bflo(v[c][e + 1]), b1 = bfhi(v[c][e + 1]);
        asm("v_add_f32 %0, %0, %1" : "+v"(s0.x) : "v"(a0)); asm("v_add_f32 %0, %0, %1" : "+v"(s0.y) : "v"(a1));
        asm("v_add_f32 %0, %0, %1" : "+v"(s1.x) : "v"(b0)); asm("v_add_f32 %0, %0, %1" : "+v"(s1.y) : "v"(b1));
        asm("v_fmac_f32 %0, %1, %1" : "+v"(q0.x) : "v"(a0)); asm("v_fmac_f32 %0, %1, %1" : "+v"(q0.y) : "v"(a1));
        asm("v_fmac_f32 %0, %1, %1" : "+v"(q1.x) : "v"(b0)); asm("v_fmac_f32 %0, %1, %1" : "+v"(q1.y) : "v"(b1));
      }
  }
  const float mu = xsum32((s0[0] + s0[1]) + (s1[0] + s1[1])) * (1.f / D_);
  const float var = fmaxf(xsum32((q0[0] + q0[1]) + (q1[0] + q1[1])) * (1.f / D_) - mu * mu, 0.f);
  if (hi == 0) ((LAS f32x2*)(lds + K1_CTL + 1024))[sw * 32 + r32] = (f32x2){mu, __builtin_amdgcn_rsqf(var + LN_EPS)};
  asm volatile("s_waitcnt lgkmcnt(0)" ::: "memory");
}

DI void qkv_loader(ldsp lds, const u16* __restrict__ x, const u16* __restrict__ wqkv, int mt, int nt, int lw, int lane) {
  const bool isx = lw < 2;
  const int half = lw & 1;
  unsigned voff[8];
#pragma unroll
  for (int i = 0; i < 8; ++i) {
    const int r = (half * 8 + i) * 8 + (lane >> 3), c = (lane & 7) ^ ((r >> 1) & 7);
    const int k = (half * 8 + i) * 4 + (lane >> 4), cp = lane & 15;
    voff[i] = (isx ? r * 1280 + c * 16 : k * 3840 + (cp ^ ((k & 3) << 2)) * 16) + 8192 - i * 1024;
  }
  const char* src = (isx ? (const char*)x + (size_t)mt * 128 * 1280 : (const char*)wqkv + nt * 256) - 4096;
  const int sstr = isx ? 128 : 64 * 3840;
  const unsigned dst = (unsigned)(uintptr_t)lds + lw * 8192 + 4096;
  loader_loop<8, K1_NT, K1_NS, 0>([&](int slot) {
    set_m0(dst + slot * K1_STG);
    sfor<0, 8>([&](auto ic) { constexpr int i = decltype(ic)::value; dma<i * 1024 - 4096>(src, voff[i]); });
    src += sstr;
  }, [] {}, [] {});
}

extern "C" __global__ __launch_bounds__(768, 3) void k_qkv(const u16* __restrict__ x, const u16* __restrict__ wqkv, const u16* __restrict__ wo, const u16* __restrict__ wd, char* __restrict__ ws) {
  extern __shared__ __attribute__((aligned(16))) char smem[];
  ldsp lds = (ldsp)smem;
  const int lane = threadIdx.x & 63, w = __builtin_amdgcn_readfirstlane(threadIdx.x >> 6);
  const int xcd = blockIdx.x & 7, idx = blockIdx.x >> 3;
  const int nt = (xcd & 1) * 8 + (idx >> 2), mt = (xcd >> 1) * 4 + (idx & 3);
  if (w < 4 && nt < 15) { qkv_loader(lds, x, wqkv, mt, nt, w, lane); return; }
  if (w == 11 && lane < 2) ((unsigned*)(ws + WS_CNT))[(blockIdx.x * 2 + lane) * CSTR] = 0u;
  if (w == 10) {
    const char *c0, *c1;
    CODE_ADDR(c0, k_qkv); CODE_ADDR(c1, k_text_end);
    const unsigned lim = c1 > c0 ? (unsigned)(c1 - c0) : 0u, off = (idx * 16 + (lane & 15)) * 128;
    set_m0((unsigned)(uintptr_t)lds + K1_CTL + 3072 + 512);
    asm volatile("global_load_lds_dword %0, %1" ::"v"(off < lim ? off : lim), "s"(c0) : "memory");
  }
  if (nt == 15) {
    const rsrc_t rw = mk_rsrc(ws, WS_END);
    constexpr int NU = (200 + NWD_A + 191) / 192;
    auto iss = [&](int r) {
      const int u = min(mt * 12 + w + r * 192, 200 + NWD_A - 1), v = u - 200;
      if (u < 200) pack_issue<0>(wo, wo, u / 10, u % 10, (unsigned)(uintptr_t)lds + (w * 2 + (r & 1)) * 4096, lane);
      else pack_issue<2>(wd, wd, v / 10, v % 10, (unsigned)(uintptr_t)lds + (w * 2 + (r & 1)) * 4096, lane);
    };
    iss(0); iss(1);
#pragma unroll 1
    for (int r = 0; r < NU; ++r) {
      if (r == 0) wait_vm<4>(); else if (r + 1 < NU) wait_vm<8>(); else wait_vm<4>();
      const int u = min(mt * 12 + w + r * 192, 200 + NWD_A - 1), v = u - 200;
      if (u < 200) pack_finish<0>(lds + (w * 2 + (r & 1)) * 4096, rw, u / 10, u % 10, lane, nullptr);
      else pack_finish<2>(lds + (w * 2 + (r & 1)) * 4096, rw, v / 10, v % 10, lane, nullptr);
      asm volatile("s_waitcnt lgkmcnt(0)" ::: "memory");
      if (r + 2 < NU) iss(r + 2);
    }
    return;
  }
  if (w >= 8) qkv_stats(lds, w - 8, lane);
  else qkv_compute(lds, ws, mt, nt, w - 4, lane);
}

#define VLEAD 3
constexpr int K2_K = 102400, K2_V = K2_K + 24576, K2_VP = 8192 + 64, K2_CS = K2_V + 4 * K2_VP, K2_MSK = K2_CS + 2560, K2_FL = K2_MSK + 832, K2_LDS = K2_FL + 64 + 256;
static_assert(K2_LDS <= 163840 && K2_FL % 16 == 0, "LDS map");

DI void n80_epilogue_h(f32x4 (&acc)[5], rsrc_t rw, int mt, int ct, int w, int lane) {
  const int fr = lane & 15, g = lane >> 4, tok = mt * 64 + w * 16 + fr;
  const int odd = ct & 1, pb = (ct >> 1) * 5 + (odd ? 3 : 0), ps = (ct >> 1) * 5 + 2;
  const int hb = WS_H + (((mt >> 1) & 1) * 40 + (g >> 1)) * PLANE + (mt >> 2) * 4096 + ((mt & 1) * 2 + (w >> 1)) * 1024 + ((g & 1) * 32 + (w & 1) * 16 + fr) * 16;
  u32x2 hv[5];
  float s = 0.f;
#pragma unroll
  for (int j = 0; j < 5; ++j) hv[j] = (u32x2){cvtpk(acc[j][0], acc[j][1]), cvtpk(acc[j][2], acc[j][3])};
  const u32x2 a0 = odd ? hv[1] : hv[0], a1 = odd ? hv[2] : hv[1], b0 = odd ? hv[3] : hv[2], b1 = odd ? hv[4] : hv[3], s1 = odd ? hv[0] : hv[4];
  st16<AUX3>(rw, hb + pb * 2 * PLANE, (u32x4){a0[0], a0[1], a1[0], a1[1]});
  st16<AUX3>(rw, hb + (pb + 1) * 2 * PLANE, (u32x4){b0[0], b0[1], b1[0], b1[1]});
  st8<AUX3>(rw, hb + ps * 2 * PLANE + odd * 8, s1);
  f32x4 v[5];
#pragma unroll
  for (int j = 0; j < 5; ++j) { v[j] = (f32x4){bflo(hv[j][0]), bfhi(hv[j][0]), bflo(hv[j][1]), bfhi(hv[j][1])}; s += (v[j][0] + v[j][1]) + (v[j][2] + v[j][3]); }
  const float mean = xsum16_32(s) * (1.f / 80.f);
  float m2 = 0.f;
#pragma unroll
  for (int j = 0; j < 5; ++j) { const f32x4 d = v[j] - mean; m2 += (d[0] * d[0] + d[1] * d[1]) + (d[2] * d[2] + d[3] * d[3]); }
  m2 = xsum16_32(m2);
  if (g == 0) st8<AUX3>(rw, WS_ST + (tok * 8 + ct) * 8, (u32x2){__float_as_uint(mean), __float_as_uint(m2)});
}

DI void attn_helper(ldsp lds, char* __restrict__ ws, int c, int h, int hw, int lane, const u16* __restrict__ x, const u16* __restrict__ wg, const u16* __restrict__ wu, const u16* __restrict__ wd) {
  const unsigned lds0 = (unsigned)(uintptr_t)lds;
  const int pm = 5 * (h >> 1) + 3 * (h & 1), pt = 5 * (h >> 1) + 2;
  const bool EDGE = c == 0;
  const int tok0 = 256 * c - 256 + 128 * hw + ((EDGE && hw < 2) ? 256 : 0);
  const char* kb = ws + WS_QK + (80 * S_ + tok0) * 16;
  {
    set_m0(lds0 + K2_K + hw * 2048); dma<0>(kb + pm * S_ * 16, lane * 16); dma<1024>(kb + pm * S_ * 16, lane * 16);
    set_m0(lds0 + K2_K + 8192 + hw * 2048); dma<0>(kb + (pm + 1) * S_ * 16, lane * 16); dma<1024>(kb + (pm + 1) * S_ * 16, lane * 16);
    set_m0(lds0 + K2_K + 16384 + hw * 2048); dma<0>(kb + pt * S_ * 16, lane * 16); dma<1024>(kb + pt * S_ * 16, lane * 16);
  }
  TS_DECL;
  const int b = blockIdx.x, xcd = b & 7, idx = b >> 3;
  if (hw == 0 && lane < 8) *(LAS unsigned*)(lds + K2_FL + lane * 4) = 0u;
  {
    const unsigned tm = EDGE && hw < 2 ? 0xff80u : 0u;
    *(LAS u32x4*)(lds + K2_V + 3 * K2_VP + hw * 2048 + lane * 16) = (u32x4){tm, 0u, ONES2, ONES2};
    *(LAS u32x4*)(lds + K2_V + 3 * K2_VP + hw * 2048 + 1024 + lane * 16) = (u32x4){tm, 0u, ONES2, ONES2};
  }
  if (hw == 1 && lane < 52) {
    const int d = lane - 20;
    u32x4 mk;
#pragma unroll
    for (int e = 0; e < 4; ++e) { const int k0 = ((2 * e) & 3) + 8 * (e >> 1); mk[e] = (k0 > d ? 0xffffu : 0u) | (k0 + 1 > d ? 0xffff0000u : 0u); }
    *(LAS u32x4*)(lds + K2_MSK + lane * 16) = mk;
  }
  wait_vm<VLEAD>();
  {
    const char* vb = kb + 80 * S_ * 16;
    set_m0(lds0 + K2_V + hw * 2048); dma<0>(vb + pm * S_ * 16, lane * 16); dma<1024>(vb + pm * S_ * 16, lane * 16);
    set_m0(lds0 + K2_V + K2_VP + hw * 2048); dma<0>(vb + (pm + 1) * S_ * 16, lane * 16); dma<1024>(vb + (pm + 1) * S_ * 16, lane * 16);
    set_m0(lds0 + K2_V + 2 * K2_VP + hw * 2048); dma<0>(vb + pt * S_ * 16, lane * 16); dma<1024>(vb + pt * S_ * 16, lane * 16);
  }
  const rsrc_t rw = mk_rsrc(ws, WS_END);
  const int mt = xcd * 4 + (idx >> 3), ct = idx & 7;
  const int q0 = hw * 25600;
  const int bb = b, pcg = bb % 24, pks = (bb / 24) * 4 + hw, v0 = NWD_A + (b - 240) * 8 + hw * 2;
  wait_vm_bar<6>();
  if (b < 240) { pack_issue<1>(wg, wu, pks, pcg, lds0 + q0, lane); wait_vm<4>(); }
  else { pack_issue<2>(wd, wd, v0 / 10, v0 % 10, lds0 + q0, lane); pack_issue<2>(wd, wd, v0 / 10, v0 % 10 + 1, lds0 + q0 + 4096, lane); wait_vm<8>(); }
  if (lane == 0) *(volatile LAS unsigned*)(lds + K2_FL + 16 + hw * 4) = 1u;
  TS();
  u32x4 xr[2]; u32x2 xs;
  {
    const char* xp = (const char*)x + (size_t)(mt * 64 + hw * 16) * 1280 + ct * 160;
    const int g = lane >> 4;
    const unsigned o16 = (lane & 15) * 1280 + (g & 1) * 32 + (g >> 1) * 16, o8 = (lane & 15) * 1280 + g * 8;
    asm volatile("global_load_dwordx4 %0, %1, %2" : "=v"(xr[0]) : "v"(o16), "s"(xp) : "memory");
    asm volatile("global_load_dwordx4 %0, %1, %2 offset:64" : "=v"(xr[1]) : "v"(o16), "s"(xp) : "memory");
    asm volatile("global_load_dwordx2 %0, %1, %2 offset:128" : "=v"(xs) : "v"(o8), "s"(xp) : "memory");
  }
  dma_long<15>(ws + WS_WO + ct * 102400 + q0 + 10240, lds0 + q0 + 10240, lane * 16);
  wait_vm<15>();
  pin(xr[0]); pin(xr[1]); pin(xs);
  TS();
  if (b >= 240) {
    pack_finish<2>(lds + q0, rw, v0 / 10, v0 % 10, lane, nullptr); pack_finish<2>(lds + q0 + 4096, rw, v0 / 10, v0 % 10 + 1, lane, nullptr);
    asm volatile("s_waitcnt lgkmcnt(0)" ::: "memory");
  } else {
    LAS float* ct4 = (LAS float*)(lds + K2_CS);
    pack_finish<1>(lds + q0, rw, pks, pcg, lane, ct4 + hw * 128);
    asm volatile("s_waitcnt lgkmcnt(0)" ::: "memory");
    if (lds_add(lds + K2_FL, lane) == 3) {
      asm volatile("" ::: "memory");
      const LAS f32x2* c2 = (const LAS f32x2*)ct4 + lane;
      const f32x2 a = (c2[0] + c2[64]) + (c2[128] + c2[192]);
      const int e = pcg * 128 + lane * 2, ft = e / 192;
      st8(rw, WS_CS + ft * 8192 + ((bb / 24) * 192 + e - ft * 192) * 4, __builtin_bit_cast(u32x2, a));
    }
  }
  TS();
  dma_long<10>(ws + WS_WO + ct * 102400 + q0, lds0 + q0, lane * 16);
  if (hw == 3) {
    const char *c0, *c1;
    CODE_ADDR(c0, k_mlp); CODE_ADDR(c1, k_text_end);
    const unsigned lim = c1 > c0 ? (unsigned)(c1 - c0) : 0u, off = (idx * 8 + (lane & 7)) * 128;
    set_m0(lds0 + K2_FL + 64);
    asm volatile("global_load_lds_dword %0, %1" ::"v"(off < lim ? off : lim), "s"(c0) : "memory");
  }
  f32x4 acc[5];
#pragma unroll
  for (int jp = 0; jp < 2; ++jp) {
    auto r0 = __builtin_amdgcn_permlane16_swap(xr[jp][0], xr[jp][2], false, false);
    auto r1 = __builtin_amdgcn_permlane16_swap(xr[jp][1], xr[jp][3], false, false);
    acc[2 * jp] = (f32x4){bflo(r0[0]), bfhi(r0[0]), bflo(r1[0]), bfhi(r1[0])};
    acc[2 * jp + 1] = (f32x4){bflo(r0[1]), bfhi(r0[1]), bflo(r1[1]), bfhi(r1[1])};
  }
  acc[4] = (f32x4){bflo(xs[0]), bfhi(xs[0]), bflo(xs[1]), bfhi(xs[1])};
#pragma unroll
  for (int j = 0; j < 5; ++j) pin(acc[j]);
  wait_vm<0>();
  lds_add(lds + K2_FL + 8, lane);
  lds_wait(lds + K2_FL + 8, 4);
  TS();
  if (hw == 0) {
    lds_wait(lds + K2_FL + 4, 8);
    __builtin_amdgcn_s_sleep(PDLY);

    cnt_wait(ws, (mt * 8 + ct) >> CSH, 32);
 if (lane == 0) *(volatile LAS unsigned*)(lds + K2_FL + 12) = 1u; }
  else lds_wait(lds + K2_FL + 12, 1);
  TS();
  bf16x8 af[10][2], wf[2][5];
  f32x4 ac1[5];
#pragma unroll
  for (int j = 0; j < 5; ++j) ac1[j] = (f32x4){0.f, 0.f, 0.f, 0.f};
  const int ao = WS_QK + ((hw & 1) * 4 + (lane >> 4)) * PLANE + (mt * 64 + hw * 16 + (lane & 15)) * 16;
#pragma unroll
  for (int t = 0; t < 10; ++t) { af[t][0] = ld16_sc1(rw, ao, t * 8 * PLANE); af[t][1] = ld16_sc1(rw, ao ^ 256, t * 8 * PLANE); }
  SB0();
  ldsp wp = lds + (hw & 1) * 5120 + lane * 16;
#pragma unroll
  for (int j = 0; j < 5; ++j) wf[0][j] = lds_ld16(wp + j * 1024);
#pragma unroll
  for (int t = 0; t < 10; ++t) {
#pragma unroll
    for (int j = 0; j < 5; ++j) {
      if (t + 1 < 10) wf[(t + 1) & 1][j] = lds_ld16(wp + (t + 1) * 10240 + j * 1024);
      ac1[j] = mfma16(wf[t & 1][j], af[t][1], ac1[j]);
    }
#pragma unroll
    for (int j = 0; j < 5; ++j) acc[j] = mfma16(wf[t & 1][j], af[t][0], acc[j]);
    SB0();
  }
  lds_wait(lds + K2_FL + 4, 8);
#pragma unroll
  for (int j = 0; j < 5; ++j) *(LAS f32x4*)(lds + K2_K + (hw * 5 + j) * 1024 + lane * 16) = ac1[j];
  bar();
#pragma unroll
  for (int j = 0; j < 5; ++j) acc[j] += *(const LAS f32x4*)(lds + K2_K + ((hw ^ 1) * 5 + j) * 1024 + lane * 16);
  TS();
  n80_epilogue_h(acc, rw, mt, ct, hw, lane);
  TS();
  if (hw == 1) { TS_END(2); }
}

DI void attn_body(ldsp lds, char* __restrict__ ws, int c, int h, int w, int lane) {
  const int r32 = lane & 31, hi = lane >> 5;
  TS_DECL;
  const int pm = 5 * (h >> 1) + 3 * (h & 1), pt = 5 * (h >> 1) + 2;
  const int dq = r32 - 4 * hi;
  const float NEG = -__builtin_inff();
  f32x16 s[8], s8;
#pragma unroll
  for (int r = 0; r < 16; ++r) {
    const bool le = (r & 3) + 8 * (r >> 2) <= dq;
    s8[r] = le ? 0.f : NEG;
    s[0][r] = le ? NEG : 0.f;
  }
  pin(s[0]); pin(s8);
  SB0();
  const char* qb = ws + WS_QK + (256 * c + 32 * w) * 16;
  const bf16x8 qm = *(const bf16x8*)(qb + ((pm + hi) * S_ + r32) * 16);
  u32x2 qtr = *(const u32x2*)(qb + (pt * S_ + r32) * 16 + (h & 1) * 8);
  if (hi) qtr = (u32x2){0x3f80u, 0u};
  bf16x4 qt = __builtin_bit_cast(bf16x4, qtr);
  pin(qt);
  TS();
  bar();
  TS();
  ldsp kp = lds + K2_K + hi * 8192 + (w * 32 + r32) * 16;
  ldsp ktp = lds + (hi ? K2_V + 3 * K2_VP + w * 512 : K2_K + 16384 + (w * 32 + r32) * 16 + (h & 1) * 8);
  const int vq = (lane & 15) >> 2, vpp = lane & 3, vblk = (lane >> 4) & 1;
  ldsp vp = lds + K2_V + (w * 32 + 4 * hi + vq) * 16 + (vblk == 0 ? (vpp >> 1) * K2_VP + (vpp & 1) * 8 : vpp == 0 ? 2 * K2_VP + (h & 1) * 8 : 3 * K2_VP + 8);
  bf16x8 km[9]; bf16x4 kt[9];
#pragma unroll
  for (int jj = 0; jj < 9; ++jj) { const int j = jj == 0 ? 8 : jj - 1; km[j] = lds_ld16(kp + j * 512); kt[j] = *(const LAS bf16x4*)(ktp + j * 512); }
  SB0();
  auto tail = [&](int j, f32x16 cin) { return __builtin_amdgcn_mfma_f32_32x32x8bf16_1k(kt[j], qt, cin, 0, 0, 0); };
  float mx[4];
  auto maxh = [&](int j, int r0) {
#pragma unroll
    for (int e = 0; e < 4; ++e) mx[e] = __builtin_elementwise_maximum(__builtin_elementwise_maximum(mx[e], s[j][r0 + 2 * e]), s[j][r0 + 2 * e + 1]);
  };
  s8 = mfma32(km[8], qm, s8); s[0] = mfma32(km[0], qm, s[0]); s8 = tail(8, s8);
  s[1] = mfma32(km[1], qm, (f32x16){}); s[0] = tail(0, s[0]);
  s[2] = mfma32(km[2], qm, (f32x16){}); s[1] = tail(1, s[1]); SB0();
  s[3] = mfma32(km[3], qm, (f32x16){});
#pragma unroll
  for (int r = 0; r < 8; ++r) s[0][r] = __builtin_elementwise_maximum(s[0][r], s8[r]);
  SB0();
  s[2] = tail(2, s[2]);
#pragma unroll
  for (int r = 8; r < 16; ++r) s[0][r] = __builtin_elementwise_maximum(s[0][r], s8[r]);
  pin(s[0]);
  SB0();
#pragma unroll
  for (int e = 0; e < 4; ++e) mx[e] = s[0][e];
  s[4] = mfma32(km[4], qm, (f32x16){}); maxh(0, 4); mx[0] = __builtin_elementwise_maximum(__builtin_elementwise_maximum(mx[0], s[0][12]), s[0][13]);
  mx[1] = __builtin_elementwise_maximum(__builtin_elementwise_maximum(mx[1], s[0][14]), s[0][15]); SB0();
  s[3] = tail(3, s[3]); maxh(1, 0); SB0();
  s[5] = mfma32(km[5], qm, (f32x16){}); maxh(1, 8); SB0();
  s[4] = tail(4, s[4]); maxh(2, 0); SB0();
  s[6] = mfma32(km[6], qm, (f32x16){}); maxh(2, 8); SB0();
  s[5] = tail(5, s[5]); maxh(3, 0); SB0();
  s[7] = mfma32(km[7], qm, (f32x16){}); maxh(3, 8); SB0();
  s[6] = tail(6, s[6]); maxh(4, 0); SB0();
  s[7] = tail(7, s[7]); maxh(4, 8);
#pragma unroll
  for (int j = 5; j < 8; ++j) { maxh(j, 0); maxh(j, 8); }
  const float m0 = __builtin_elementwise_maximum(mx[0], mx[1]), m1 = __builtin_elementwise_maximum(mx[2], mx[3]);
  float m = __builtin_elementwise_maximum(m0, m1);
  {
    auto rr = __builtin_amdgcn_permlane32_swap(__float_as_uint(m), __float_as_uint(m), false, false);
    m = __builtin_elementwise_maximum(__uint_as_float(rr[0]), __uint_as_float(rr[1]));
  }
  bf16x4 nm, one4;
  {
    const unsigned u0 = __float_as_uint(m) & 0xffff0000u;
    const float r1 = m - __uint_as_float(u0);
    const unsigned u1 = __float_as_uint(r1) & 0xffff0000u;
    const float r2 = r1 - __uint_as_float(u1);
    const u32x2 v = {hi ? 0u : ((u0 >> 16) | u1), hi ? 0u : (__float_as_uint(r2) >> 16)};
    nm = __builtin_bit_cast(bf16x4, v);
    one4 = __builtin_bit_cast(bf16x4, (u32x2){0xbf80bf80u, 0xbf80bf80u});
  }
  SB0();
  TS();
  f32x16 o = {};
  u32x4 pk[8][2];
  auto sub = [&](int j) { s[j] = __builtin_amdgcn_mfma_f32_32x32x8bf16_1k(one4, nm, s[j], 0, 0, 0); };
  auto ex8 = [&](int j, int ks) {
#pragma unroll
    for (int e = 0; e < 4; ++e) pk[j][ks][e] = cvtpk(__builtin_amdgcn_exp2f(s[j][8 * ks + 2 * e]), __builtin_amdgcn_exp2f(s[j][8 * ks + 2 * e + 1]));
  };
  auto vld = [&](int slot, int ks) { return lds_tr2(vp + slot * 512 + ks * 256, vp + slot * 512 + ks * 256 + 128); };
  auto pv = [&](bf16x8 v, u32x4 p) { o = mfma32(v, __builtin_bit_cast(bf16x8, p), o); };
  sub(7); sub(6); sub(5);
  __builtin_amdgcn_s_setprio(0);
  ex8(7, 0); ex8(7, 1);
  const u32x4 vrdy = *(volatile LAS u32x4*)(lds + K2_FL + 16);
  ex8(6, 0); ex8(6, 1);
  pin(pk[7][0]); pin(pk[7][1]); pin(pk[6][0]); pin(pk[6][1]);
  if (__builtin_expect(__builtin_amdgcn_readfirstlane((vrdy[0] & vrdy[1]) & (vrdy[2] & vrdy[3])) == 0, 0)) {
    for (int sp = 0; sp < (1 << 20); ++sp) {
      const u32x4 v = *(volatile LAS u32x4*)(lds + K2_FL + 16);
      if (__builtin_amdgcn_readfirstlane((v[0] & v[1]) & (v[2] & v[3]))) break;
      __builtin_amdgcn_s_sleep(1);
    }
  }
  asm volatile("" ::: "memory");
  bf16x8 vf[9][2];
  vf[7][0] = vld(7, 0); vf[7][1] = vld(7, 1);
  SB0();
  u32x4 mk[2];
#pragma unroll
  for (int j = 5; j >= 0; --j) {
    vf[j + 1][0] = vld(j + 1, 0); vf[j + 1][1] = vld(j + 1, 1);
    if (j == 0) { vf[0][0] = vld(0, 0); vf[8][0] = vld(8, 0); vf[0][1] = vld(0, 1); vf[8][1] = vld(8, 1); }
    if (j == 1) { mk[0] = *(const LAS u32x4*)(lds + K2_MSK + (dq + 20) * 16); mk[1] = *(const LAS u32x4*)(lds + K2_MSK + (dq + 4) * 16); }
    if (j > 0) sub(j - 1);
    SB0();
    ex8(j, 0); pv(vf[j + 2][0], pk[j + 2][0]); SB0();
    ex8(j, 1); pv(vf[j + 2][1], pk[j + 2][1]); SB0();
  }
  pv(vf[1][0], pk[1][0]); pv(vf[1][1], pk[1][1]);
#pragma unroll
  for (int ks = 0; ks < 2; ++ks) {
    const u32x4 p0 = pk[0][ks] & mk[ks], p8 = pk[0][ks] ^ p0;
    pv(vf[0][ks], p0); pv(vf[8][ks], p8);
  }
  TS();
  const float l = __uint_as_float(__builtin_amdgcn_permlane32_swap(__float_as_uint(o[8]), __float_as_uint(o[8]), false, false)[1]);
  const float il = __builtin_amdgcn_rcpf(l);
  const int tok = 256 * c + 32 * w + r32;
  const rsrc_t rw = mk_rsrc(ws, WS_END);
  const u32x4 w0 = {cvtpk(o[0] * il, o[1] * il), cvtpk(o[2] * il, o[3] * il), cvtpk(o[4] * il, o[5] * il), cvtpk(o[6] * il, o[7] * il)};
  st16(rw, WS_QK + (pm + hi) * PLANE + tok * 16, w0);
  if (hi == 0) st8(rw, WS_QK + pt * PLANE + tok * 16 + (h & 1) * 8, (u32x2){cvtpk(o[8] * il, o[9] * il), cvtpk(o[10] * il, o[11] * il)});
  TS();
  wait_vm<0>();
  if (lds_add(lds + K2_FL + 4, lane) == 7 && lane < (32 >> CSH)) cnt_add(ws, (c * 32 >> CSH) + lane);
  if (w == 7 || w == 0) { TS_END(w == 7 ? 1 : 5); }
}

extern "C" __global__ __launch_bounds__(768, 3) void k_attn(char* __restrict__ ws, const u16* __restrict__ x, const u16* __restrict__ wg, const u16* __restrict__ wu, const u16* __restrict__ wd) {
  extern __shared__ __attribute__((aligned(16))) char smem[];
  const int lane = threadIdx.x & 63, w = __builtin_amdgcn_readfirstlane(threadIdx.x >> 6);
  const int xcd = blockIdx.x & 7, idx = blockIdx.x >> 3, h = xcd * 4 + (idx >> 3), c = idx & 7;
  if (w < 4) attn_helper((ldsp)smem, ws, c, h, w, lane, x, wg, wu, wd);
  else attn_body((ldsp)smem, ws, c, h, w - 4, lane);
}

constexpr int K4_NS = 4, K4_STG = 20480, K4_NT = 20, K4_RING = 61440, K4_CTL = K4_RING + K4_NS * K4_STG, K4_SD = K4_CTL + 2048, K4_LDS = K4_SD + 8192 + 8192;

extern "C" __global__ __launch_bounds__(512, 2) void k_mlp(char* __restrict__ ws, u16* __restrict__ out) {
  extern __shared__ __attribute__((aligned(16))) char smem[];
  ldsp lds = (ldsp)smem + K4_RING;
  ldsp ctl = (ldsp)smem + K4_CTL;
  const int lane = threadIdx.x & 63, w = __builtin_amdgcn_readfirstlane(threadIdx.x >> 6);
  const unsigned lds00 = (unsigned)(uintptr_t)(ldsp)smem, lds0 = lds00 + K4_RING;
  const int xcd = blockIdx.x & 7, idx = blockIdx.x >> 3, mt = (xcd >> 2) * 8 + (idx >> 2), ft = (xcd & 3) * 4 + (idx & 3);
  const int mt5 = (xcd >> 1) * 8 + (idx >> 2), ct = (xcd & 1) * 4 + (idx & 3);
  const int odd = ct & 1, pb = (ct >> 1) * 5 + (odd ? 3 : 0), ps = (ct >> 1) * 5 + 2;
  if (w >= 4) {
    const int lw = w - 4;
    const char* wd = ws + WS_WD + ct * 245760;
    {
      const bool isa = w < 6;
      const char* src = isa ? ws + WS_H + ((mt & 1) * 40 + lw) * PLANE + (mt >> 1) * 4096 : ws + WS_WGU + ft * K4_NT * 12288 + (w - 6) * 6144;
      const unsigned dst = isa ? lds0 + lw * 4096 : lds0 + 8192 + (w - 6) * 6144;
      auto one = [] {};
      constexpr int XP = 0;
      if (isa) loader_loop<4 + XP, K4_NT, K4_NS, 0>([&](int slot) { dma_run<4>(src, dst + slot * K4_STG, lane * 16); src += 2 * PLANE; one(); },
                                               [&] { dma_run<4>(ws + WS_ST + mt * 8192 + lw * 4096, lds00 + K4_SD + lw * 4096, lane * 16); }, [] {});
      else loader_loop<6 + XP, K4_NT, K4_NS, 0>([&](int slot) { dma_run<6>(src, dst + slot * K4_STG, lane * 16); src += 12288; one(); },
                                           [&] { dma_run<4>(ws + WS_CS + ft * 8192 + (w - 6) * 4096, lds00 + K4_SD + 8192 + (w - 6) * 4096, lane * 16); }, [] {});
    }
    dma_long<15>(wd + lw * 15360, lds00 + lw * 15360, lane * 16);
    {
      set_m0(lds00 + K4_SD + lw * 3072);
      const char* hs = ws + WS_H + ((mt5 >> 1) & 1) * 40 * PLANE + (mt5 >> 2) * 4096 + ((mt5 & 1) * 2 + (lw >> 1)) * 1024;
      const unsigned vo = (lane >> 5) * PLANE + (((lane >> 4) & 1) * 32 + (lw & 1) * 16 + (lane & 15)) * 16 + 2048;
      dma<0>(hs + pb * 2 * PLANE - 2048, vo); dma<1024>(hs + (pb + 1) * 2 * PLANE - 3072, vo); dma<2048>(hs + ps * 2 * PLANE - 4096, vo);
    }
    lds_wait(ctl, 4);
    dma_long<20>(wd + 61440 + lw * 20480, lds0 + lw * 20480, lane * 16);
    wait_vm_bar<0>();
#pragma unroll 1
    for (int g = 0; g < 5; ++g) {
      lds_wait4(ctl + 16, g + 1);
      dma_run<5>(wd + 143360 + g * 20480 + lw * 5120, lds00 + g * 20480 + lw * 5120, lane * 16);
      wait_vm<0>();
      lds_post(ctl + 32 + lw * 4, g + 1, lane);
    }
    return;
  }
  if (threadIdx.x < 12) ((LAS unsigned*)ctl)[threadIdx.x] = 0u;
  const int r32 = lane & 31, hi = lane >> 5, wm = w >> 1, wn = w & 1;
  TS_DECL;
  f32x16 acc[2][3];
#pragma unroll
  for (int i = 0; i < 2; ++i)
#pragma unroll
    for (int j = 0; j < 3; ++j) { acc[i][j] = (f32x16){}; pin(acc[i][j]); }
  bf16x8 af[2][2], wf[2][3];
  const int aoff = wm * 2048 + lane * 16, woff = 8192 + wn * 3072 + lane * 16;
  auto rda = [&](ldsp sb, int s, int i) { af[s][i] = lds_ld16(sb + aoff + s * 4096 + i * 1024); };
  auto rdw = [&](ldsp sb, int s, int j) { wf[s][j] = lds_ld16(sb + woff + s * 6144 + j * 1024); };
  auto mm = [&](int s, int i, int j) { acc[i][j] = mfma32(wf[s][j], af[s][i], acc[i][j]); };
  bar();
  const int tok = mt * 128 + wm * 64 + r32;
  f32x4 sv[2][2];
  float cv[10];
  const LAS float* cp = (const LAS float*)((ldsp)smem + K4_SD + 8192) + w * 48 + (lane < 48 ? lane : 47);
  ldsp sp = (ldsp)smem + K4_SD + (wm * 64 + r32) * 64 + hi * 32;
  TS();
  rda(lds, 0, 0); rdw(lds, 0, 0); rda(lds, 0, 1); rdw(lds, 0, 1); rdw(lds, 0, 2); SB0();
  constexpr float GC0 = -2.f * 0.7978845608028654f * 1.4426950408889634f, GC1 = GC0 * 0.044715f;
  float ca[2], cb[2], nmu[2], var[2];
  sfor<0, K4_NT>([&](auto tc) {
    constexpr int t = decltype(tc)::value;
    constexpr bool more = t + 1 < K4_NT;
    ldsp sb = lds + (t % K4_NS) * K4_STG;
    ldsp nb = lds + ((t + 1) % K4_NS) * K4_STG;
    mm(0, 0, 0); rda(sb, 1, 0); rdw(sb, 1, 0); SB0();
    mm(0, 1, 0); rda(sb, 1, 1); rdw(sb, 1, 1); SB0();
    mm(0, 0, 1); rdw(sb, 1, 2); SB0();
    mm(0, 1, 1); SB0();
    mm(0, 0, 2); SB0();
    mm(0, 1, 2); SB0();
    if (more) { bar(); SB0(); }
    else { asm volatile("s_waitcnt lgkmcnt(0)" ::: "memory"); lds_add(ctl, lane); SB0(); }
    mm(1, 0, 0); if (more) { rda(nb, 0, 0); rdw(nb, 0, 0); } SB0();
    mm(1, 1, 0); if (more) { rda(nb, 0, 1); rdw(nb, 0, 1); } SB0();
    mm(1, 0, 1); if (more) rdw(nb, 0, 2); SB0();
    if constexpr (t == 3) { cv[8] = cp[8 * 192]; cv[9] = cp[9 * 192]; }
    if constexpr (t >= 4 && t <= 7) { cv[2 * t - 8] = cp[(2 * t - 8) * 192]; cv[2 * t - 7] = cp[(2 * t - 7) * 192]; sv[(t - 4) >> 1][t & 1] = *(const LAS f32x4*)(sp + ((t - 4) >> 1) * 2048 + (t & 1) * 16); }
    if constexpr (t == 8) { if (lane < 48) ((LAS float*)(ctl + 1024))[w * 48 + lane] = (((cv[0] + cv[1]) + (cv[2] + cv[3])) + ((cv[4] + cv[5]) + (cv[6] + cv[7]))) + (cv[8] + cv[9]); }
    if constexpr (t == 9 || t == 10) {
      constexpr int i = t - 9;
      const f32x4 s0 = sv[i][0], s1 = sv[i][1];
      const float mu = xsum32((s0[0] + s0[2]) + (s1[0] + s1[2])) * 0.125f;
      const float d0 = s0[0] - mu, d1 = s0[2] - mu, d2 = s1[0] - mu, d3 = s1[2] - mu;
      var[i] = xsum32((s0[1] + s0[3]) + (s1[1] + s1[3]) + 80.f * ((d0 * d0 + d1 * d1) + (d2 * d2 + d3 * d3))) * (1.f / D_) + LN_EPS;
      const float rstd = __builtin_amdgcn_rsqf(var[i]);
      ca[i] = GC0 * rstd; cb[i] = GC1 * rstd * rstd * rstd; nmu[i] = -mu;
    }
    mm(1, 1, 1);
    mm(1, 0, 2);
    mm(1, 1, 2);
    if constexpr (t == 9 || t == 10) { pin(ca[t - 9]); pin(cb[t - 9]); pin(nmu[t - 9]); pin(var[t - 9]); }
    SB0();
  });
  TS();
  const rsrc_t rw = mk_rsrc(ws, WS_END);
  const LAS float* csb = (const LAS float*)(ctl + 1024) + wn * 96 + 4 * hi;
  f32x4 cq[3][4];
#pragma unroll
  for (int j = 0; j < 3; ++j)
#pragma unroll
    for (int b = 0; b < 4; ++b) cq[j][b] = *(const LAS f32x4*)(csb + j * 32 + 8 * b);
#pragma unroll
  for (int i = 0; i < 2; ++i) {
    const int tk = tok + 32 * i;
    const int ob = WS_ACT + (((tk >> 6) * 48 + ft * 3) * 4 + ((tk >> 4) & 3)) * 1024 + (hi * 16 + (tk & 15)) * 16;
#pragma unroll
    for (int j = 0; j < 3; ++j) {
      unsigned pk[4];
#pragma unroll
      for (int r = 0; r < 8; r += 2) {
        const f32x2 gv = (f32x2){acc[i][j][r], acc[i][j][r + 1]} + nmu[i] * (f32x2){cq[j][r >> 2][r & 3], cq[j][r >> 2][(r & 3) + 1]};
        const f32x2 uv = (f32x2){acc[i][j][8 + r], acc[i][j][9 + r]} + nmu[i] * (f32x2){cq[j][2 + (r >> 2)][r & 3], cq[j][2 + (r >> 2)][(r & 3) + 1]};
        const f32x2 arg = gv * (gv * gv * cb[i] + ca[i]);
        const f32x2 ex = {__builtin_amdgcn_exp2f(arg[0]), __builtin_amdgcn_exp2f(arg[1])};
        const f32x2 den = ex * var[i] + var[i];
        const f32x2 rc = {__builtin_amdgcn_rcpf(den[0]), __builtin_amdgcn_rcpf(den[1])};
        const f32x2 a = gv * uv * rc;
        pk[r >> 1] = cvtpk(a[0], a[1]);
      }
      const int T = wn * 3 + j;
      st16<AUX4>(rw, ob + (T >> 1) * 4096 + (T & 1) * 512, (u32x4){pk[0], pk[1], pk[2], pk[3]});
    }
  }
  TS();
  wait_vm<0>();
  if (lds_add(ctl + 4 + wm * 4, lane) == 1 && lane < (8 >> CSH)) cnt_add(ws, 256 + ((mt * 2 + wm) * 8 >> CSH) + lane);
  TS();
  if (w == 0) { __builtin_amdgcn_s_sleep(PDLY); cnt_wait(ws, 256 + ((mt5 * 8 + ct) >> CSH), 16); if (lane == 0) *(volatile LAS unsigned*)(ctl + 12) = 1u; }
  else lds_wait(ctl + 12, 1);
  TS();
  constexpr int WIN = WIN5;
  bf16x8 a5[WIN][2], w5[2][5];
  const int ao = WS_ACT + mt5 * 196608 + (w & 1) * 4096 + w * 1024 + lane * 16;
#pragma unroll
  for (int t = 0; t < WIN; ++t) { a5[t][0] = ld16_sc1(rw, ao, t * 8192); a5[t][1] = ld16_sc1(rw, ao ^ 1024, t * 8192); }
  SB0();
  bar();
  TS();
  f32x4 ac5[5];
  {
    ldsp rp = (ldsp)smem + K4_SD + w * 3072 + lane * 16;
#pragma unroll
    for (int j = 0; j < 5; ++j) {
      const int e = odd ? (j == 0 ? 5 : j - 1) : j;
      const u32x2 r = *(const LAS u32x2*)(rp + (e >> 1) * 1024 + (e & 1) * 8);
      ac5[j] = (f32x4){bflo(r[0]), bfhi(r[0]), bflo(r[1]), bfhi(r[1])};
    }
  }
  f32x4 ad5[5];
#pragma unroll
  for (int j = 0; j < 5; ++j) ad5[j] = (f32x4){0.f, 0.f, 0.f, 0.f};
  ldsp wp = (ldsp)smem + (w & 1) * 5120 + lane * 16;
#pragma unroll
  for (int j = 0; j < 5; ++j) w5[0][j] = lds_ld16(wp + j * 1024);
  sfor<0, 24>([&](auto tc) {
    constexpr int t = decltype(tc)::value, tn = t + 1;
    if constexpr (tn >= 14 && tn < 24 && (tn & 1) == 0) { lds_wait4(ctl + 32, (tn - 14) / 2 + 1); SB0(); }
    if constexpr (tn == REL0 || tn == REL1 || tn == REL2 || tn == REL3 || tn == REL4) {
      asm volatile("" ::: "memory");
      lds_post(ctl + 16 + w * 4, tn == REL0 ? 1 : tn == REL1 ? 2 : tn == REL2 ? 3 : tn == REL3 ? 4 : 5, lane);
    }
#pragma unroll
    for (int j = 0; j < 5; ++j) {
      if constexpr (tn < 24) w5[tn & 1][j] = lds_ld16(wp + (tn % 14) * 10240 + j * 1024);
      ad5[j] = mfma16(w5[t & 1][j], a5[t % WIN][1], ad5[j]);
    }
#pragma unroll
    for (int j = 0; j < 5; ++j) ac5[j] = mfma16(w5[t & 1][j], a5[t % WIN][0], ac5[j]);
    if constexpr (t + WIN < 24) { a5[t % WIN][0] = ld16_sc1(rw, ao, (t + WIN) * 8192); a5[t % WIN][1] = ld16_sc1(rw, ao ^ 1024, (t + WIN) * 8192); }
    SB0();
  });
  TS();
#pragma unroll
  for (int j = 0; j < 5; ++j) *(LAS f32x4*)((ldsp)smem + 102400 + (w * 5 + j) * 1024 + lane * 16) = ad5[j];
  bar();
#pragma unroll
  for (int j = 0; j < 5; ++j) ac5[j] += *(const LAS f32x4*)((ldsp)smem + 102400 + ((w ^ 1) * 5 + j) * 1024 + lane * 16);
  TS();
  {
    const int g = lane >> 4;
    u32x2 hv[5];
#pragma unroll
    for (int j = 0; j < 5; ++j) hv[j] = (u32x2){cvtpk(ac5[j][0], ac5[j][1]), cvtpk(ac5[j][2], ac5[j][3])};
    const rsrc_t ro = mk_rsrc(out, S_ * D_ * 2);
    const int oo = (mt5 * 64 + w * 16 + (lane & 15)) * 1280 + ct * 160;
#pragma unroll
    for (int jp = 0; jp < 2; ++jp) {
      auto r0 = __builtin_amdgcn_permlane16_swap(hv[2 * jp][0], hv[2 * jp + 1][0], false, false);
      auto r1 = __builtin_amdgcn_permlane16_swap(hv[2 * jp][1], hv[2 * jp + 1][1], false, false);
      st16<AUX5>(ro, oo + jp * 64 + (g & 1) * 32 + (g >> 1) * 16, (u32x4){r0[0], r1[0], r0[1], r1[1]});
    }
    st8<AUX5>(ro, oo + 128 + g * 8, hv[4]);
  }
  TS();
  if (w == 0) { TS_END(3); }
}

extern "C" __global__ void k_text_end() {}

extern "C" void kernel_launch(void* const* d_in, const int* in_sizes, int n_in, void* d_out, int out_size, void* d_ws, size_t ws_size,
                              hipStream_t stream) {
  const u16* x = (const u16*)d_in[0];
  const u16* wqkv = (const u16*)d_in[1];
  const u16* wo = (const u16*)d_in[2];
  const u16* wg = (const u16*)d_in[4];
  const u16* wu = (const u16*)d_in[5];
  const u16* wd = (const u16*)d_in[6];
  char* ws = (char*)d_ws;
  u16* out = (u16*)d_out;
  (void)hipFuncSetAttribute((const void*)k_qkv, hipFuncAttributeMaxDynamicSharedMemorySize, K1_LDS);
  (void)hipFuncSetAttribute((const void*)k_attn, hipFuncAttributeMaxDynamicSharedMemorySize, K2_LDS);
  (void)hipFuncSetAttribute((const void*)k_mlp, hipFuncAttributeMaxDynamicSharedMemorySize, K4_LDS);
  k_qkv<<<256, 768, K1_LDS, stream>>>(x, wqkv, wo, wd, ws);
  k_attn<<<256, 768, K2_LDS, stream>>>(ws, x, wg, wu, wd);
  k_mlp<<<256, 512, K4_LDS, stream>>>(ws, out);
}
```

```cpp
#include <hip/hip_runtime.h>
#include <stdint.h>
#include <type_traits>

#define LAS __attribute__((address_space(3)))
#define DI __device__ __forceinline__
typedef unsigned short u16;
typedef short bf16x8 __attribute__((ext_vector_type(8)));
typedef short bf16x4 __attribute__((ext_vector_type(4)));
typedef __bf16 bf16x2_t __attribute__((ext_vector_type(2)));
typedef float f32x2 __attribute__((ext_vector_type(2)));
typedef float f32x4 __attribute__((ext_vector_type(4)));
typedef float f32x16 __attribute__((ext_vector_type(16)));
typedef unsigned u32x4 __attribute__((ext_vector_type(4)));
typedef unsigned u32x2 __attribute__((ext_vector_type(2)));
typedef LAS char* ldsp;
typedef __amdgpu_buffer_rsrc_t rsrc_t;

constexpr int S_ = 2048, D_ = 640, NH = 32, DF = 1536;
constexpr float LN_EPS = 1e-5f;
constexpr float QSCALE = 0.22360679774997896f * 1.4426950408889634f;
constexpr unsigned ONES2 = 0x3f803f80u;
#define AUX_ST 16

constexpr int PLANE = S_ * 16;
constexpr int WS_QK = 0;
constexpr int WS_ACT = WS_QK + 80 * PLANE;
constexpr int WS_H = WS_ACT + S_ * DF * 2;
constexpr int WS_WO = WS_H + S_ * D_ * 2;
constexpr int WS_ST = WS_WO + D_ * D_ * 2;
constexpr int WS_CS = WS_ST + S_ * 8 * 8;
constexpr int WS_WGU = WS_CS + 16 * 8192;
constexpr int WS_WD = WS_WGU + D_ * DF * 4;
static_assert(WS_WO >= 0 && WS_QK % 4096 == 0, "workspace map");
constexpr int WS_CNT = WS_WD + D_ * DF * 2;
#define CSTR 64
constexpr int WS_END = WS_CNT + 512 * CSTR * 4;

#define TS_DECL
#define TS()
#define TS_END(kid)

template <int I, int N, class F> DI void sfor(F&& f) {
  if constexpr (I < N) { f(std::integral_constant<int, I>{}); sfor<I + 1, N>(f); }
}
#define SB0() __builtin_amdgcn_sched_barrier(0)
DI unsigned cvtpk(float lo, float hi) { f32x2 v = {lo, hi}; return __builtin_bit_cast(unsigned, __builtin_convertvector(v, bf16x2_t)); }
DI float bflo(unsigned u) { return __uint_as_float(u << 16); }
DI float bfhi(unsigned u) { return __uint_as_float(u & 0xffff0000u); }
DI float dot2(unsigned a, unsigned b, float c) {
  return __builtin_amdgcn_fdot2_f32_bf16(__builtin_bit_cast(bf16x2_t, a), __builtin_bit_cast(bf16x2_t, b), c, false);
}
DI f32x4 mfma16(bf16x8 a, bf16x8 b, f32x4 c) { return __builtin_amdgcn_mfma_f32_16x16x32_bf16(a, b, c, 0, 0, 0); }
DI f32x16 mfma32(bf16x8 a, bf16x8 b, f32x16 c) { return __builtin_amdgcn_mfma_f32_32x32x16_bf16(a, b, c, 0, 0, 0); }
DI void set_m0(unsigned v) { asm volatile("s_mov_b32 m0, %0\n\ts_nop 0" ::"s"(v) : "memory"); }
template <int OFF> DI void dma(const void* sbase, unsigned voff) {
  asm volatile("global_load_lds_dwordx4 %0, %1 offset:%2" ::"v"(voff), "s"(sbase), "n"(OFF) : "memory");
}
template <int OFF> DI void dma_nt(const void* sbase, unsigned voff) {
  asm volatile("global_load_lds_dwordx4 %0, %1 offset:%2 nt" ::"v"(voff), "s"(sbase), "n"(OFF) : "memory");
}
template <int OFF> DI void gld16(u32x4& d, const void* sbase, unsigned voff) {
  asm volatile("global_load_dwordx4 %0, %1, %2 offset:%3" : "=v"(d) : "v"(voff), "s"(sbase), "n"(OFF) : "memory");
}
template <int N> DI void wait_vm() { asm volatile("s_waitcnt vmcnt(%0)" ::"n"(N) : "memory"); }
template <int N> DI void wait_vm_bar() { asm volatile("s_waitcnt vmcnt(%0) lgkmcnt(0)\n\ts_barrier" ::"n"(N) : "memory"); }
DI void bar() { asm volatile("s_waitcnt lgkmcnt(0)\n\ts_barrier" ::: "memory"); }
template <class T> DI void pin(T& v) { asm volatile("" : "+v"(v)); }
DI bf16x8 lds_ld16(ldsp p) { return *(const LAS bf16x8*)p; }
DI bf16x4 lds_tr(ldsp p) { return __builtin_amdgcn_ds_read_tr16_b64_v4i16((LAS bf16x4*)p); }
DI bf16x8 lds_tr2(ldsp p0, ldsp p1) {
  bf16x4 a = lds_tr(p0), b = lds_tr(p1);
  return __builtin_shufflevector(a, b, 0, 1, 2, 3, 4, 5, 6, 7);
}
DI float xsum32(float v) {
  auto r = __builtin_amdgcn_permlane32_swap(__float_as_uint(v), __float_as_uint(v), false, false);
  return __uint_as_float(r[0]) + __uint_as_float(r[1]);
}
DI float xsum16_32(float v) {
  auto r = __builtin_amdgcn_permlane16_swap(__float_as_uint(v), __float_as_uint(v), false, false);
  return xsum32(__uint_as_float(r[0]) + __uint_as_float(r[1]));
}
DI rsrc_t mk_rsrc(void* p, int bytes) { return __builtin_amdgcn_make_buffer_rsrc(p, 0, bytes, 0x00020000); }
template <int AUX = 16> DI void st16(rsrc_t r, int off, u32x4 v) { __builtin_amdgcn_raw_buffer_store_b128(v, r, off, 0, AUX); }
template <int AUX = 16> DI void st8(rsrc_t r, int off, u32x2 v) { __builtin_amdgcn_raw_buffer_store_b64(v, r, off, 0, AUX); }
#define CSH 2
#define PDLY 0
#define PSLP 2
#define REL0 2
#define REL1 6
#define REL2 10
#define REL3 13
#define REL4 16
#define SLP4 1
#define LDAUX 16
#define WIN5 4
#define AUX3 0
#define AUX5 0
#define AUX4 16

template <int P, int NT, int NS, int X, class F, class G0, class G> DI void loader_loop(F&& issue, G0&& early, G&& extra) {
  static_assert(NS >= 4, "three stages in flight + one being read");
  issue(0); issue(1);
  wait_vm_bar<P>();
  early();
  issue(2); issue(3);
  int slot = 4 % NS;
#pragma unroll 1
  for (int t = 1; t < NT - 3; ++t) {
    wait_vm_bar<2 * P>();
    issue(slot);
    slot = slot == NS - 1 ? 0 : slot + 1;
  }
  extra();
  wait_vm_bar<2 * P + X>(); wait_vm_bar<P + X>(); wait_vm_bar<X>();
}
DI void touch(const char* p, int lane, int nlines, unsigned lds_scratch) {
  set_m0(lds_scratch);
  asm volatile("global_load_lds_dword %0, %1" ::"v"((unsigned)((lane < nlines ? lane : nlines - 1) * 128)), "s"(p) : "memory");
}
#define CODE_ADDR(dst, sym) asm volatile("s_getpc_b64 s[40:41]\n\ts_add_u32 s40, s40, " #sym "@rel32@lo+4\n\ts_addc_u32 s41, s41, " #sym "@rel32@hi+12\n\ts_mov_b64 %0, s[40:41]" : "=s"(dst) :: "s40", "s41", "scc")
template <int P> DI void dma_run(const char* src, unsigned dst, unsigned lane16) {
  static_assert(P <= 8, "offset range");
  set_m0(dst + 4096);
  sfor<0, P>([&](auto ic) { dma<decltype(ic)::value * 1024 - 4096>(src + 4096, lane16); });
}
template <int P, int NT, int NS, int SSTR, int LSTR, int X, class G0, class G> DI void linear_loader(const char* src, unsigned dst, unsigned lane16, G0&& early, G&& extra) {
  loader_loop<P, NT, NS, X>([&](int slot) { dma_run<P>(src, dst + slot * LSTR, lane16); src += SSTR; }, early, extra);
}

template <int N> DI void dma_long(const char* src, unsigned dst, unsigned lane16) {
  sfor<0, N / 5>([&](auto ic) { constexpr int i = decltype(ic)::value; dma_run<5>(src + i * 5120, dst + i * 5120, lane16); });
}
DI unsigned lds_add(ldsp p, int lane) {
  unsigned old = 0;
  if (lane == 0) old = __hip_atomic_fetch_add((LAS unsigned*)p, 1u, __ATOMIC_RELAXED, __HIP_MEMORY_SCOPE_WORKGROUP);
  return __builtin_amdgcn_readfirstlane(old);
}
DI void lds_wait(ldsp p, unsigned want) {
  for (int sp = 0; sp < (1 << 22); ++sp) {
    if ((unsigned)__builtin_amdgcn_readfirstlane(*(volatile LAS unsigned*)p) >= want) break;
    __builtin_amdgcn_s_sleep(1);
  }
  asm volatile("" ::: "memory");
}
DI void lds_post(ldsp p, unsigned v, int lane) { if (lane == 0) *(volatile LAS unsigned*)p = v; asm volatile("" ::: "memory"); }
DI void lds_wait4(ldsp p, unsigned want) {
  for (int sp = 0; sp < (1 << 22); ++sp) {
    const u32x4 v = *(volatile LAS u32x4*)p;
    if ((unsigned)__builtin_amdgcn_readfirstlane(min(min(v[0], v[1]), min(v[2], v[3]))) >= want) break;
    __builtin_amdgcn_s_sleep(SLP4);
  }
  asm volatile("" ::: "memory");
}
DI void cnt_add(char* ws, int idx) { __hip_atomic_fetch_add((unsigned*)(ws + WS_CNT) + idx * CSTR, 1u, __ATOMIC_RELAXED, __HIP_MEMORY_SCOPE_AGENT); }
DI void cnt_wait(char* ws, int idx, unsigned want) {
  unsigned* p = (unsigned*)(ws + WS_CNT) + idx * CSTR;
  for (int sp = 0; sp < (1 << 18); ++sp) {
    if ((unsigned)__builtin_amdgcn_readfirstlane(__hip_atomic_load(p, __ATOMIC_RELAXED, __HIP_MEMORY_SCOPE_AGENT)) >= want) break;
    __builtin_amdgcn_s_sleep(PSLP);
  }
  __builtin_amdgcn_fence(__ATOMIC_ACQUIRE, "wavefront");
}
DI bf16x8 ld16_sc1(rsrc_t r, int voff, int soff) { return __builtin_bit_cast(bf16x8, __builtin_amdgcn_raw_buffer_load_b128(r, voff, soff, LDAUX)); }

template <int KIND> DI void pack_issue(const u16* wsrc, const u16* wsrc2, int ks, int cg, unsigned ldsdst, int lane) {
  constexpr int PITCH = KIND == 1 ? DF * 2 : D_ * 2;
  set_m0(ldsdst);
  sfor<0, 4>([&](auto ic) {
    constexpr int g = decltype(ic)::value;
    const int t = (lane >> 5) & 1, q = (lane >> 3) & 3;
    int row;
    if (KIND == 0) { const int P = 4 * ks + g, hp = P / 5, r = P - 5 * hp; row = 40 * hp + q + (r < 2 ? 8 * t + 4 * r : r == 2 ? 16 + 4 * t : 12 + 8 * t + 4 * r); }
    else if (KIND == 1) row = 32 * (ks >> 1) + 16 * t + 4 * (2 * (ks & 1) + (g & 1)) + q;
    else row = 32 * ks + 16 * (g >> 1) + 8 * t + 4 * (g & 1) + q;
    dma_nt<g * 1024>((const char*)(KIND == 1 && g >= 2 ? wsrc2 : wsrc) + cg * 128 - 4096, (unsigned)(row * PITCH + (lane & 7) * 16 + 4096 - g * 1024));
  });
}
template <int KIND> DI void pack_finish(ldsp u, rsrc_t rw, int ks, int cg, int lane, LAS float* cs) {
  const int q = (lane & 15) >> 2, p = lane & 3;
  ldsp a = KIND == 1 ? u + ((lane >> 4) & 1) * 2048 + (8 * (lane >> 5) + q) * 128 + p * 8 : u + (8 * (lane >> 4) + q) * 128 + p * 8;
#pragma unroll
  for (int i = 0; i < 4; ++i) {
    const int nt = 4 * cg + i;
    const u32x4 f = __builtin_bit_cast(u32x4, lds_tr2(a + i * 32, a + i * 32 + 512));
    if (KIND == 1) {
      const int ft = nt / 6, j = nt - ft * 6;
      st16(rw, WS_WGU + ((ft * 40 + ks) * 6 + j) * 1024 + lane * 16, f);
      const unsigned sv = (lane & 15) == ((lane >> 4) & 1) ? ONES2 : 0u;
      const f32x4 c = mfma16(__builtin_bit_cast(bf16x8, (u32x4){sv, sv, sv, sv}), __builtin_bit_cast(bf16x8, f), (f32x4){0.f, 0.f, 0.f, 0.f});
      if (lane < 16) { cs[i * 32 + lane] = c[0]; cs[i * 32 + 16 + lane] = c[1]; }
    } else {
      const int ct = nt / 5, j = nt - ct * 5;
      st16(rw, (KIND == 0 ? WS_WO : WS_WD) + ((ct * (KIND == 0 ? 20 : 48) + ks) * 5 + j) * 1024 + lane * 16, f);
    }
  }
}

#define STATMODE 1
constexpr int NWD_A = 352;
#define WD0 8
#define WD1 24
constexpr int K1_NS = 4, K1_STG = 32768, K1_NT = 10, K1_CTL = K1_NS * K1_STG, K1_LDS = K1_CTL + 4096;

DI void qkv_compute(ldsp lds, char* __restrict__ ws, int mt, int nt, int w, int lane) {
  const int wm = w >> 1, wn = w & 1, r32 = lane & 31, hi = lane >> 5, q = (lane & 15) >> 2, p = lane & 3, blk = (lane >> 4) & 1;
  TS_DECL;
  ldsp ctl = lds + K1_CTL;
  int aoff[4], woff[2];
#pragma unroll
  for (int s = 0; s < 4; ++s) aoff[s] = (wm * 64 + r32) * 128 + (((2 * s + hi) ^ ((r32 >> 1) & 7)) * 16);
#pragma unroll
  for (int j = 0; j < 2; ++j) woff[j] = 16384 + (8 * hi + q) * 256 + (((wn * 2 + (j ^ wm)) ^ q) * 64) + blk * 32 + p * 8;
  f32x16 acc[2][2];
#pragma unroll
  for (int j = 0; j < 2; ++j)
#pragma unroll
    for (int i = 0; i < 2; ++i) { acc[j][i] = (f32x16){}; pin(acc[j][i]); }
  f32x4 cs = {};
  const unsigned sv = (lane & 15) == blk ? ONES2 : 0u;
  const bf16x8 sel = __builtin_bit_cast(bf16x8, (u32x4){sv, sv, sv, sv});
  bf16x8 xf[2][2], wf[2][2];
  auto rdx = [&](ldsp sb, int s, int i) { xf[s & 1][i] = lds_ld16(sb + aoff[s] + i * 4096); };
  auto rdw = [&](ldsp sb, int s, int j) { wf[s & 1][j] = lds_tr2(sb + woff[j] + s * 4096, sb + woff[j] + s * 4096 + 1024); };
  auto mm = [&](int s, int j, int i) { acc[j][i] = mfma32(wf[s & 1][j], xf[s & 1][i], acc[j][i]); };
  auto step = [&](ldsp nb, int s, int sn, bool rd) {
    cs = mfma16(sel, wf[s & 1][0], cs);
    mm(s, 0, 0); if (rd) { rdx(nb, sn, 0); rdw(nb, sn, 0); } SB0();
    mm(s, 0, 1); if (rd) { rdx(nb, sn, 1); rdw(nb, sn, 1); } SB0();
    mm(s, 1, 0); SB0();
    mm(s, 1, 1); SB0();
  };
  bar();
  TS();
  ldsp sb = lds;
  rdx(sb, 0, 0); rdw(sb, 0, 0); rdx(sb, 0, 1); rdw(sb, 0, 1); SB0();
  int slot = 0;
#pragma unroll 1
  for (int t = 0; t < K1_NT - 1; ++t) {
    step(sb, 0, 1, true); step(sb, 1, 2, true); step(sb, 2, 3, true);
    slot = slot == K1_NS - 1 ? 0 : slot + 1;
    sb = lds + slot * K1_STG;
    bar(); SB0();
    step(sb, 3, 0, true);
  }
  step(sb, 0, 1, true); step(sb, 1, 2, true); step(sb, 2, 3, true);
  const LAS f32x2* rst = (const LAS f32x2*)(ctl + 1024);
  LAS float* cst = (LAS float*)(ctl + 2048);
  cs = mfma16(sel, wf[1][0], cs);
  mm(3, 0, 0); mm(3, 0, 1); SB0();
  if (lane < 16) { cst[wn * 64 + wm * 32 + lane] = cs[0]; cst[wn * 64 + wm * 32 + 16 + lane] = cs[1]; }
  mm(3, 1, 0); mm(3, 1, 1); SB0();
  TS();
  bar();
  TS();
  const rsrc_t rw = mk_rsrc(ws, WS_END);
  const float sc = nt < 5 ? QSCALE : 1.f;
  f32x4 c4[2][4];
#pragma unroll
  for (int j = 0; j < 2; ++j)
#pragma unroll
    for (int b = 0; b < 4; ++b) c4[j][b] = *(const LAS f32x4*)(cst + wn * 64 + (j ^ wm) * 32 + 8 * b + 4 * hi);
#pragma unroll
  for (int i = 0; i < 2; ++i) {
    const int ml = wm * 64 + i * 32 + r32;
    const f32x2 st = rst[ml];
    const float a = st.y * sc, nm = -st.x * a;
    const int ob = WS_QK + ((nt * 16 + wn * 8 + hi) * S_ + mt * 128 + ml) * 16;
#pragma unroll
    for (int j = 0; j < 2; ++j)
#pragma unroll
      for (int bp = 0; bp < 2; ++bp) {
        unsigned pk[2][2];
#pragma unroll
        for (int e = 0; e < 2; ++e) {
          const int b = 2 * bp + e;
          const f32x2 v0 = (f32x2){acc[j][i][4 * b], acc[j][i][4 * b + 1]} * a + nm * (f32x2){c4[j][b][0], c4[j][b][1]};
          const f32x2 v1 = (f32x2){acc[j][i][4 * b + 2], acc[j][i][4 * b + 3]} * a + nm * (f32x2){c4[j][b][2], c4[j][b][3]};
          pk[e][0] = cvtpk(v0[0], v0[1]); pk[e][1] = cvtpk(v1[0], v1[1]);
        }
        auto r0 = __builtin_amdgcn_permlane32_swap(pk[0][0], pk[1][0], false, false);
        auto r1 = __builtin_amdgcn_permlane32_swap(pk[0][1], pk[1][1], false, false);
        st16(rw, ob + ((j ^ wm) * 4 + bp * 2) * S_ * 16, (u32x4){r0[0], r1[0], r0[1], r1[1]});
      }
  }
  TS();
  if (w == 0) { TS_END(0); }
}

DI void qkv_stats(ldsp lds, int sw, int lane) {
  const int r32 = lane & 31, hi = lane >> 5;
  const int off = (sw * 32 + r32) * 128, x = (r32 >> 1) & 7;
  f32x2 s0 = {0.f, 0.f}, s1 = {0.f, 0.f}, q0 = {0.f, 0.f}, q1 = {0.f, 0.f};
  int slot = 0;
#pragma unroll 1
  for (int t = 0; t < K1_NT; ++t) {
    bar();
    ldsp sb = lds + slot * K1_STG + off;
    slot = slot == K1_NS - 1 ? 0 : slot + 1;
    u32x4 v[4];
#pragma unroll
    for (int c = 0; c < 4; ++c) v[c] = *(const LAS u32x4*)(sb + (((2 * c + hi) ^ x) * 16));
#pragma unroll
    for (int c = 0; c < 4; ++c)
#pragma unroll
      for (int e = 0; e < 4; e += 2) {
        const float a0 = bflo(v[c][e]), a1 = bfhi(v[c][e]), b0 = bflo(v[c][e + 1]), b1 = bfhi(v[c][e + 1]);
        asm("v_add_f32 %0, %0, %1" : "+v"(s0.x) : "v"(a0)); asm("v_add_f32 %0, %0, %1" : "+v"(s0.y) : "v"(a1));
        asm("v_add_f32 %0, %0, %1" : "+v"(s1.x) : "v"(b0)); asm("v_add_f32 %0, %0, %1" : "+v"(s1.y) : "v"(b1));
        asm("v_fmac_f32 %0, %1, %1" : "+v"(q0.x) : "v"(a0)); asm("v_fmac_f32 %0, %1, %1" : "+v"(q0.y) : "v"(a1));
        asm("v_fmac_f32 %0, %1, %1" : "+v"(q1.x) : "v"(b0)); asm("v_fmac_f32 %0, %1, %1" : "+v"(q1.y) : "v"(b1));
      }
  }
  const float mu = xsum32((s0[0] + s0[1]) + (s1[0] + s1[1])) * (1.f / D_);
  const float var = fmaxf(xsum32((q0[0] + q0[1]) + (q1[0] + q1[1])) * (1.f / D_) - mu * mu, 0.f);
  if (hi == 0) ((LAS f32x2*)(lds + K1_CTL + 1024))[sw * 32 + r32] = (f32x2){mu, __builtin_amdgcn_rsqf(var + LN_EPS)};
  asm volatile("s_waitcnt lgkmcnt(0)" ::: "memory");
}

DI void qkv_loader(ldsp lds, const u16* __restrict__ x, const u16* __restrict__ wqkv, int mt, int nt, int lw, int lane) {
  const bool isx = lw < 2;
  const int half = lw & 1;
  unsigned voff[8];
#pragma unroll
  for (int i = 0; i < 8; ++i) {
    const int r = (half * 8 + i) * 8 + (lane >> 3), c = (lane & 7) ^ ((r >> 1) & 7);
    const int k = (half * 8 + i) * 4 + (lane >> 4), cp = lane & 15;
    voff[i] = (isx ? r * 1280 + c * 16 : k * 3840 + (cp ^ ((k & 3) << 2)) * 16) + 8192 - i * 1024;
  }
  const char* src = (isx ? (const char*)x + (size_t)mt * 128 * 1280 : (const char*)wqkv + nt * 256) - 4096;
  const int sstr = isx ? 128 : 64 * 3840;
  const unsigned dst = (unsigned)(uintptr_t)lds + lw * 8192 + 4096;
  loader_loop<8, K1_NT, K1_NS, 0>([&](int slot) {
    set_m0(dst + slot * K1_STG);
    sfor<0, 8>([&](auto ic) { constexpr int i = decltype(ic)::value; dma<i * 1024 - 4096>(src, voff[i]); });
    src += sstr;
  }, [] {}, [] {});
}

extern "C" __global__ __launch_bounds__(768, 3) void k_qkv(const u16* __restrict__ x, const u16* __restrict__ wqkv, const u16* __restrict__ wo, const u16* __restrict__ wd, char* __restrict__ ws) {
  extern __shared__ __attribute__((aligned(16))) char smem[];
  ldsp lds = (ldsp)smem;
  const int lane = threadIdx.x & 63, w = __builtin_amdgcn_readfirstlane(threadIdx.x >> 6);
  const int xcd = blockIdx.x & 7, idx = blockIdx.x >> 3;
  const int nt = (xcd & 1) * 8 + (idx >> 2), mt = (xcd >> 1) * 4 + (idx & 3);
  if (w < 4 && nt < 15) { qkv_loader(lds, x, wqkv, mt, nt, w, lane); return; }
  if (w == 11 && lane < 2) ((unsigned*)(ws + WS_CNT))[(blockIdx.x * 2 + lane) * CSTR] = 0u;
  if (w == 10) {
    const char *c0, *c1;
    CODE_ADDR(c0, k_qkv); CODE_ADDR(c1, k_text_end);
    const unsigned lim = c1 > c0 ? (unsigned)(c1 - c0) : 0u, off = (idx * 16 + (lane & 15)) * 128;
    set_m0((unsigned)(uintptr_t)lds + K1_CTL + 3072 + 512);
    asm volatile("global_load_lds_dword %0, %1" ::"v"(off < lim ? off : lim), "s"(c0) : "memory");
  }
  if (w == 8 || w == 9) {
    const int id = blockIdx.x * 64 + lane, u = w - 8;
    const bool isx = blockIdx.x < 128;
    const int i2 = id - 8192 < 7680 ? id - 8192 : 7679;
    const unsigned off = isx ? (id >> 2) * 1280 + 256 + u * 512 + (id & 3) * 128 : (128 + u * 256) * 3840 + i2 * 128;
    set_m0((unsigned)(uintptr_t)lds + K1_CTL + 3072 + u * 256);
    const char* src = isx ? (const char*)x : (const char*)wqkv;
    asm volatile("" : "+s"(src));
    if (u == 0) __builtin_amdgcn_s_sleep(WD0); else __builtin_amdgcn_s_sleep(WD1);
    asm volatile("global_load_lds_dword %0, %1" ::"v"(off), "s"(src) : "memory");
  }
  if (nt == 15) {
    const rsrc_t rw = mk_rsrc(ws, WS_END);
    constexpr int NU = (200 + NWD_A + 191) / 192;
    auto iss = [&](int r) {
      const int u = min(mt * 12 + w + r * 192, 200 + NWD_A - 1), v = u - 200;
      if (u < 200) pack_issue<0>(wo, wo, u / 10, u % 10, (unsigned)(uintptr_t)lds + (w * 2 + (r & 1)) * 4096, lane);
      else pack_issue<2>(wd, wd, v / 10, v % 10, (unsigned)(uintptr_t)lds + (w * 2 + (r & 1)) * 4096, lane);
    };
    iss(0); iss(1);
#pragma unroll 1
    for (int r = 0; r < NU; ++r) {
      if (r == 0) wait_vm<4>(); else if (r + 1 < NU) wait_vm<8>(); else wait_vm<4>();
      const int u = min(mt * 12 + w + r * 192, 200 + NWD_A - 1), v = u - 200;
      if (u < 200) pack_finish<0>(lds + (w * 2 + (r & 1)) * 4096, rw, u / 10, u % 10, lane, nullptr);
      else pack_finish<2>(lds + (w * 2 + (r & 1)) * 4096, rw, v / 10, v % 10, lane, nullptr);
      asm volatile("s_waitcnt lgkmcnt(0)" ::: "memory");
      if (r + 2 < NU) iss(r + 2);
    }
    return;
  }
  if (w >= 8) qkv_stats(lds, w - 8, lane);
  else qkv_compute(lds, ws, mt, nt, w - 4, lane);
}

#define LAZY_LIM 1.0995116e12f
#define VLEAD 3
constexpr int K2_K = 102400, K2_V = K2_K + 24576, K2_VP = 8192 + 64, K2_CS = K2_V + 4 * K2_VP, K2_MSK = K2_CS + 2560, K2_FL = K2_MSK + 832, K2_LDS = K2_FL + 64 + 256;
static_assert(K2_LDS <= 163840 && K2_FL % 16 == 0, "LDS map");

DI void n80_epilogue_h(f32x4 (&acc)[5], rsrc_t rw, int mt, int ct, int w, int lane) {
  const int fr = lane & 15, g = lane >> 4, tok = mt * 64 + w * 16 + fr;
  const int odd = ct & 1, pb = (ct >> 1) * 5 + (odd ? 3 : 0), ps = (ct >> 1) * 5 + 2;
  const int hb = WS_H + (((mt >> 1) & 1) * 40 + (g >> 1)) * PLANE + (mt >> 2) * 4096 + ((mt & 1) * 2 + (w >> 1)) * 1024 + ((g & 1) * 32 + (w & 1) * 16 + fr) * 16;
  u32x2 hv[5];
  float s = 0.f;
#pragma unroll
  for (int j = 0; j < 5; ++j) hv[j] = (u32x2){cvtpk(acc[j][0], acc[j][1]), cvtpk(acc[j][2], acc[j][3])};
  const u32x2 a0 = odd ? hv[1] : hv[0], a1 = odd ? hv[2] : hv[1], b0 = odd ? hv[3] : hv[2], b1 = odd ? hv[4] : hv[3], s1 = odd ? hv[0] : hv[4];
  st16<AUX3>(rw, hb + pb * 2 * PLANE, (u32x4){a0[0], a0[1], a1[0], a1[1]});
  st16<AUX3>(rw, hb + (pb + 1) * 2 * PLANE, (u32x4){b0[0], b0[1], b1[0], b1[1]});
  st8<AUX3>(rw, hb + ps * 2 * PLANE + odd * 8, s1);
  f32x4 v[5];
#pragma unroll
  for (int j = 0; j < 5; ++j) { v[j] = (f32x4){bflo(hv[j][0]), bfhi(hv[j][0]), bflo(hv[j][1]), bfhi(hv[j][1])}; s += (v[j][0] + v[j][1]) + (v[j][2] + v[j][3]); }
  const float mean = xsum16_32(s) * (1.f / 80.f);
  float m2 = 0.f;
#pragma unroll
  for (int j = 0; j < 5; ++j) { const f32x4 d = v[j] - mean; m2 += (d[0] * d[0] + d[1] * d[1]) + (d[2] * d[2] + d[3] * d[3]); }
  m2 = xsum16_32(m2);
  if (g == 0) st8<AUX3>(rw, WS_ST + (((ct >> 2) * S_ + tok) * 4 + (ct & 3)) * 8, (u32x2){__float_as_uint(mean), __float_as_uint(m2)});
}

DI void attn_helper(ldsp lds, char* __restrict__ ws, int c, int h, int hw, int lane, const u16* __restrict__ x, const u16* __restrict__ wg, const u16* __restrict__ wu, const u16* __restrict__ wd) {
  const unsigned lds0 = (unsigned)(uintptr_t)lds;
  const int pm = 5 * (h >> 1) + 3 * (h & 1), pt = 5 * (h >> 1) + 2;
  const bool EDGE = c == 0;
  const int tok0 = 256 * c - 256 + 128 * hw + ((EDGE && hw < 2) ? 256 : 0);
  const char* kb = ws + WS_QK + (80 * S_ + tok0) * 16;
  {
    set_m0(lds0 + K2_K + hw * 2048); dma<0>(kb + pm * S_ * 16, lane * 16); dma<1024>(kb + pm * S_ * 16, lane * 16);
    set_m0(lds0 + K2_K + 8192 + hw * 2048); dma<0>(kb + (pm + 1) * S_ * 16, lane * 16); dma<1024>(kb + (pm + 1) * S_ * 16, lane * 16);
    set_m0(lds0 + K2_K + 16384 + hw * 2048); dma<0>(kb + pt * S_ * 16, lane * 16); dma<1024>(kb + pt * S_ * 16, lane * 16);
  }
  TS_DECL;
  const int b = blockIdx.x, xcd = b & 7, idx = b >> 3;
  if (hw == 0 && lane < 8) *(LAS unsigned*)(lds + K2_FL + lane * 4) = 0u;
  {
    const unsigned tm = (EDGE && hw < 2 ? 0xff80u : 0u) | 0xbf800000u;
    *(LAS u32x4*)(lds + K2_V + 3 * K2_VP + hw * 2048 + lane * 16) = (u32x4){tm, 0xbf80bf80u, ONES2, ONES2};
    *(LAS u32x4*)(lds + K2_V + 3 * K2_VP + hw * 2048 + 1024 + lane * 16) = (u32x4){tm, 0xbf80bf80u, ONES2, ONES2};
  }
  if (hw == 1 && lane < 52) {
    const int d = lane - 20;
    u32x4 mk;
#pragma unroll
    for (int e = 0; e < 4; ++e) { const int k0 = ((2 * e) & 3) + 8 * (e >> 1); mk[e] = (k0 > d ? 0xffffu : 0u) | (k0 + 1 > d ? 0xffff0000u : 0u); }
    *(LAS u32x4*)(lds + K2_MSK + lane * 16) = mk;
  }
  wait_vm<VLEAD>();
  {
    const char* vb = kb + 80 * S_ * 16;
    set_m0(lds0 + K2_V + hw * 2048); dma<0>(vb + pm * S_ * 16, lane * 16); dma<1024>(vb + pm * S_ * 16, lane * 16);
    set_m0(lds0 + K2_V + K2_VP + hw * 2048); dma<0>(vb + (pm + 1) * S_ * 16, lane * 16); dma<1024>(vb + (pm + 1) * S_ * 16, lane * 16);
    set_m0(lds0 + K2_V + 2 * K2_VP + hw * 2048); dma<0>(vb + pt * S_ * 16, lane * 16); dma<1024>(vb + pt * S_ * 16, lane * 16);
  }
  const rsrc_t rw = mk_rsrc(ws, WS_END);
  const int mt = xcd * 4 + (idx >> 3), ct = idx & 7;
  const int q0 = hw * 25600;
  const int bb = b, pcg = bb % 24, pks = (bb / 24) * 4 + hw, v0 = NWD_A + (b - 240) * 8 + hw * 2;
  wait_vm_bar<6>();
  if (b < 240) { pack_issue<1>(wg, wu, pks, pcg, lds0 + q0, lane); wait_vm<4>(); }
  else { pack_issue<2>(wd, wd, v0 / 10, v0 % 10, lds0 + q0, lane); pack_issue<2>(wd, wd, v0 / 10, v0 % 10 + 1, lds0 + q0 + 4096, lane); wait_vm<8>(); }
  if (lane == 0) *(volatile LAS unsigned*)(lds + K2_FL + 16 + hw * 4) = 1u;
  TS();
  u32x4 xr[2]; u32x2 xs;
  {
    const char* xp = (const char*)x + (size_t)(mt * 64 + hw * 16) * 1280 + ct * 160;
    const int g = lane >> 4;
    const unsigned o16 = (lane & 15) * 1280 + (g & 1) * 32 + (g >> 1) * 16, o8 = (lane & 15) * 1280 + g * 8;
    asm volatile("global_load_dwordx4 %0, %1, %2" : "=v"(xr[0]) : "v"(o16), "s"(xp) : "memory");
    asm volatile("global_load_dwordx4 %0, %1, %2 offset:64" : "=v"(xr[1]) : "v"(o16), "s"(xp) : "memory");
    asm volatile("global_load_dwordx2 %0, %1, %2 offset:128" : "=v"(xs) : "v"(o8), "s"(xp) : "memory");
  }
  dma_long<15>(ws + WS_WO + ct * 102400 + q0 + 10240, lds0 + q0 + 10240, lane * 16);
  wait_vm<15>();
  pin(xr[0]); pin(xr[1]); pin(xs);
  TS();
  if (b >= 240) {
    pack_finish<2>(lds + q0, rw, v0 / 10, v0 % 10, lane, nullptr); pack_finish<2>(lds + q0 + 4096, rw, v0 / 10, v0 % 10 + 1, lane, nullptr);
    asm volatile("s_waitcnt lgkmcnt(0)" ::: "memory");
  } else {
    LAS float* ct4 = (LAS float*)(lds + K2_CS);
    pack_finish<1>(lds + q0, rw, pks, pcg, lane, ct4 + hw * 128);
    asm volatile("s_waitcnt lgkmcnt(0)" ::: "memory");
    if (lds_add(lds + K2_FL, lane) == 3) {
      asm volatile("" ::: "memory");
      const LAS f32x2* c2 = (const LAS f32x2*)ct4 + lane;
      const f32x2 a = (c2[0] + c2[64]) + (c2[128] + c2[192]);
      const int e = pcg * 128 + lane * 2, ft = e / 192;
      st8(rw, WS_CS + ft * 8192 + ((bb / 24) * 192 + e - ft * 192) * 4, __builtin_bit_cast(u32x2, a));
    }
  }
  TS();
  dma_long<10>(ws + WS_WO + ct * 102400 + q0, lds0 + q0, lane * 16);
  if (hw == 3) {
    const char *c0, *c1;
    CODE_ADDR(c0, k_mlp); CODE_ADDR(c1, k_text_end);
    const unsigned lim = c1 > c0 ? (unsigned)(c1 - c0) : 0u, off = (idx * 8 + (lane & 7)) * 128;
    set_m0(lds0 + K2_FL + 64);
    asm volatile("global_load_lds_dword %0, %1" ::"v"(off < lim ? off : lim), "s"(c0) : "memory");
  }
  f32x4 acc[5];
#pragma unroll
  for (int jp = 0; jp < 2; ++jp) {
    auto r0 = __builtin_amdgcn_permlane16_swap(xr[jp][0], xr[jp][2], false, false);
    auto r1 = __builtin_amdgcn_permlane16_swap(xr[jp][1], xr[jp][3], false, false);
    acc[2 * jp] = (f32x4){bflo(r0[0]), bfhi(r0[0]), bflo(r1[0]), bfhi(r1[0])};
    acc[2 * jp + 1] = (f32x4){bflo(r0[1]), bfhi(r0[1]), bflo(r1[1]), bfhi(r1[1])};
  }
  acc[4] = (f32x4){bflo(xs[0]), bfhi(xs[0]), bflo(xs[1]), bfhi(xs[1])};
#pragma unroll
  for (int j = 0; j < 5; ++j) pin(acc[j]);
  wait_vm<0>();
  lds_add(lds + K2_FL + 8, lane);
  lds_wait(lds + K2_FL + 8, 4);
  TS();
  if (hw == 0) {
    lds_wait(lds + K2_FL + 4, 8);
    __builtin_amdgcn_s_sleep(PDLY);

    cnt_wait(ws, (mt * 8 + ct) >> CSH, 32);
 if (lane == 0) *(volatile LAS unsigned*)(lds + K2_FL + 12) = 1u; }
  else lds_wait(lds + K2_FL + 12, 1);
  TS();
  bf16x8 af[10][2], wf[2][5];
  f32x4 ac1[5];
#pragma unroll
  for (int j = 0; j < 5; ++j) ac1[j] = (f32x4){0.f, 0.f, 0.f, 0.f};
  const int ao = WS_QK + ((hw & 1) * 4 + (lane >> 4)) * PLANE + (mt * 64 + hw * 16 + (lane & 15)) * 16;
#pragma unroll
  for (int t = 0; t < 10; ++t) { af[t][0] = ld16_sc1(rw, ao, t * 8 * PLANE); af[t][1] = ld16_sc1(rw, ao ^ 256, t * 8 * PLANE); }
  SB0();
  ldsp wp = lds + (hw & 1) * 5120 + lane * 16;
#pragma unroll
  for (int j = 0; j < 5; ++j) wf[0][j] = lds_ld16(wp + j * 1024);
#pragma unroll
  for (int t = 0; t < 10; ++t) {
#pragma unroll
    for (int j = 0; j < 5; ++j) {
      if (t + 1 < 10) wf[(t + 1) & 1][j] = lds_ld16(wp + (t + 1) * 10240 + j * 1024);
      ac1[j] = mfma16(wf[t & 1][j], af[t][1], ac1[j]);
    }
#pragma unroll
    for (int j = 0; j < 5; ++j) acc[j] = mfma16(wf[t & 1][j], af[t][0], acc[j]);
    SB0();
  }
  lds_wait(lds + K2_FL + 4, 8);
#pragma unroll
  for (int j = 0; j < 5; ++j) *(LAS f32x4*)(lds + K2_K + (hw * 5 + j) * 1024 + lane * 16) = ac1[j];
  bar();
#pragma unroll
  for (int j = 0; j < 5; ++j) acc[j] += *(const LAS f32x4*)(lds + K2_K + ((hw ^ 1) * 5 + j) * 1024 + lane * 16);
  TS();
  n80_epilogue_h(acc, rw, mt, ct, hw, lane);
  TS();
  if (hw == 1) { TS_END(2); }
}

template <bool EXACT> DI f32x16 attn_rows(ldsp lds, ldsp kp, ldsp ktp, ldsp vp, bf16x8 qm, u32x2 qtr, int dq, int hi) {
  const float NEG = -__builtin_inff();
  auto mx3 = [](float a, float b, float c) { return __builtin_elementwise_maximum(__builtin_elementwise_maximum(a, b), c); };
  auto qk = [&](int j, u32x2 q2, f32x16 cin) {
    return __builtin_amdgcn_mfma_f32_32x32x8bf16_1k(*(const LAS bf16x4*)(ktp + j * 512), __builtin_bit_cast(bf16x4, q2), mfma32(lds_ld16(kp + j * 512), qm, cin), 0, 0, 0);
  };
  f32x16 s0, s8;
#pragma unroll
  for (int r = 0; r < 16; ++r) { const bool le = (r & 3) + 8 * (r >> 2) <= dq; s8[r] = le ? 0.f : NEG; s0[r] = le ? NEG : 0.f; }
  if (!EXACT) { pin(s0); pin(s8); bar(); }
  bf16x8 km[2]; bf16x4 kt[2];
  s8 = qk(8, qtr, s8); s0 = qk(0, qtr, s0);
  km[1] = lds_ld16(kp + 512); kt[1] = *(const LAS bf16x4*)(ktp + 512);
  km[0] = lds_ld16(kp + 1024); kt[0] = *(const LAS bf16x4*)(ktp + 1024);
  f32x16 sa = mfma32(km[1], qm, (f32x16){});
#pragma unroll
  for (int r = 0; r < 16; ++r) s0[r] = __builtin_elementwise_maximum(s0[r], s8[r]);
  float m = mx3(mx3(mx3(s0[0], s0[1], s0[2]), mx3(s0[3], s0[4], s0[5]), mx3(s0[6], s0[7], s0[8])), mx3(mx3(s0[9], s0[10], s0[11]), s0[12], s0[13]), __builtin_elementwise_maximum(s0[14], s0[15]));
  if (EXACT) {
#pragma unroll 1
    for (int j = 1; j < 8; ++j) {
      const f32x16 t = qk(j, qtr, (f32x16){});
#pragma unroll
      for (int r = 0; r < 16; r += 2) m = mx3(m, t[r], t[r + 1]);
    }
  }
  {
    auto rr = __builtin_amdgcn_permlane32_swap(__float_as_uint(m), __float_as_uint(m), false, false);
    m = __builtin_elementwise_maximum(__uint_as_float(rr[0]), __uint_as_float(rr[1]));
  }
  u32x2 q2 = qtr;
  {
    const unsigned u0 = __float_as_uint(m) & 0xffff0000u;
    const float r1 = m - __uint_as_float(u0);
    const unsigned u1 = __float_as_uint(r1) & 0xffff0000u;
    const float r2 = r1 - __uint_as_float(u1);
    if (hi) q2 = (u32x2){0x3f80u | u0, (u1 >> 16) | (__float_as_uint(r2) & 0xffff0000u)};
  }
  const bf16x4 qs = __builtin_bit_cast(bf16x4, q2);
  s0 = __builtin_amdgcn_mfma_f32_32x32x8bf16_1k(__builtin_bit_cast(bf16x4, hi ? (u32x2){0xbf800000u, 0xbf80bf80u} : (u32x2){0u, 0u}), qs, s0, 0, 0, 0);
  sa = __builtin_amdgcn_mfma_f32_32x32x8bf16_1k(kt[1], qs, sa, 0, 0, 0);
  f32x16 sb = __builtin_amdgcn_mfma_f32_32x32x8bf16_1k(kt[0], qs, mfma32(km[0], qm, (f32x16){}), 0, 0, 0);
  SB0();
  auto ex8 = [&](const f32x16& s, int ks) {
    u32x4 p;
#pragma unroll
    for (int e = 0; e < 4; ++e) p[e] = cvtpk(__builtin_amdgcn_exp2f(s[8 * ks + 2 * e]), __builtin_amdgcn_exp2f(s[8 * ks + 2 * e + 1]));
    return p;
  };
  auto vld = [&](int slot, int ks) { return lds_tr2(vp + slot * 512 + ks * 256, vp + slot * 512 + ks * 256 + 128); };
  f32x16 o = {};
  auto pv = [&](bf16x8 v, u32x4 p) { o = mfma32(v, __builtin_bit_cast(bf16x8, p), o); };
  const u32x4 mk0 = *(const LAS u32x4*)(lds + K2_MSK + (dq + 20) * 16), mk1 = *(const LAS u32x4*)(lds + K2_MSK + (dq + 4) * 16);
  u32x4 p0[2], pp[2], pc[2];
  p0[0] = ex8(s0, 0); p0[1] = ex8(s0, 1);
  const u32x4 vrdy = *(volatile LAS u32x4*)(lds + K2_FL + 16);
  km[1] = lds_ld16(kp + 3 * 512); kt[1] = *(const LAS bf16x4*)(ktp + 3 * 512);
  pp[0] = ex8(sa, 0); pp[1] = ex8(sa, 1);
  pin(p0[0]); pin(p0[1]); pin(pp[0]); pin(pp[1]);
  if (!EXACT && __builtin_expect(__builtin_amdgcn_readfirstlane((vrdy[0] & vrdy[1]) & (vrdy[2] & vrdy[3])) == 0, 0)) lds_wait4(lds + K2_FL + 16, 1);
  asm volatile("" ::: "memory");
  bf16x8 vf[2][2], vz[4];
  vf[1][0] = vld(1, 0); vf[1][1] = vld(1, 1);
  SB0();
  sfor<2, 8>([&](auto jc) {
    constexpr int j = decltype(jc)::value;
    f32x16& cur = (j & 1) ? sa : sb;
    f32x16& nxt = (j & 1) ? sb : sa;
    vf[j & 1][0] = vld(j, 0); vf[j & 1][1] = vld(j, 1);
    if constexpr (j + 2 < 8) { km[j & 1] = lds_ld16(kp + (j + 2) * 512); kt[j & 1] = *(const LAS bf16x4*)(ktp + (j + 2) * 512); }
    if constexpr (j == 6) { vz[0] = vld(0, 0); vz[1] = vld(8, 0); }
    if constexpr (j == 7) { vz[2] = vld(0, 1); vz[3] = vld(8, 1); }
    if constexpr (j + 1 < 8) nxt = mfma32(km[(j + 1) & 1], qm, (f32x16){});
    else { const u32x4 a = p0[0] & mk0; pv(vz[0], a); pv(vz[1], p0[0] ^ a); }
    SB0();
    pc[0] = ex8(cur, 0); pv(vf[(j - 1) & 1][0], pp[0]);
    if constexpr (j + 1 < 8) nxt = __builtin_amdgcn_mfma_f32_32x32x8bf16_1k(kt[(j + 1) & 1], qs, nxt, 0, 0, 0);
    SB0();
    pc[1] = ex8(cur, 1); pv(vf[(j - 1) & 1][1], pp[1]); SB0();
    pp[0] = pc[0]; pp[1] = pc[1];
  });
  { const u32x4 a = p0[1] & mk1; pv(vz[2], a); pv(vz[3], p0[1] ^ a); }
  pv(vf[1][0], pp[0]); pv(vf[1][1], pp[1]);
  return o;
}

DI void attn_body(ldsp lds, char* __restrict__ ws, int c, int h, int w, int lane) {
  const int r32 = lane & 31, hi = lane >> 5;
  TS_DECL;
  const int pm = 5 * (h >> 1) + 3 * (h & 1), pt = 5 * (h >> 1) + 2;
  const char* qb = ws + WS_QK + (256 * c + 32 * w) * 16;
  const bf16x8 qm = *(const bf16x8*)(qb + ((pm + hi) * S_ + r32) * 16);
  u32x2 qtr = *(const u32x2*)(qb + (pt * S_ + r32) * 16 + (h & 1) * 8);
  if (hi) qtr = (u32x2){0x3f80u, 0u};
  ldsp kp = lds + K2_K + hi * 8192 + (w * 32 + r32) * 16;
  ldsp ktp = lds + (hi ? K2_V + 3 * K2_VP + w * 512 : K2_K + 16384 + (w * 32 + r32) * 16 + (h & 1) * 8);
  const int vq = (lane & 15) >> 2, vpp = lane & 3, vblk = (lane >> 4) & 1;
  ldsp vp = lds + K2_V + (w * 32 + 4 * hi + vq) * 16 + (vblk == 0 ? (vpp >> 1) * K2_VP + (vpp & 1) * 8 : vpp == 0 ? 2 * K2_VP + (h & 1) * 8 : 3 * K2_VP + 8);
  f32x16 o = attn_rows<false>(lds, kp, ktp, vp, qm, qtr, r32 - 4 * hi, hi);
  float l = __uint_as_float(__builtin_amdgcn_permlane32_swap(__float_as_uint(o[8]), __float_as_uint(o[8]), false, false)[1]);
  if (__builtin_expect(__builtin_amdgcn_ballot_w64(!(l < LAZY_LIM)) != 0, 0)) {
    o = attn_rows<true>(lds, kp, ktp, vp, qm, qtr, r32 - 4 * hi, hi);
    l = __uint_as_float(__builtin_amdgcn_permlane32_swap(__float_as_uint(o[8]), __float_as_uint(o[8]), false, false)[1]);
  }
  TS();
  const float il = __builtin_amdgcn_rcpf(l);
  const int tok = 256 * c + 32 * w + r32;
  const rsrc_t rw = mk_rsrc(ws, WS_END);
  const u32x4 w0 = {cvtpk(o[0] * il, o[1] * il), cvtpk(o[2] * il, o[3] * il), cvtpk(o[4] * il, o[5] * il), cvtpk(o[6] * il, o[7] * il)};
  st16(rw, WS_QK + (pm + hi) * PLANE + tok * 16, w0);
  if (hi == 0) st8(rw, WS_QK + pt * PLANE + tok * 16 + (h & 1) * 8, (u32x2){cvtpk(o[8] * il, o[9] * il), cvtpk(o[10] * il, o[11] * il)});
  TS();
  wait_vm<0>();
  if (lds_add(lds + K2_FL + 4, lane) == 7 && lane < (32 >> CSH)) cnt_add(ws, (c * 32 >> CSH) + lane);
  if (w == 7 || w == 0) { TS_END(w == 7 ? 1 : 5); }
}

extern "C" __global__ __launch_bounds__(768, 3) void k_attn(char* __restrict__ ws, const u16* __restrict__ x, const u16* __restrict__ wg, const u16* __restrict__ wu, const u16* __restrict__ wd) {
  extern __shared__ __attribute__((aligned(16))) char smem[];
  const int lane = threadIdx.x & 63, w = __builtin_amdgcn_readfirstlane(threadIdx.x >> 6);
  const int xcd = blockIdx.x & 7, idx = blockIdx.x >> 3, h = xcd * 4 + (idx >> 3), c = idx & 7;
  if (w < 4) attn_helper((ldsp)smem, ws, c, h, w, lane, x, wg, wu, wd);
  else attn_body((ldsp)smem, ws, c, h, w - 4, lane);
}

constexpr int K4_NS = 4, K4_STG = 20480, K4_NT = 20, K4_RING = 61440, K4_CTL = K4_RING + K4_NS * K4_STG, K4_SD = K4_CTL + 2048, K4_LDS = K4_SD + 8192 + 8192;

extern "C" __global__ __launch_bounds__(512, 2) void k_mlp(char* __restrict__ ws, u16* __restrict__ out) {
  extern __shared__ __attribute__((aligned(16))) char smem[];
  ldsp lds = (ldsp)smem + K4_RING;
  ldsp ctl = (ldsp)smem + K4_CTL;
  const int lane = threadIdx.x & 63, w = __builtin_amdgcn_readfirstlane(threadIdx.x >> 6);
  const unsigned lds00 = (unsigned)(uintptr_t)(ldsp)smem, lds0 = lds00 + K4_RING;
  const int xcd = blockIdx.x & 7, idx = blockIdx.x >> 3, mt = (xcd >> 2) * 8 + (idx >> 2), ft = (xcd & 3) * 4 + (idx & 3);
  const int mt5 = (xcd >> 1) * 8 + (idx >> 2), ct = (xcd & 1) * 4 + (idx & 3);
  const int odd = ct & 1, pb = (ct >> 1) * 5 + (odd ? 3 : 0), ps = (ct >> 1) * 5 + 2;
  if (w >= 4) {
    const int lw = w - 4;
    const char* wd = ws + WS_WD + ct * 245760;
    {
      const bool isa = w < 6;
      const char* src = isa ? ws + WS_H + ((mt & 1) * 40 + lw) * PLANE + (mt >> 1) * 4096 : ws + WS_WGU + ft * K4_NT * 12288 + (w - 6) * 6144;
      const unsigned dst = isa ? lds0 + lw * 4096 : lds0 + 8192 + (w - 6) * 6144;
      auto one = [] {};
      constexpr int XP = 0;
      if (isa) loader_loop<4 + XP, K4_NT, K4_NS, 0>([&](int slot) { dma_run<4>(src, dst + slot * K4_STG, lane * 16); src += 2 * PLANE; one(); },
                                               [&] { dma_run<4>(ws + WS_ST + lw * S_ * 32 + mt * 4096, lds00 + K4_SD + lw * 4096, lane * 16); }, [] {});
      else loader_loop<6 + XP, K4_NT, K4_NS, 0>([&](int slot) { dma_run<6>(src, dst + slot * K4_STG, lane * 16); src += 12288; one(); },
                                           [&] { dma_run<4>(ws + WS_CS + ft * 8192 + (w - 6) * 4096, lds00 + K4_SD + 8192 + (w - 6) * 4096, lane * 16); }, [] {});
    }
    dma_long<15>(wd + lw * 15360, lds00 + lw * 15360, lane * 16);
    {
      set_m0(lds00 + K4_SD + lw * 3072);
      const char* hs = ws + WS_H + ((mt5 >> 1) & 1) * 40 * PLANE + (mt5 >> 2) * 4096 + ((mt5 & 1) * 2 + (lw >> 1)) * 1024;
      const unsigned vo = (lane >> 5) * PLANE + (((lane >> 4) & 1) * 32 + (lw & 1) * 16 + (lane & 15)) * 16 + 2048;
      dma<0>(hs + pb * 2 * PLANE - 2048, vo); dma<1024>(hs + (pb + 1) * 2 * PLANE - 3072, vo); dma<2048>(hs + ps * 2 * PLANE - 4096, vo);
    }
    lds_wait(ctl, 4);
    dma_long<20>(wd + 61440 + lw * 20480, lds0 + lw * 20480, lane * 16);
    wait_vm_bar<0>();
#pragma unroll 1
    for (int g = 0; g < 5; ++g) {
      lds_wait4(ctl + 16, g + 1);
      dma_run<5>(wd + 143360 + g * 20480 + lw * 5120, lds00 + g * 20480 + lw * 5120, lane * 16);
      wait_vm<0>();
      lds_post(ctl + 32 + lw * 4, g + 1, lane);
    }
    return;
  }
  if (threadIdx.x < 12) ((LAS unsigned*)ctl)[threadIdx.x] = 0u;
  const int r32 = lane & 31, hi = lane >> 5, wm = w >> 1, wn = w & 1;
  TS_DECL;
  f32x16 acc[2][3];
#pragma unroll
  for (int i = 0; i < 2; ++i)
#pragma unroll
    for (int j = 0; j < 3; ++j) { acc[i][j] = (f32x16){}; pin(acc[i][j]); }
  bf16x8 af[2][2], wf[2][3];
  const int aoff = wm * 2048 + lane * 16, woff = 8192 + wn * 3072 + lane * 16;
  auto rda = [&](ldsp sb, int s, int i) { af[s][i] = lds_ld16(sb + aoff + s * 4096 + i * 1024); };
  auto rdw = [&](ldsp sb, int s, int j) { wf[s][j] = lds_ld16(sb + woff + s * 6144 + j * 1024); };
  auto mm = [&](int s, int i, int j) { acc[i][j] = mfma32(wf[s][j], af[s][i], acc[i][j]); };
  bar();
  const int tok = mt * 128 + wm * 64 + r32;
  f32x4 sv[2][2];
  float cv[10];
  const LAS float* cp = (const LAS float*)((ldsp)smem + K4_SD + 8192) + w * 48 + (lane < 48 ? lane : 47);
  ldsp sp = (ldsp)smem + K4_SD + hi * 4096 + (wm * 64 + r32) * 32;
  TS();
  rda(lds, 0, 0); rdw(lds, 0, 0); rda(lds, 0, 1); rdw(lds, 0, 1); rdw(lds, 0, 2); SB0();
  constexpr float GC0 = -2.f * 0.7978845608028654f * 1.4426950408889634f, GC1 = GC0 * 0.044715f;
  float ca[2], cb[2], nmu[2], var[2];
  sfor<0, K4_NT>([&](auto tc) {
    constexpr int t = decltype(tc)::value;
    constexpr bool more = t + 1 < K4_NT;
    ldsp sb = lds + (t % K4_NS) * K4_STG;
    ldsp nb = lds + ((t + 1) % K4_NS) * K4_STG;
    mm(0, 0, 0); rda(sb, 1, 0); rdw(sb, 1, 0); SB0();
    mm(0, 1, 0); rda(sb, 1, 1); rdw(sb, 1, 1); SB0();
    mm(0, 0, 1); rdw(sb, 1, 2); SB0();
    mm(0, 1, 1); SB0();
    mm(0, 0, 2); SB0();
    mm(0, 1, 2); SB0();
    if (more) { bar(); SB0(); }
    else { asm volatile("s_waitcnt lgkmcnt(0)" ::: "memory"); lds_add(ctl, lane); SB0(); }
    mm(1, 0, 0); if (more) { rda(nb, 0, 0); rdw(nb, 0, 0); } SB0();
    mm(1, 1, 0); if (more) { rda(nb, 0, 1); rdw(nb, 0, 1); } SB0();
    mm(1, 0, 1); if (more) rdw(nb, 0, 2); SB0();
    if constexpr (t == 3) { cv[8] = cp[8 * 192]; cv[9] = cp[9 * 192]; }
    if constexpr (t >= 4 && t <= 7) { cv[2 * t - 8] = cp[(2 * t - 8) * 192]; cv[2 * t - 7] = cp[(2 * t - 7) * 192]; sv[(t - 4) >> 1][t & 1] = *(const LAS f32x4*)(sp + ((t - 4) >> 1) * 1024 + (t & 1) * 16); }
    if constexpr (t == 8) { if (lane < 48) ((LAS float*)(ctl + 1024))[w * 48 + lane] = (((cv[0] + cv[1]) + (cv[2] + cv[3])) + ((cv[4] + cv[5]) + (cv[6] + cv[7]))) + (cv[8] + cv[9]); }
    if constexpr (t == 9 || t == 10) {
      constexpr int i = t - 9;
      const f32x4 s0 = sv[i][0], s1 = sv[i][1];
      const float mu = xsum32((s0[0] + s0[2]) + (s1[0] + s1[2])) * 0.125f;
      const float d0 = s0[0] - mu, d1 = s0[2] - mu, d2 = s1[0] - mu, d3 = s1[2] - mu;
      var[i] = xsum32((s0[1] + s0[3]) + (s1[1] + s1[3]) + 80.f * ((d0 * d0 + d1 * d1) + (d2 * d2 + d3 * d3))) * (1.f / D_) + LN_EPS;
      const float rstd = __builtin_amdgcn_rsqf(var[i]);
      ca[i] = GC0 * rstd; cb[i] = GC1 * rstd * rstd * rstd; nmu[i] = -mu;
    }
    mm(1, 1, 1);
    mm(1, 0, 2);
    mm(1, 1, 2);
    if constexpr (t == 9 || t == 10) { pin(ca[t - 9]); pin(cb[t - 9]); pin(nmu[t - 9]); pin(var[t - 9]); }
    SB0();
  });
  TS();
  const rsrc_t rw = mk_rsrc(ws, WS_END);
  const LAS float* csb = (const LAS float*)(ctl + 1024) + wn * 96 + 4 * hi;
  f32x4 cq[3][4];
#pragma unroll
  for (int j = 0; j < 3; ++j)
#pragma unroll
    for (int b = 0; b < 4; ++b) cq[j][b] = *(const LAS f32x4*)(csb + j * 32 + 8 * b);
#pragma unroll
  for (int i = 0; i < 2; ++i) {
    const int tk = tok + 32 * i;
    const int ob = WS_ACT + (((tk >> 6) * 48 + ft * 3) * 4 + ((tk >> 4) & 3)) * 1024 + (hi * 16 + (tk & 15)) * 16;
#pragma unroll
    for (int j = 0; j < 3; ++j) {
      unsigned pk[4];
#pragma unroll
      for (int r = 0; r < 8; r += 2) {
        const f32x2 gv = (f32x2){acc[i][j][r], acc[i][j][r + 1]} + nmu[i] * (f32x2){cq[j][r >> 2][r & 3], cq[j][r >> 2][(r & 3) + 1]};
        const f32x2 uv = (f32x2){acc[i][j][8 + r], acc[i][j][9 + r]} + nmu[i] * (f32x2){cq[j][2 + (r >> 2)][r & 3], cq[j][2 + (r >> 2)][(r & 3) + 1]};
        const f32x2 arg = gv * (gv * gv * cb[i] + ca[i]);
        const f32x2 ex = {__builtin_amdgcn_exp2f(arg[0]), __builtin_amdgcn_exp2f(arg[1])};
        const f32x2 den = ex * var[i] + var[i];
        const f32x2 rc = {__builtin_amdgcn_rcpf(den[0]), __builtin_amdgcn_rcpf(den[1])};
        const f32x2 a = gv * uv * rc;
        pk[r >> 1] = cvtpk(a[0], a[1]);
      }
      const int T = wn * 3 + j;
      st16<AUX4>(rw, ob + (T >> 1) * 4096 + (T & 1) * 512, (u32x4){pk[0], pk[1], pk[2], pk[3]});
    }
  }
  TS();
  wait_vm<0>();
  if (lds_add(ctl + 4 + wm * 4, lane) == 1 && lane < (8 >> CSH)) cnt_add(ws, 256 + ((mt * 2 + wm) * 8 >> CSH) + lane);
  TS();
  if (w == 0) { __builtin_amdgcn_s_sleep(PDLY); cnt_wait(ws, 256 + ((mt5 * 8 + ct) >> CSH), 16); if (lane == 0) *(volatile LAS unsigned*)(ctl + 12) = 1u; }
  else lds_wait(ctl + 12, 1);
  TS();
  constexpr int WIN = WIN5;
  bf16x8 a5[WIN][2], w5[2][5];
  const int ao = WS_ACT + mt5 * 196608 + (w & 1) * 4096 + w * 1024 + lane * 16;
#pragma unroll
  for (int t = 0; t < WIN; ++t) { a5[t][0] = ld16_sc1(rw, ao, t * 8192); a5[t][1] = ld16_sc1(rw, ao ^ 1024, t * 8192); }
  SB0();
  bar();
  TS();
  f32x4 ac5[5];
  {
    ldsp rp = (ldsp)smem + K4_SD + w * 3072 + lane * 16;
#pragma unroll
    for (int j = 0; j < 5; ++j) {
      const int e = odd ? (j == 0 ? 5 : j - 1) : j;
      const u32x2 r = *(const LAS u32x2*)(rp + (e >> 1) * 1024 + (e & 1) * 8);
      ac5[j] = (f32x4){bflo(r[0]), bfhi(r[0]), bflo(r[1]), bfhi(r[1])};
    }
  }
  f32x4 ad5[5];
#pragma unroll
  for (int j = 0; j < 5; ++j) ad5[j] = (f32x4){0.f, 0.f, 0.f, 0.f};
  ldsp wp = (ldsp)smem + (w & 1) * 5120 + lane * 16;
#pragma unroll
  for (int j = 0; j < 5; ++j) w5[0][j] = lds_ld16(wp + j * 1024);
  sfor<0, 24>([&](auto tc) {
    constexpr int t = decltype(tc)::value, tn = t + 1;
    if constexpr (tn >= 14 && tn < 24 && (tn & 1) == 0) { lds_wait4(ctl + 32, (tn - 14) / 2 + 1); SB0(); }
    if constexpr (tn == REL0 || tn == REL1 || tn == REL2 || tn == REL3 || tn == REL4) {
      asm volatile("" ::: "memory");
      lds_post(ctl + 16 + w * 4, tn == REL0 ? 1 : tn == REL1 ? 2 : tn == REL2 ? 3 : tn == REL3 ? 4 : 5, lane);
    }
#pragma unroll
    for (int j = 0; j < 5; ++j) {
      if constexpr (tn < 24) w5[tn & 1][j] = lds_ld16(wp + (tn % 14) * 10240 + j * 1024);
      ad5[j] = mfma16(w5[t & 1][j], a5[t % WIN][1], ad5[j]);
    }
#pragma unroll
    for (int j = 0; j < 5; ++j) ac5[j] = mfma16(w5[t & 1][j], a5[t % WIN][0], ac5[j]);
    if constexpr (t + WIN < 24) { a5[t % WIN][0] = ld16_sc1(rw, ao, (t + WIN) * 8192); a5[t % WIN][1] = ld16_sc1(rw, ao ^ 1024, (t + WIN) * 8192); }
    SB0();
  });
  TS();
#pragma unroll
  for (int j = 0; j < 5; ++j) *(LAS f32x4*)((ldsp)smem + 102400 + (w * 5 + j) * 1024 + lane * 16) = ad5[j];
  bar();
#pragma unroll
  for (int j = 0; j < 5; ++j) ac5[j] += *(const LAS f32x4*)((ldsp)smem + 102400 + ((w ^ 1) * 5 + j) * 1024 + lane * 16);
  TS();
  {
    const int g = lane >> 4;
    u32x2 hv[5];
#pragma unroll
    for (int j = 0; j < 5; ++j) hv[j] = (u32x2){cvtpk(ac5[j][0], ac5[j][1]), cvtpk(ac5[j][2], ac5[j][3])};
    const rsrc_t ro = mk_rsrc(out, S_ * D_ * 2);
    const int oo = (mt5 * 64 + w * 16 + (lane & 15)) * 1280 + ct * 160;
#pragma unroll
    for (int jp = 0; jp < 2; ++jp) {
      auto r0 = __builtin_amdgcn_permlane16_swap(hv[2 * jp][0], hv[2 * jp + 1][0], false, false);
      auto r1 = __builtin_amdgcn_permlane16_swap(hv[2 * jp][1], hv[2 * jp + 1][1], false, false);
      st16<AUX5>(ro, oo + jp * 64 + (g & 1) * 32 + (g >> 1) * 16, (u32x4){r0[0], r1[0], r0[1], r1[1]});
    }
    st8<AUX5>(ro, oo + 128 + g * 8, hv[4]);
  }
  TS();
  if (w == 0) { TS_END(3); }
}

extern "C" __global__ void k_text_end() {}

extern "C" void kernel_launch(void* const* d_in, const int* in_sizes, int n_in, void* d_out, int out_size, void* d_ws, size_t ws_size,
                              hipStream_t stream) {
  const u16* x = (const u16*)d_in[0];
  const u16* wqkv = (const u16*)d_in[1];
  const u16* wo = (const u16*)d_in[2];
  const u16* wg = (const u16*)d_in[4];
  const u16* wu = (const u16*)d_in[5];
  const u16* wd = (const u16*)d_in[6];
  char* ws = (char*)d_ws;
  u16* out = (u16*)d_out;
  (void)hipFuncSetAttribute((const void*)k_qkv, hipFuncAttributeMaxDynamicSharedMemorySize, K1_LDS);
  (void)hipFuncSetAttribute((const void*)k_attn, hipFuncAttributeMaxDynamicSharedMemorySize, K2_LDS);
  (void)hipFuncSetAttribute((const void*)k_mlp, hipFuncAttributeMaxDynamicSharedMemorySize, K4_LDS);
  k_qkv<<<256, 768, K1_LDS, stream>>>(x, wqkv, wo, wd, ws);
  k_attn<<<256, 768, K2_LDS, stream>>>(ws, x, wg, wu, wd);
  k_mlp<<<256, 512, K4_LDS, stream>>>(ws, out);
}
```

```cpp
#include <hip/hip_runtime.h>
#include <stdint.h>
#include <type_traits>

#define LAS __attribute__((address_space(3)))
#define DI __device__ __forceinline__
typedef unsigned short u16;
typedef short bf16x8 __attribute__((ext_vector_type(8)));
typedef short bf16x4 __attribute__((ext_vector_type(4)));
typedef __bf16 bf16x2_t __attribute__((ext_vector_type(2)));
typedef float f32x2 __attribute__((ext_vector_type(2)));
typedef float f32x4 __attribute__((ext_vector_type(4)));
typedef float f32x16 __attribute__((ext_vector_type(16)));
typedef unsigned u32x4 __attribute__((ext_vector_type(4)));
typedef unsigned u32x2 __attribute__((ext_vector_type(2)));
typedef LAS char* ldsp;
typedef __amdgpu_buffer_rsrc_t rsrc_t;

constexpr int S_ = 2048, D_ = 640, NH = 32, DF = 1536;
constexpr float LN_EPS = 1e-5f;
constexpr float QSCALE = 0.22360679774997896f * 1.4426950408889634f;
constexpr unsigned ONES2 = 0x3f803f80u;
#define AUX_ST 16

constexpr int PLANE = S_ * 16;
constexpr int WS_QK = 0;
constexpr int WS_ACT = WS_QK + 80 * PLANE;
constexpr int WS_H = WS_ACT + S_ * DF * 2;
constexpr int WS_WO = WS_H + S_ * D_ * 2;
constexpr int WS_ST = WS_WO + D_ * D_ * 2;
constexpr int WS_CS = WS_ST + S_ * 8 * 8;
constexpr int WS_WGU = WS_CS + 16 * 8192;
constexpr int WS_WD = WS_WGU + D_ * DF * 4;
static_assert(WS_WO >= 0 && WS_QK % 4096 == 0, "workspace map");
constexpr int WS_CNT = WS_WD + D_ * DF * 2;
#define CSTR 64
constexpr int WS_END = WS_CNT + 512 * CSTR * 4;

#define TS_DECL
#define TS()
#define TS_END(kid)

template <int I, int N, class F> DI void sfor(F&& f) {
  if constexpr (I < N) { f(std::integral_constant<int, I>{}); sfor<I + 1, N>(f); }
}
#define SB0() __builtin_amdgcn_sched_barrier(0)
DI unsigned cvtpk(float lo, float hi) { f32x2 v = {lo, hi}; return __builtin_bit_cast(unsigned, __builtin_convertvector(v, bf16x2_t)); }
DI float bflo(unsigned u) { return __uint_as_float(u << 16); }
DI float bfhi(unsigned u) { return __uint_as_float(u & 0xffff0000u); }
DI float dot2(unsigned a, unsigned b, float c) {
  return __builtin_amdgcn_fdot2_f32_bf16(__builtin_bit_cast(bf16x2_t, a), __builtin_bit_cast(bf16x2_t, b), c, false);
}
DI f32x4 mfma16(bf16x8 a, bf16x8 b, f32x4 c) { return __builtin_amdgcn_mfma_f32_16x16x32_bf16(a, b, c, 0, 0, 0); }
DI f32x16 mfma32(bf16x8 a, bf16x8 b, f32x16 c) { return __builtin_amdgcn_mfma_f32_32x32x16_bf16(a, b, c, 0, 0, 0); }
DI void set_m0(unsigned v) { asm volatile("s_mov_b32 m0, %0\n\ts_nop 0" ::"s"(v) : "memory"); }
template <int OFF> DI void dma(const void* sbase, unsigned voff) {
  asm volatile("global_load_lds_dwordx4 %0, %1 offset:%2" ::"v"(voff), "s"(sbase), "n"(OFF) : "memory");
}
template <int OFF> DI void dma_nt(const void* sbase, unsigned voff) {
  asm volatile("global_load_lds_dwordx4 %0, %1 offset:%2 nt" ::"v"(voff), "s"(sbase), "n"(OFF) : "memory");
}
template <int OFF> DI void gld16(u32x4& d, const void* sbase, unsigned voff) {
  asm volatile("global_load_dwordx4 %0, %1, %2 offset:%3" : "=v"(d) : "v"(voff), "s"(sbase), "n"(OFF) : "memory");
}
template <int N> DI void wait_vm() { asm volatile("s_waitcnt vmcnt(%0)" ::"n"(N) : "memory"); }
template <int N> DI void wait_vm_bar() { asm volatile("s_waitcnt vmcnt(%0) lgkmcnt(0)\n\ts_barrier" ::"n"(N) : "memory"); }
DI void bar() { asm volatile("s_waitcnt lgkmcnt(0)\n\ts_barrier" ::: "memory"); }
template <class T> DI void pin(T& v) { asm volatile("" : "+v"(v)); }
DI bf16x8 lds_ld16(ldsp p) { return *(const LAS bf16x8*)p; }
DI bf16x4 lds_tr(ldsp p) { return __builtin_amdgcn_ds_read_tr16_b64_v4i16((LAS bf16x4*)p); }
DI bf16x8 lds_tr2(ldsp p0, ldsp p1) {
  bf16x4 a = lds_tr(p0), b = lds_tr(p1);
  return __builtin_shufflevector(a, b, 0, 1, 2, 3, 4, 5, 6, 7);
}
DI float xsum32(float v) {
  auto r = __builtin_amdgcn_permlane32_swap(__float_as_uint(v), __float_as_uint(v), false, false);
  return __uint_as_float(r[0]) + __uint_as_float(r[1]);
}
DI float xsum16_32(float v) {
  auto r = __builtin_amdgcn_permlane16_swap(__float_as_uint(v), __float_as_uint(v), false, false);
  return xsum32(__uint_as_float(r[0]) + __uint_as_float(r[1]));
}
DI rsrc_t mk_rsrc(void* p, int bytes) { return __builtin_amdgcn_make_buffer_rsrc(p, 0, bytes, 0x00020000); }
template <int AUX = 16> DI void st16(rsrc_t r, int off, u32x4 v) { __builtin_amdgcn_raw_buffer_store_b128(v, r, off, 0, AUX); }
template <int AUX = 16> DI void st8(rsrc_t r, int off, u32x2 v) { __builtin_amdgcn_raw_buffer_store_b64(v, r, off, 0, AUX); }
#define CSH 2
#define PDLY 0
#define PSLP 2
#define REL0 2
#define REL1 6
#define REL2 10
#define REL3 13
#define REL4 16
#define SLP4 1
#define LDAUX 16
#define WIN5 4
#define AUX3 0
#define AUX5 0
#define AUX4 16

template <int P, int NT, int NS, int X, class F, class G0, class G> DI void loader_loop(F&& issue, G0&& early, G&& extra) {
  static_assert(NS >= 4, "three stages in flight + one being read");
  issue(0); issue(1);
  wait_vm_bar<P>();
  early();
  issue(2); issue(3);
  int slot = 4 % NS;
#pragma unroll 1
  for (int t = 1; t < NT - 3; ++t) {
    wait_vm_bar<2 * P>();
    issue(slot);
    slot = slot == NS - 1 ? 0 : slot + 1;
  }
  extra();
  wait_vm_bar<2 * P + X>(); wait_vm_bar<P + X>(); wait_vm_bar<X>();
}
DI void touch(const char* p, int lane, int nlines, unsigned lds_scratch) {
  set_m0(lds_scratch);
  asm volatile("global_load_lds_dword %0, %1" ::"v"((unsigned)((lane < nlines ? lane : nlines - 1) * 128)), "s"(p) : "memory");
}
#define CODE_ADDR(dst, sym) asm volatile("s_getpc_b64 s[40:41]\n\ts_add_u32 s40, s40, " #sym "@rel32@lo+4\n\ts_addc_u32 s41, s41, " #sym "@rel32@hi+12\n\ts_mov_b64 %0, s[40:41]" : "=s"(dst) :: "s40", "s41", "scc")
template <int P> DI void dma_run(const char* src, unsigned dst, unsigned lane16) {
  static_assert(P <= 8, "offset range");
  set_m0(dst + 4096);
  sfor<0, P>([&](auto ic) { dma<decltype(ic)::value * 1024 - 4096>(src + 4096, lane16); });
}
template <int P, int NT, int NS, int SSTR, int LSTR, int X, class G0, class G> DI void linear_loader(const char* src, unsigned dst, unsigned lane16, G0&& early, G&& extra) {
  loader_loop<P, NT, NS, X>([&](int slot) { dma_run<P>(src, dst + slot * LSTR, lane16); src += SSTR; }, early, extra);
}

template <int N> DI void dma_long(const char* src, unsigned dst, unsigned lane16) {
  sfor<0, N / 5>([&](auto ic) { constexpr int i = decltype(ic)::value; dma_run<5>(src + i * 5120, dst + i * 5120, lane16); });
}
DI unsigned lds_add(ldsp p, int lane) {
  unsigned old = 0;
  if (lane == 0) old = __hip_atomic_fetch_add((LAS unsigned*)p, 1u, __ATOMIC_RELAXED, __HIP_MEMORY_SCOPE_WORKGROUP);
  return __builtin_amdgcn_readfirstlane(old);
}
DI void lds_wait(ldsp p, unsigned want) {
  for (int sp = 0; sp < (1 << 22); ++sp) {
    if ((unsigned)__builtin_amdgcn_readfirstlane(*(volatile LAS unsigned*)p) >= want) break;
    __builtin_amdgcn_s_sleep(1);
  }
  asm volatile("" ::: "memory");
}
DI void lds_post(ldsp p, unsigned v, int lane) { if (lane == 0) *(volatile LAS unsigned*)p = v; asm volatile("" ::: "memory"); }
DI void lds_wait4(ldsp p, unsigned want) {
  for (int sp = 0; sp < (1 << 22); ++sp) {
    const u32x4 v = *(volatile LAS u32x4*)p;
    if ((unsigned)__builtin_amdgcn_readfirstlane(min(min(v[0], v[1]), min(v[2], v[3]))) >= want) break;
    __builtin_amdgcn_s_sleep(SLP4);
  }
  asm volatile("" ::: "memory");
}
DI void cnt_add(char* ws, int idx) { __hip_atomic_fetch_add((unsigned*)(ws + WS_CNT) + idx * CSTR, 1u, __ATOMIC_RELAXED, __HIP_MEMORY_SCOPE_AGENT); }
DI void cnt_wait(char* ws, int idx, unsigned want) {
  unsigned* p = (unsigned*)(ws + WS_CNT) + idx * CSTR;
  for (int sp = 0; sp < (1 << 18); ++sp) {
    if ((unsigned)__builtin_amdgcn_readfirstlane(__hip_atomic_load(p, __ATOMIC_RELAXED, __HIP_MEMORY_SCOPE_AGENT)) >= want) break;
    __builtin_amdgcn_s_sleep(PSLP);
  }
  __builtin_amdgcn_fence(__ATOMIC_ACQUIRE, "wavefront");
}
DI bf16x8 ld16_sc1(rsrc_t r, int voff, int soff) { return __builtin_bit_cast(bf16x8, __builtin_amdgcn_raw_buffer_load_b128(r, voff, soff, LDAUX)); }

template <int KIND> DI void pack_issue(const u16* wsrc, const u16* wsrc2, int ks, int cg, unsigned ldsdst, int lane) {
  constexpr int PITCH = KIND == 1 ? DF * 2 : D_ * 2;
  set_m0(ldsdst);
  sfor<0, 4>([&](auto ic) {
    constexpr int g = decltype(ic)::value;
    const int t = (lane >> 5) & 1, q = (lane >> 3) & 3;
    int row;
    if (KIND == 0) { const int P = 4 * ks + g, hp = P / 5, r = P - 5 * hp; row = 40 * hp + q + (r < 2 ? 8 * t + 4 * r : r == 2 ? 16 + 4 * t : 12 + 8 * t + 4 * r); }
    else if (KIND == 1) row = 32 * (ks >> 1) + 16 * t + 4 * (2 * (ks & 1) + (g & 1)) + q;
    else row = 32 * ks + 16 * (g >> 1) + 8 * t + 4 * (g & 1) + q;
    dma_nt<g * 1024>((const char*)(KIND == 1 && g >= 2 ? wsrc2 : wsrc) + cg * 128 - 4096, (unsigned)(row * PITCH + (lane & 7) * 16 + 4096 - g * 1024));
  });
}
template <int KIND> DI void pack_finish(ldsp u, rsrc_t rw, int ks, int cg, int lane, LAS float* cs) {
  const int q = (lane & 15) >> 2, p = lane & 3;
  ldsp a = KIND == 1 ? u + ((lane >> 4) & 1) * 2048 + (8 * (lane >> 5) + q) * 128 + p * 8 : u + (8 * (lane >> 4) + q) * 128 + p * 8;
#pragma unroll
  for (int i = 0; i < 4; ++i) {
    const int nt = 4 * cg + i;
    const u32x4 f = __builtin_bit_cast(u32x4, lds_tr2(a + i * 32, a + i * 32 + 512));
    if (KIND == 1) {
      const int ft = nt / 6, j = nt - ft * 6;
      st16(rw, WS_WGU + ((ft * 40 + ks) * 6 + j) * 1024 + lane * 16, f);
      const unsigned sv = (lane & 15) == ((lane >> 4) & 1) ? ONES2 : 0u;
      const f32x4 c = mfma16(__builtin_bit_cast(bf16x8, (u32x4){sv, sv, sv, sv}), __builtin_bit_cast(bf16x8, f), (f32x4){0.f, 0.f, 0.f, 0.f});
      if (lane < 16) { cs[i * 32 + lane] = c[0]; cs[i * 32 + 16 + lane] = c[1]; }
    } else {
      const int ct = nt / 5, j = nt - ct * 5;
      st16(rw, (KIND == 0 ? WS_WO : WS_WD) + ((ct * (KIND == 0 ? 20 : 48) + ks) * 5 + j) * 1024 + lane * 16, f);
    }
  }
}

#define STATMODE 1
constexpr int NWD_A = 352;
#define WD0 8
#define WD1 24
constexpr int K1_NS = 4, K1_STG = 32768, K1_NT = 10, K1_CTL = K1_NS * K1_STG, K1_LDS = K1_CTL + 4096;

DI void qkv_compute(ldsp lds, char* __restrict__ ws, int mt, int nt, int w, int lane) {
  const int wm = w >> 1, wn = w & 1, r32 = lane & 31, hi = lane >> 5, q = (lane & 15) >> 2, p = lane & 3, blk = (lane >> 4) & 1;
  TS_DECL;
  ldsp ctl = lds + K1_CTL;
  int aoff[4], woff[2];
#pragma unroll
  for (int s = 0; s < 4; ++s) aoff[s] = (wm * 64 + r32) * 128 + (((2 * s + hi) ^ ((r32 >> 1) & 7)) * 16);
#pragma unroll
  for (int j = 0; j < 2; ++j) woff[j] = 16384 + (8 * hi + q) * 256 + (((wn * 2 + (j ^ wm)) ^ q) * 64) + blk * 32 + p * 8;
  f32x16 acc[2][2];
#pragma unroll
  for (int j = 0; j < 2; ++j)
#pragma unroll
    for (int i = 0; i < 2; ++i) { acc[j][i] = (f32x16){}; pin(acc[j][i]); }
  f32x4 cs = {};
  const unsigned sv = (lane & 15) == blk ? ONES2 : 0u;
  const bf16x8 sel = __builtin_bit_cast(bf16x8, (u32x4){sv, sv, sv, sv});
  bf16x8 xf[2][2], wf[2][2];
  auto rdx = [&](ldsp sb, int s, int i) { xf[s & 1][i] = lds_ld16(sb + aoff[s] + i * 4096); };
  auto rdw = [&](ldsp sb, int s, int j) { wf[s & 1][j] = lds_tr2(sb + woff[j] + s * 4096, sb + woff[j] + s * 4096 + 1024); };
  auto mm = [&](int s, int j, int i) { acc[j][i] = mfma32(wf[s & 1][j], xf[s & 1][i], acc[j][i]); };
  auto step = [&](ldsp nb, int s, int sn, bool rd) {
    cs = mfma16(sel, wf[s & 1][0], cs);
    mm(s, 0, 0); if (rd) { rdx(nb, sn, 0); rdw(nb, sn, 0); } SB0();
    mm(s, 0, 1); if (rd) { rdx(nb, sn, 1); rdw(nb, sn, 1); } SB0();
    mm(s, 1, 0); SB0();
    mm(s, 1, 1); SB0();
  };
  bar();
  TS();
  ldsp sb = lds;
  rdx(sb, 0, 0); rdw(sb, 0, 0); rdx(sb, 0, 1); rdw(sb, 0, 1); SB0();
  int slot = 0;
#pragma unroll 1
  for (int t = 0; t < K1_NT - 1; ++t) {
    step(sb, 0, 1, true); step(sb, 1, 2, true); step(sb, 2, 3, true);
    slot = slot == K1_NS - 1 ? 0 : slot + 1;
    sb = lds + slot * K1_STG;
    bar(); SB0();
    step(sb, 3, 0, true);
  }
  step(sb, 0, 1, true); step(sb, 1, 2, true); step(sb, 2, 3, true);
  const LAS f32x2* rst = (const LAS f32x2*)(ctl + 1024);
  LAS float* cst = (LAS float*)(ctl + 2048);
  cs = mfma16(sel, wf[1][0], cs);
  mm(3, 0, 0); mm(3, 0, 1); SB0();
  if (lane < 16) { cst[wn * 64 + wm * 32 + lane] = cs[0]; cst[wn * 64 + wm * 32 + 16 + lane] = cs[1]; }
  mm(3, 1, 0); mm(3, 1, 1); SB0();
  TS();
  bar();
  TS();
  const rsrc_t rw = mk_rsrc(ws, WS_END);
  const float sc = nt < 5 ? QSCALE : 1.f;
  f32x4 c4[2][4];
#pragma unroll
  for (int j = 0; j < 2; ++j)
#pragma unroll
    for (int b = 0; b < 4; ++b) c4[j][b] = *(const LAS f32x4*)(cst + wn * 64 + (j ^ wm) * 32 + 8 * b + 4 * hi);
#pragma unroll
  for (int i = 0; i < 2; ++i) {
    const int ml = wm * 64 + i * 32 + r32;
    const f32x2 st = rst[ml];
    const float a = st.y * sc, nm = -st.x * a;
    const int ob = WS_QK + ((nt * 16 + wn * 8 + hi) * S_ + mt * 128 + ml) * 16;
#pragma unroll
    for (int j = 0; j < 2; ++j)
#pragma unroll
      for (int bp = 0; bp < 2; ++bp) {
        unsigned pk[2][2];
#pragma unroll
        for (int e = 0; e < 2; ++e) {
          const int b = 2 * bp + e;
          const f32x2 v0 = (f32x2){acc[j][i][4 * b], acc[j][i][4 * b + 1]} * a + nm * (f32x2){c4[j][b][0], c4[j][b][1]};
          const f32x2 v1 = (f32x2){acc[j][i][4 * b + 2], acc[j][i][4 * b + 3]} * a + nm * (f32x2){c4[j][b][2], c4[j][b][3]};
          pk[e][0] = cvtpk(v0[0], v0[1]); pk[e][1] = cvtpk(v1[0], v1[1]);
        }
        auto r0 = __builtin_amdgcn_permlane32_swap(pk[0][0], pk[1][0], false, false);
        auto r1 = __builtin_amdgcn_permlane32_swap(pk[0][1], pk[1][1], false, false);
        st16(rw, ob + ((j ^ wm) * 4 + bp * 2) * S_ * 16, (u32x4){r0[0], r1[0], r0[1], r1[1]});
      }
  }
  TS();
  if (w == 0) { TS_END(0); }
}

DI void qkv_stats(ldsp lds, int sw, int lane) {
  const int fr = lane & 15, g = lane >> 4;
  const int off = (sw * 32 + fr) * 128, x = fr >> 1;
  const bf16x8 ones = __builtin_bit_cast(bf16x8, (u32x4){ONES2, ONES2, ONES2, ONES2});
  f32x4 sm[2] = {}, sq[2] = {};
  int slot = 0;
#pragma unroll 1
  for (int t = 0; t < K1_NT; ++t) {
    bar();
    ldsp sb = lds + slot * K1_STG + off;
    slot = slot == K1_NS - 1 ? 0 : slot + 1;
    bf16x8 v[2][2];
#pragma unroll
    for (int ks = 0; ks < 2; ++ks)
#pragma unroll
      for (int i = 0; i < 2; ++i) v[ks][i] = lds_ld16(sb + i * 2048 + (((4 * ks + g) ^ x) * 16));
#pragma unroll
    for (int ks = 0; ks < 2; ++ks)
#pragma unroll
      for (int i = 0; i < 2; ++i) { sq[i] = mfma16(v[ks][i], v[ks][i], sq[i]); sm[i] = mfma16(ones, v[ks][i], sm[i]); }
  }
#pragma unroll
  for (int i = 0; i < 2; ++i) {
    const int p = lane & 3;
    const float q = p == 0 ? sq[i][0] : p == 1 ? sq[i][1] : p == 2 ? sq[i][2] : sq[i][3];
    const float mu = sm[i][0] * (1.f / D_), var = fmaxf(q * (1.f / D_) - mu * mu, 0.f);
    if ((fr >> 2) == g) ((LAS f32x2*)(lds + K1_CTL + 1024))[sw * 32 + i * 16 + fr] = (f32x2){mu, __builtin_amdgcn_rsqf(var + LN_EPS)};
  }
  asm volatile("s_waitcnt lgkmcnt(0)" ::: "memory");
}

DI void qkv_loader(ldsp lds, const u16* __restrict__ x, const u16* __restrict__ wqkv, int mt, int nt, int lw, int lane) {
  const bool isx = lw < 2;
  const int half = lw & 1;
  unsigned voff[8];
#pragma unroll
  for (int i = 0; i < 8; ++i) {
    const int r = (half * 8 + i) * 8 + (lane >> 3), c = (lane & 7) ^ ((r >> 1) & 7);
    const int k = (half * 8 + i) * 4 + (lane >> 4), cp = lane & 15;
    voff[i] = (isx ? r * 1280 + c * 16 : k * 3840 + (cp ^ ((k & 3) << 2)) * 16) + 8192 - i * 1024;
  }
  const char* src = (isx ? (const char*)x + (size_t)mt * 128 * 1280 : (const char*)wqkv + nt * 256) - 4096;
  const int sstr = isx ? 128 : 64 * 3840;
  const unsigned dst = (unsigned)(uintptr_t)lds + lw * 8192 + 4096;
  loader_loop<8, K1_NT, K1_NS, 0>([&](int slot) {
    set_m0(dst + slot * K1_STG);
    sfor<0, 8>([&](auto ic) { constexpr int i = decltype(ic)::value; dma<i * 1024 - 4096>(src, voff[i]); });
    src += sstr;
  }, [] {}, [] {});
}

extern "C" __global__ __launch_bounds__(768, 3) void k_qkv(const u16* __restrict__ x, const u16* __restrict__ wqkv, const u16* __restrict__ wo, const u16* __restrict__ wd, char* __restrict__ ws) {
  extern __shared__ __attribute__((aligned(16))) char smem[];
  ldsp lds = (ldsp)smem;
  const int lane = threadIdx.x & 63, w = __builtin_amdgcn_readfirstlane(threadIdx.x >> 6);
  const int xcd = blockIdx.x & 7, idx = blockIdx.x >> 3;
  const int nt = (xcd & 1) * 8 + (idx >> 2), mt = (xcd >> 1) * 4 + (idx & 3);
  if (w < 4 && nt < 15) { qkv_loader(lds, x, wqkv, mt, nt, w, lane); return; }
  if (w == 11 && lane < 2) ((unsigned*)(ws + WS_CNT))[(blockIdx.x * 2 + lane) * CSTR] = 0u;
  if (w == 10) {
    const char *c0, *c1;
    CODE_ADDR(c0, k_qkv); CODE_ADDR(c1, k_text_end);
    const unsigned lim = c1 > c0 ? (unsigned)(c1 - c0) : 0u, off = (idx * 16 + (lane & 15)) * 128;
    set_m0((unsigned)(uintptr_t)lds + K1_CTL + 3072 + 512);
    asm volatile("global_load_lds_dword %0, %1" ::"v"(off < lim ? off : lim), "s"(c0) : "memory");
  }
  if (w == 8 || w == 9) {
    const int id = blockIdx.x * 64 + lane, u = w - 8;
    const bool isx = blockIdx.x < 128;
    const int i2 = id - 8192 < 7680 ? id - 8192 : 7679;
    const unsigned off = isx ? (id >> 2) * 1280 + 256 + u * 512 + (id & 3) * 128 : (128 + u * 256) * 3840 + i2 * 128;
    set_m0((unsigned)(uintptr_t)lds + K1_CTL + 3072 + u * 256);
    const char* src = isx ? (const char*)x : (const char*)wqkv;
    asm volatile("" : "+s"(src));
    if (u == 0) __builtin_amdgcn_s_sleep(WD0); else __builtin_amdgcn_s_sleep(WD1);
    asm volatile("global_load_lds_dword %0, %1" ::"v"(off), "s"(src) : "memory");
  }
  if (nt == 15) {
    const rsrc_t rw = mk_rsrc(ws, WS_END);
    constexpr int NU = (200 + NWD_A + 191) / 192;
    auto iss = [&](int r) {
      const int u = min(mt * 12 + w + r * 192, 200 + NWD_A - 1), v = u - 200;
      if (u < 200) pack_issue<0>(wo, wo, u / 10, u % 10, (unsigned)(uintptr_t)lds + (w * 2 + (r & 1)) * 4096, lane);
      else pack_issue<2>(wd, wd, v / 10, v % 10, (unsigned)(uintptr_t)lds + (w * 2 + (r & 1)) * 4096, lane);
    };
    iss(0); iss(1);
#pragma unroll 1
    for (int r = 0; r < NU; ++r) {
      if (r == 0) wait_vm<4>(); else if (r + 1 < NU) wait_vm<8>(); else wait_vm<4>();
      const int u = min(mt * 12 + w + r * 192, 200 + NWD_A - 1), v = u - 200;
      if (u < 200) pack_finish<0>(lds + (w * 2 + (r & 1)) * 4096, rw, u / 10, u % 10, lane, nullptr);
      else pack_finish<2>(lds + (w * 2 + (r & 1)) * 4096, rw, v / 10, v % 10, lane, nullptr);
      asm volatile("s_waitcnt lgkmcnt(0)" ::: "memory");
      if (r + 2 < NU) iss(r + 2);
    }
    return;
  }
  if (w >= 8) qkv_stats(lds, w - 8, lane);
  else qkv_compute(lds, ws, mt, nt, w - 4, lane);
}

#define LAZY_LIM 1.0995116e12f
#define VLEAD 3
constexpr int K2_K = 102400, K2_V = K2_K + 24576, K2_VP = 8192 + 64, K2_CS = K2_V + 4 * K2_VP, K2_MSK = K2_CS + 2560, K2_FL = K2_MSK + 832, K2_LDS = K2_FL + 64 + 256;
static_assert(K2_LDS <= 163840 && K2_FL % 16 == 0, "LDS map");

DI void n80_epilogue_h(f32x4 (&acc)[5], rsrc_t rw, int mt, int ct, int w, int lane) {
  const int fr = lane & 15, g = lane >> 4, tok = mt * 64 + w * 16 + fr;
  const int odd = ct & 1, pb = (ct >> 1) * 5 + (odd ? 3 : 0), ps = (ct >> 1) * 5 + 2;
  const int hb = WS_H + (((mt >> 1) & 1) * 40 + (g >> 1)) * PLANE + (mt >> 2) * 4096 + ((mt & 1) * 2 + (w >> 1)) * 1024 + ((g & 1) * 32 + (w & 1) * 16 + fr) * 16;
  u32x2 hv[5];
  float s = 0.f;
#pragma unroll
  for (int j = 0; j < 5; ++j) hv[j] = (u32x2){cvtpk(acc[j][0], acc[j][1]), cvtpk(acc[j][2], acc[j][3])};
  const u32x2 a0 = odd ? hv[1] : hv[0], a1 = odd ? hv[2] : hv[1], b0 = odd ? hv[3] : hv[2], b1 = odd ? hv[4] : hv[3], s1 = odd ? hv[0] : hv[4];
  st16<AUX3>(rw, hb + pb * 2 * PLANE, (u32x4){a0[0], a0[1], a1[0], a1[1]});
  st16<AUX3>(rw, hb + (pb + 1) * 2 * PLANE, (u32x4){b0[0], b0[1], b1[0], b1[1]});
  st8<AUX3>(rw, hb + ps * 2 * PLANE + odd * 8, s1);
  f32x4 v[5];
#pragma unroll
  for (int j = 0; j < 5; ++j) { v[j] = (f32x4){bflo(hv[j][0]), bfhi(hv[j][0]), bflo(hv[j][1]), bfhi(hv[j][1])}; s += (v[j][0] + v[j][1]) + (v[j][2] + v[j][3]); }
  const float mean = xsum16_32(s) * (1.f / 80.f);
  float m2 = 0.f;
#pragma unroll
  for (int j = 0; j < 5; ++j) { const f32x4 d = v[j] - mean; m2 += (d[0] * d[0] + d[1] * d[1]) + (d[2] * d[2] + d[3] * d[3]); }
  m2 = xsum16_32(m2);
  if (g == 0) st8<AUX3>(rw, WS_ST + (((ct >> 2) * S_ + tok) * 4 + (ct & 3)) * 8, (u32x2){__float_as_uint(mean), __float_as_uint(m2)});
}

DI void attn_helper(ldsp lds, char* __restrict__ ws, int c, int h, int hw, int lane, const u16* __restrict__ x, const u16* __restrict__ wg, const u16* __restrict__ wu, const u16* __restrict__ wd) {
  const unsigned lds0 = (unsigned)(uintptr_t)lds;
  const int pm = 5 * (h >> 1) + 3 * (h & 1), pt = 5 * (h >> 1) + 2;
  const bool EDGE = c == 0;
  const int tok0 = 256 * c - 256 + 128 * hw + ((EDGE && hw < 2) ? 256 : 0);
  const char* kb = ws + WS_QK + (80 * S_ + tok0) * 16;
  {
    set_m0(lds0 + K2_K + hw * 2048); dma<0>(kb + pm * S_ * 16, lane * 16); dma<1024>(kb + pm * S_ * 16, lane * 16);
    set_m0(lds0 + K2_K + 8192 + hw * 2048); dma<0>(kb + (pm + 1) * S_ * 16, lane * 16); dma<1024>(kb + (pm + 1) * S_ * 16, lane * 16);
    set_m0(lds0 + K2_K + 16384 + hw * 2048); dma<0>(kb + pt * S_ * 16, lane * 16); dma<1024>(kb + pt * S_ * 16, lane * 16);
  }
  TS_DECL;
  const int b = blockIdx.x, xcd = b & 7, idx = b >> 3;
  if (hw == 0 && lane < 8) *(LAS unsigned*)(lds + K2_FL + lane * 4) = 0u;
  {
    const unsigned tm = (EDGE && hw < 2 ? 0xff80u : 0u) | 0xbf800000u;
    *(LAS u32x4*)(lds + K2_V + 3 * K2_VP + hw * 2048 + lane * 16) = (u32x4){tm, 0xbf80bf80u, ONES2, ONES2};
    *(LAS u32x4*)(lds + K2_V + 3 * K2_VP + hw * 2048 + 1024 + lane * 16) = (u32x4){tm, 0xbf80bf80u, ONES2, ONES2};
  }
  if (hw == 1 && lane < 52) {
    const int d = lane - 20;
    u32x4 mk;
#pragma unroll
    for (int e = 0; e < 4; ++e) { const int k0 = ((2 * e) & 3) + 8 * (e >> 1); mk[e] = (k0 > d ? 0xffffu : 0u) | (k0 + 1 > d ? 0xffff0000u : 0u); }
    *(LAS u32x4*)(lds + K2_MSK + lane * 16) = mk;
  }
  wait_vm<VLEAD>();
  {
    const char* vb = kb + 80 * S_ * 16;
    set_m0(lds0 + K2_V + hw * 2048); dma<0>(vb + pm * S_ * 16, lane * 16); dma<1024>(vb + pm * S_ * 16, lane * 16);
    set_m0(lds0 + K2_V + K2_VP + hw * 2048); dma<0>(vb + (pm + 1) * S_ * 16, lane * 16); dma<1024>(vb + (pm + 1) * S_ * 16, lane * 16);
    set_m0(lds0 + K2_V + 2 * K2_VP + hw * 2048); dma<0>(vb + pt * S_ * 16, lane * 16); dma<1024>(vb + pt * S_ * 16, lane * 16);
  }
  const rsrc_t rw = mk_rsrc(ws, WS_END);
  const int mt = xcd * 4 + (idx >> 3), ct = idx & 7;
  const int q0 = hw * 25600;
  const int bb = b, pcg = bb % 24, pks = (bb / 24) * 4 + hw, v0 = NWD_A + (b - 240) * 8 + hw * 2;
  wait_vm_bar<6>();
  if (b < 240) { pack_issue<1>(wg, wu, pks, pcg, lds0 + q0, lane); wait_vm<4>(); }
  else { pack_issue<2>(wd, wd, v0 / 10, v0 % 10, lds0 + q0, lane); pack_issue<2>(wd, wd, v0 / 10, v0 % 10 + 1, lds0 + q0 + 4096, lane); wait_vm<8>(); }
  if (lane == 0) *(volatile LAS unsigned*)(lds + K2_FL + 16 + hw * 4) = 1u;
  TS();
  u32x4 xr[2]; u32x2 xs;
  {
    const char* xp = (const char*)x + (size_t)(mt * 64 + hw * 16) * 1280 + ct * 160;
    const int g = lane >> 4;
    const unsigned o16 = (lane & 15) * 1280 + (g & 1) * 32 + (g >> 1) * 16, o8 = (lane & 15) * 1280 + g * 8;
    asm volatile("global_load_dwordx4 %0, %1, %2" : "=v"(xr[0]) : "v"(o16), "s"(xp) : "memory");
    asm volatile("global_load_dwordx4 %0, %1, %2 offset:64" : "=v"(xr[1]) : "v"(o16), "s"(xp) : "memory");
    asm volatile("global_load_dwordx2 %0, %1, %2 offset:128" : "=v"(xs) : "v"(o8), "s"(xp) : "memory");
  }
  dma_long<15>(ws + WS_WO + ct * 102400 + q0 + 10240, lds0 + q0 + 10240, lane * 16);
  wait_vm<15>();
  pin(xr[0]); pin(xr[1]); pin(xs);
  TS();
  if (b >= 240) {
    pack_finish<2>(lds + q0, rw, v0 / 10, v0 % 10, lane, nullptr); pack_finish<2>(lds + q0 + 4096, rw, v0 / 10, v0 % 10 + 1, lane, nullptr);
    asm volatile("s_waitcnt lgkmcnt(0)" ::: "memory");
  } else {
    LAS float* ct4 = (LAS float*)(lds + K2_CS);
    pack_finish<1>(lds + q0, rw, pks, pcg, lane, ct4 + hw * 128);
    asm volatile("s_waitcnt lgkmcnt(0)" ::: "memory");
    if (lds_add(lds + K2_FL, lane) == 3) {
      asm volatile("" ::: "memory");
      const LAS f32x2* c2 = (const LAS f32x2*)ct4 + lane;
      const f32x2 a = (c2[0] + c2[64]) + (c2[128] + c2[192]);
      const int e = pcg * 128 + lane * 2, ft = e / 192;
      st8(rw, WS_CS + ft * 8192 + ((bb / 24) * 192 + e - ft * 192) * 4, __builtin_bit_cast(u32x2, a));
    }
  }
  TS();
  dma_long<10>(ws + WS_WO + ct * 102400 + q0, lds0 + q0, lane * 16);
  if (hw == 3) {
    const char *c0, *c1;
    CODE_ADDR(c0, k_mlp); CODE_ADDR(c1, k_text_end);
    const unsigned lim = c1 > c0 ? (unsigned)(c1 - c0) : 0u, off = (idx * 8 + (lane & 7)) * 128;
    set_m0(lds0 + K2_FL + 64);
    asm volatile("global_load_lds_dword %0, %1" ::"v"(off < lim ? off : lim), "s"(c0) : "memory");
  }
  f32x4 acc[5];
#pragma unroll
  for (int jp = 0; jp < 2; ++jp) {
    auto r0 = __builtin_amdgcn_permlane16_swap(xr[jp][0], xr[jp][2], false, false);
    auto r1 = __builtin_amdgcn_permlane16_swap(xr[jp][1], xr[jp][3], false, false);
    acc[2 * jp] = (f32x4){bflo(r0[0]), bfhi(r0[0]), bflo(r1[0]), bfhi(r1[0])};
    acc[2 * jp + 1] = (f32x4){bflo(r0[1]), bfhi(r0[1]), bflo(r1[1]), bfhi(r1[1])};
  }
  acc[4] = (f32x4){bflo(xs[0]), bfhi(xs[0]), bflo(xs[1]), bfhi(xs[1])};
#pragma unroll
  for (int j = 0; j < 5; ++j) pin(acc[j]);
  wait_vm<0>();
  lds_add(lds + K2_FL + 8, lane);
  lds_wait(lds + K2_FL + 8, 4);
  TS();
  if (hw == 0) {
    lds_wait(lds + K2_FL + 4, 8);
    __builtin_amdgcn_s_sleep(PDLY);

    cnt_wait(ws, (mt * 8 + ct) >> CSH, 32);
 if (lane == 0) *(volatile LAS unsigned*)(lds + K2_FL + 12) = 1u; }
  else lds_wait(lds + K2_FL + 12, 1);
  TS();
  bf16x8 af[10][2], wf[2][5];
  f32x4 ac1[5];
#pragma unroll
  for (int j = 0; j < 5; ++j) ac1[j] = (f32x4){0.f, 0.f, 0.f, 0.f};
  const int ao = WS_QK + ((hw & 1) * 4 + (lane >> 4)) * PLANE + (mt * 64 + hw * 16 + (lane & 15)) * 16;
#pragma unroll
  for (int t = 0; t < 10; ++t) { af[t][0] = ld16_sc1(rw, ao, t * 8 * PLANE); af[t][1] = ld16_sc1(rw, ao ^ 256, t * 8 * PLANE); }
  SB0();
  ldsp wp = lds + (hw & 1) * 5120 + lane * 16;
#pragma unroll
  for (int j = 0; j < 5; ++j) wf[0][j] = lds_ld16(wp + j * 1024);
#pragma unroll
  for (int t = 0; t < 10; ++t) {
#pragma unroll
    for (int j = 0; j < 5; ++j) {
      if (t + 1 < 10) wf[(t + 1) & 1][j] = lds_ld16(wp + (t + 1) * 10240 + j * 1024);
      ac1[j] = mfma16(wf[t & 1][j], af[t][1], ac1[j]);
    }
#pragma unroll
    for (int j = 0; j < 5; ++j) acc[j] = mfma16(wf[t & 1][j], af[t][0], acc[j]);
    SB0();
  }
  lds_wait(lds + K2_FL + 4, 8);
#pragma unroll
  for (int j = 0; j < 5; ++j) *(LAS f32x4*)(lds + K2_K + (hw * 5 + j) * 1024 + lane * 16) = ac1[j];
  bar();
#pragma unroll
  for (int j = 0; j < 5; ++j) acc[j] += *(const LAS f32x4*)(lds + K2_K + ((hw ^ 1) * 5 + j) * 1024 + lane * 16);
  TS();
  n80_epilogue_h(acc, rw, mt, ct, hw, lane);
  TS();
  if (hw == 1) { TS_END(2); }
}

template <bool EXACT> DI f32x16 attn_rows(ldsp lds, ldsp kp, ldsp ktp, ldsp vp, bf16x8 qm, u32x2 qtr, int dq, int hi) {
  const float NEG = -__builtin_inff();
  auto mx3 = [](float a, float b, float c) { return __builtin_elementwise_maximum(__builtin_elementwise_maximum(a, b), c); };
  auto qk = [&](int j, u32x2 q2, f32x16 cin) {
    return __builtin_amdgcn_mfma_f32_32x32x8bf16_1k(*(const LAS bf16x4*)(ktp + j * 512), __builtin_bit_cast(bf16x4, q2), mfma32(lds_ld16(kp + j * 512), qm, cin), 0, 0, 0);
  };
  f32x16 s0, s8;
#pragma unroll
  for (int r = 0; r < 16; ++r) { const bool le = (r & 3) + 8 * (r >> 2) <= dq; s8[r] = le ? 0.f : NEG; s0[r] = le ? NEG : 0.f; }
  if (!EXACT) { pin(s0); pin(s8); bar(); }
  bf16x8 km[2]; bf16x4 kt[2];
  s8 = qk(8, qtr, s8); s0 = qk(0, qtr, s0);
  km[1] = lds_ld16(kp + 512); kt[1] = *(const LAS bf16x4*)(ktp + 512);
  km[0] = lds_ld16(kp + 1024); kt[0] = *(const LAS bf16x4*)(ktp + 1024);
  f32x16 sa = mfma32(km[1], qm, (f32x16){});
#pragma unroll
  for (int r = 0; r < 16; ++r) s0[r] = __builtin_elementwise_maximum(s0[r], s8[r]);
  float m = mx3(mx3(mx3(s0[0], s0[1], s0[2]), mx3(s0[3], s0[4], s0[5]), mx3(s0[6], s0[7], s0[8])), mx3(mx3(s0[9], s0[10], s0[11]), s0[12], s0[13]), __builtin_elementwise_maximum(s0[14], s0[15]));
  if (EXACT) {
#pragma unroll 1
    for (int j = 1; j < 8; ++j) {
      const f32x16 t = qk(j, qtr, (f32x16){});
#pragma unroll
      for (int r = 0; r < 16; r += 2) m = mx3(m, t[r], t[r + 1]);
    }
  }
  {
    auto rr = __builtin_amdgcn_permlane32_swap(__float_as_uint(m), __float_as_uint(m), false, false);
    m = __builtin_elementwise_maximum(__uint_as_float(rr[0]), __uint_as_float(rr[1]));
  }
  u32x2 q2 = qtr;
  {
    const unsigned u0 = __float_as_uint(m) & 0xffff0000u;
    const float r1 = m - __uint_as_float(u0);
    const unsigned u1 = __float_as_uint(r1) & 0xffff0000u;
    const float r2 = r1 - __uint_as_float(u1);
    if (hi) q2 = (u32x2){0x3f80u | u0, (u1 >> 16) | (__float_as_uint(r2) & 0xffff0000u)};
  }
  const bf16x4 qs = __builtin_bit_cast(bf16x4, q2);
  s0 = __builtin_amdgcn_mfma_f32_32x32x8bf16_1k(__builtin_bit_cast(bf16x4, hi ? (u32x2){0xbf800000u, 0xbf80bf80u} : (u32x2){0u, 0u}), qs, s0, 0, 0, 0);
  sa = __builtin_amdgcn_mfma_f32_32x32x8bf16_1k(kt[1], qs, sa, 0, 0, 0);
  f32x16 sb = __builtin_amdgcn_mfma_f32_32x32x8bf16_1k(kt[0], qs, mfma32(km[0], qm, (f32x16){}), 0, 0, 0);
  SB0();
  auto ex8 = [&](const f32x16& s, int ks) {
    u32x4 p;
#pragma unroll
    for (int e = 0; e < 4; ++e) p[e] = cvtpk(__builtin_amdgcn_exp2f(s[8 * ks + 2 * e]), __builtin_amdgcn_exp2f(s[8 * ks + 2 * e + 1]));
    return p;
  };
  auto vld = [&](int slot, int ks) { return lds_tr2(vp + slot * 512 + ks * 256, vp + slot * 512 + ks * 256 + 128); };
  f32x16 o = {};
  auto pv = [&](bf16x8 v, u32x4 p) { o = mfma32(v, __builtin_bit_cast(bf16x8, p), o); };
  const u32x4 mk0 = *(const LAS u32x4*)(lds + K2_MSK + (dq + 20) * 16), mk1 = *(const LAS u32x4*)(lds + K2_MSK + (dq + 4) * 16);
  u32x4 p0[2], pp[2], pc[2];
  p0[0] = ex8(s0, 0); p0[1] = ex8(s0, 1);
  const u32x4 vrdy = *(volatile LAS u32x4*)(lds + K2_FL + 16);
  km[1] = lds_ld16(kp + 3 * 512); kt[1] = *(const LAS bf16x4*)(ktp + 3 * 512);
  pp[0] = ex8(sa, 0); pp[1] = ex8(sa, 1);
  pin(p0[0]); pin(p0[1]); pin(pp[0]); pin(pp[1]);
  if (!EXACT && __builtin_expect(__builtin_amdgcn_readfirstlane((vrdy[0] & vrdy[1]) & (vrdy[2] & vrdy[3])) == 0, 0)) lds_wait4(lds + K2_FL + 16, 1);
  asm volatile("" ::: "memory");
  bf16x8 vf[2][2], vz[4];
  vf[1][0] = vld(1, 0); vf[1][1] = vld(1, 1);
  SB0();
  sfor<2, 8>([&](auto jc) {
    constexpr int j = decltype(jc)::value;
    f32x16& cur = (j & 1) ? sa : sb;
    f32x16& nxt = (j & 1) ? sb : sa;
    vf[j & 1][0] = vld(j, 0); vf[j & 1][1] = vld(j, 1);
    if constexpr (j + 2 < 8) { km[j & 1] = lds_ld16(kp + (j + 2) * 512); kt[j & 1] = *(const LAS bf16x4*)(ktp + (j + 2) * 512); }
    if constexpr (j == 6) { vz[0] = vld(0, 0); vz[1] = vld(8, 0); }
    if constexpr (j == 7) { vz[2] = vld(0, 1); vz[3] = vld(8, 1); }
    if constexpr (j + 1 < 8) nxt = mfma32(km[(j + 1) & 1], qm, (f32x16){});
    else { const u32x4 a = p0[0] & mk0; pv(vz[0], a); pv(vz[1], p0[0] ^ a); }
    SB0();
    pc[0] = ex8(cur, 0); pv(vf[(j - 1) & 1][0], pp[0]);
    if constexpr (j + 1 < 8) nxt = __builtin_amdgcn_mfma_f32_32x32x8bf16_1k(kt[(j + 1) & 1], qs, nxt, 0, 0, 0);
    SB0();
    pc[1] = ex8(cur, 1); pv(vf[(j - 1) & 1][1], pp[1]); SB0();
    pp[0] = pc[0]; pp[1] = pc[1];
  });
  { const u32x4 a = p0[1] & mk1; pv(vz[2], a); pv(vz[3], p0[1] ^ a); }
  pv(vf[1][0], pp[0]); pv(vf[1][1], pp[1]);
  return o;
}

DI void attn_body(ldsp lds, char* __restrict__ ws, int c, int h, int w, int lane) {
  const int r32 = lane & 31, hi = lane >> 5;
  TS_DECL;
  const int pm = 5 * (h >> 1) + 3 * (h & 1), pt = 5 * (h >> 1) + 2;
  const char* qb = ws + WS_QK + (256 * c + 32 * w) * 16;
  const bf16x8 qm = *(const bf16x8*)(qb + ((pm + hi) * S_ + r32) * 16);
  u32x2 qtr = *(const u32x2*)(qb + (pt * S_ + r32) * 16 + (h & 1) * 8);
  if (hi) qtr = (u32x2){0x3f80u, 0u};
  ldsp kp = lds + K2_K + hi * 8192 + (w * 32 + r32) * 16;
  ldsp ktp = lds + (hi ? K2_V + 3 * K2_VP + w * 512 : K2_K + 16384 + (w * 32 + r32) * 16 + (h & 1) * 8);
  const int vq = (lane & 15) >> 2, vpp = lane & 3, vblk = (lane >> 4) & 1;
  ldsp vp = lds + K2_V + (w * 32 + 4 * hi + vq) * 16 + (vblk == 0 ? (vpp >> 1) * K2_VP + (vpp & 1) * 8 : vpp == 0 ? 2 * K2_VP + (h & 1) * 8 : 3 * K2_VP + 8);
  f32x16 o = attn_rows<false>(lds, kp, ktp, vp, qm, qtr, r32 - 4 * hi, hi);
  float l = __uint_as_float(__builtin_amdgcn_permlane32_swap(__float_as_uint(o[8]), __float_as_uint(o[8]), false, false)[1]);
  if (__builtin_expect(__builtin_amdgcn_ballot_w64(!(l < LAZY_LIM)) != 0, 0)) {
    o = attn_rows<true>(lds, kp, ktp, vp, qm, qtr, r32 - 4 * hi, hi);
    l = __uint_as_float(__builtin_amdgcn_permlane32_swap(__float_as_uint(o[8]), __float_as_uint(o[8]), false, false)[1]);
  }
  TS();
  const float il = __builtin_amdgcn_rcpf(l);
  const int tok = 256 * c + 32 * w + r32;
  const rsrc_t rw = mk_rsrc(ws, WS_END);
  const u32x4 w0 = {cvtpk(o[0] * il, o[1] * il), cvtpk(o[2] * il, o[3] * il), cvtpk(o[4] * il, o[5] * il), cvtpk(o[6] * il, o[7] * il)};
  st16(rw, WS_QK + (pm + hi) * PLANE + tok * 16, w0);
  if (hi == 0) st8(rw, WS_QK + pt * PLANE + tok * 16 + (h & 1) * 8, (u32x2){cvtpk(o[8] * il, o[9] * il), cvtpk(o[10] * il, o[11] * il)});
  TS();
  wait_vm<0>();
  if (lds_add(lds + K2_FL + 4, lane) == 7 && lane < (32 >> CSH)) cnt_add(ws, (c * 32 >> CSH) + lane);
  if (w == 7 || w == 0) { TS_END(w == 7 ? 1 : 5); }
}

extern "C" __global__ __launch_bounds__(768, 3) void k_attn(char* __restrict__ ws, const u16* __restrict__ x, const u16* __restrict__ wg, const u16* __restrict__ wu, const u16* __restrict__ wd) {
  extern __shared__ __attribute__((aligned(16))) char smem[];
  const int lane = threadIdx.x & 63, w = __builtin_amdgcn_readfirstlane(threadIdx.x >> 6);
  const int xcd = blockIdx.x & 7, idx = blockIdx.x >> 3, h = xcd * 4 + (idx >> 3), c = idx & 7;
  if (w < 4) attn_helper((ldsp)smem, ws, c, h, w, lane, x, wg, wu, wd);
  else attn_body((ldsp)smem, ws, c, h, w - 4, lane);
}

constexpr int K4_NS = 4, K4_STG = 20480, K4_NT = 20, K4_RING = 61440, K4_CTL = K4_RING + K4_NS * K4_STG, K4_SD = K4_CTL + 2048, K4_LDS = K4_SD + 8192 + 8192;

extern "C" __global__ __launch_bounds__(512, 2) void k_mlp(char* __restrict__ ws, u16* __restrict__ out) {
  extern __shared__ __attribute__((aligned(16))) char smem[];
  ldsp lds = (ldsp)smem + K4_RING;
  ldsp ctl = (ldsp)smem + K4_CTL;
  const int lane = threadIdx.x & 63, w = __builtin_amdgcn_readfirstlane(threadIdx.x >> 6);
  const unsigned lds00 = (unsigned)(uintptr_t)(ldsp)smem, lds0 = lds00 + K4_RING;
  const int xcd = blockIdx.x & 7, idx = blockIdx.x >> 3, mt = (xcd >> 2) * 8 + (idx >> 2), ft = (xcd & 3) * 4 + (idx & 3);
  const int mt5 = (xcd >> 1) * 8 + (idx >> 2), ct = (xcd & 1) * 4 + (idx & 3);
  const int odd = ct & 1, pb = (ct >> 1) * 5 + (odd ? 3 : 0), ps = (ct >> 1) * 5 + 2;
  if (w >= 4) {
    const int lw = w - 4;
    const char* wd = ws + WS_WD + ct * 245760;
    {
      const bool isa = w < 6;
      const char* src = isa ? ws + WS_H + ((mt & 1) * 40 + lw) * PLANE + (mt >> 1) * 4096 : ws + WS_WGU + ft * K4_NT * 12288 + (w - 6) * 6144;
      const unsigned dst = isa ? lds0 + lw * 4096 : lds0 + 8192 + (w - 6) * 6144;
      auto one = [] {};
      constexpr int XP = 0;
      if (isa) loader_loop<4 + XP, K4_NT, K4_NS, 0>([&](int slot) { dma_run<4>(src, dst + slot * K4_STG, lane * 16); src += 2 * PLANE; one(); },
                                               [&] { dma_run<4>(ws + WS_ST + lw * S_ * 32 + mt * 4096, lds00 + K4_SD + lw * 4096, lane * 16); }, [] {});
      else loader_loop<6 + XP, K4_NT, K4_NS, 0>([&](int slot) { dma_run<6>(src, dst + slot * K4_STG, lane * 16); src += 12288; one(); },
                                           [&] { dma_run<4>(ws + WS_CS + ft * 8192 + (w - 6) * 4096, lds00 + K4_SD + 8192 + (w - 6) * 4096, lane * 16); }, [] {});
    }
    dma_long<15>(wd + lw * 15360, lds00 + lw * 15360, lane * 16);
    {
      set_m0(lds00 + K4_SD + lw * 3072);
      const char* hs = ws + WS_H + ((mt5 >> 1) & 1) * 40 * PLANE + (mt5 >> 2) * 4096 + ((mt5 & 1) * 2 + (lw >> 1)) * 1024;
      const unsigned vo = (lane >> 5) * PLANE + (((lane >> 4) & 1) * 32 + (lw & 1) * 16 + (lane & 15)) * 16 + 2048;
      dma<0>(hs + pb * 2 * PLANE - 2048, vo); dma<1024>(hs + (pb + 1) * 2 * PLANE - 3072, vo); dma<2048>(hs + ps * 2 * PLANE - 4096, vo);
    }
    lds_wait(ctl, 4);
    dma_long<20>(wd + 61440 + lw * 20480, lds0 + lw * 20480, lane * 16);
    wait_vm_bar<0>();
#pragma unroll 1
    for (int g = 0; g < 5; ++g) {
      lds_wait4(ctl + 16, g + 1);
      dma_run<5>(wd + 143360 + g * 20480 + lw * 5120, lds00 + g * 20480 + lw * 5120, lane * 16);
      wait_vm<0>();
      lds_post(ctl + 32 + lw * 4, g + 1, lane);
    }
    return;
  }
  if (threadIdx.x < 12) ((LAS unsigned*)ctl)[threadIdx.x] = 0u;
  const int r32 = lane & 31, hi = lane >> 5, wm = w >> 1, wn = w & 1;
  TS_DECL;
  f32x16 acc[2][3];
#pragma unroll
  for (int i = 0; i < 2; ++i)
#pragma unroll
    for (int j = 0; j < 3; ++j) { acc[i][j] = (f32x16){}; pin(acc[i][j]); }
  bf16x8 af[2][2], wf[2][3];
  const int aoff = wm * 2048 + lane * 16, woff = 8192 + wn * 3072 + lane * 16;
  auto rda = [&](ldsp sb, int s, int i) { af[s][i] = lds_ld16(sb + aoff + s * 4096 + i * 1024); };
  auto rdw = [&](ldsp sb, int s, int j) { wf[s][j] = lds_ld16(sb + woff + s * 6144 + j * 1024); };
  auto mm = [&](int s, int i, int j) { acc[i][j] = mfma32(wf[s][j], af[s][i], acc[i][j]); };
  bar();
  const int tok = mt * 128 + wm * 64 + r32;
  f32x4 sv[2][2];
  float cv[10];
  const LAS float* cp = (const LAS float*)((ldsp)smem + K4_SD + 8192) + w * 48 + (lane < 48 ? lane : 47);
  ldsp sp = (ldsp)smem + K4_SD + hi * 4096 + (wm * 64 + r32) * 32;
  TS();
  rda(lds, 0, 0); rdw(lds, 0, 0); rda(lds, 0, 1); rdw(lds, 0, 1); rdw(lds, 0, 2); SB0();
  constexpr float GC0 = -2.f * 0.7978845608028654f * 1.4426950408889634f, GC1 = GC0 * 0.044715f;
  float ca[2], cb[2], nmu[2], var[2];
  sfor<0, K4_NT>([&](auto tc) {
    constexpr int t = decltype(tc)::value;
    constexpr bool more = t + 1 < K4_NT;
    ldsp sb = lds + (t % K4_NS) * K4_STG;
    ldsp nb = lds + ((t + 1) % K4_NS) * K4_STG;
    mm(0, 0, 0); rda(sb, 1, 0); rdw(sb, 1, 0); SB0();
    mm(0, 1, 0); rda(sb, 1, 1); rdw(sb, 1, 1); SB0();
    mm(0, 0, 1); rdw(sb, 1, 2); SB0();
    mm(0, 1, 1); SB0();
    mm(0, 0, 2); SB0();
    mm(0, 1, 2); SB0();
    if (more) { bar(); SB0(); }
    else { asm volatile("s_waitcnt lgkmcnt(0)" ::: "memory"); lds_add(ctl, lane); SB0(); }
    mm(1, 0, 0); if (more) { rda(nb, 0, 0); rdw(nb, 0, 0); } SB0();
    mm(1, 1, 0); if (more) { rda(nb, 0, 1); rdw(nb, 0, 1); } SB0();
    mm(1, 0, 1); if (more) rdw(nb, 0, 2); SB0();
    if constexpr (t == 3) { cv[8] = cp[8 * 192]; cv[9] = cp[9 * 192]; }
    if constexpr (t >= 4 && t <= 7) { cv[2 * t - 8] = cp[(2 * t - 8) * 192]; cv[2 * t - 7] = cp[(2 * t - 7) * 192]; sv[(t - 4) >> 1][t & 1] = *(const LAS f32x4*)(sp + ((t - 4) >> 1) * 1024 + (t & 1) * 16); }
    if constexpr (t == 8) { if (lane < 48) ((LAS float*)(ctl + 1024))[w * 48 + lane] = (((cv[0] + cv[1]) + (cv[2] + cv[3])) + ((cv[4] + cv[5]) + (cv[6] + cv[7]))) + (cv[8] + cv[9]); }
    if constexpr (t == 9 || t == 10) {
      constexpr int i = t - 9;
      const f32x4 s0 = sv[i][0], s1 = sv[i][1];
      const float mu = xsum32((s0[0] + s0[2]) + (s1[0] + s1[2])) * 0.125f;
      const float d0 = s0[0] - mu, d1 = s0[2] - mu, d2 = s1[0] - mu, d3 = s1[2] - mu;
      var[i] = xsum32((s0[1] + s0[3]) + (s1[1] + s1[3]) + 80.f * ((d0 * d0 + d1 * d1) + (d2 * d2 + d3 * d3))) * (1.f / D_) + LN_EPS;
      const float rstd = __builtin_amdgcn_rsqf(var[i]);
      ca[i] = GC0 * rstd; cb[i] = GC1 * rstd * rstd * rstd; nmu[i] = -mu;
    }
    mm(1, 1, 1);
    mm(1, 0, 2);
    mm(1, 1, 2);
    if constexpr (t == 9 || t == 10) { pin(ca[t - 9]); pin(cb[t - 9]); pin(nmu[t - 9]); pin(var[t - 9]); }
    SB0();
  });
  TS();
  const rsrc_t rw = mk_rsrc(ws, WS_END);
  const LAS float* csb = (const LAS float*)(ctl + 1024) + wn * 96 + 4 * hi;
  f32x4 cq[3][4];
#pragma unroll
  for (int j = 0; j < 3; ++j)
#pragma unroll
    for (int b = 0; b < 4; ++b) cq[j][b] = *(const LAS f32x4*)(csb + j * 32 + 8 * b);
#pragma unroll
  for (int i = 0; i < 2; ++i) {
    const int tk = tok + 32 * i;
    const int ob = WS_ACT + (((tk >> 6) * 48 + ft * 3) * 4 + ((tk >> 4) & 3)) * 1024 + (hi * 16 + (tk & 15)) * 16;
#pragma unroll
    for (int j = 0; j < 3; ++j) {
      unsigned pk[4];
#pragma unroll
      for (int r = 0; r < 8; r += 2) {
        const f32x2 gv = (f32x2){acc[i][j][r], acc[i][j][r + 1]} + nmu[i] * (f32x2){cq[j][r >> 2][r & 3], cq[j][r >> 2][(r & 3) + 1]};
        const f32x2 uv = (f32x2){acc[i][j][8 + r], acc[i][j][9 + r]} + nmu[i] * (f32x2){cq[j][2 + (r >> 2)][r & 3], cq[j][2 + (r >> 2)][(r & 3) + 1]};
        const f32x2 arg = gv * (gv * gv * cb[i] + ca[i]);
        const f32x2 ex = {__builtin_amdgcn_exp2f(arg[0]), __builtin_amdgcn_exp2f(arg[1])};
        const f32x2 den = ex * var[i] + var[i];
        const f32x2 rc = {__builtin_amdgcn_rcpf(den[0]), __builtin_amdgcn_rcpf(den[1])};
        const f32x2 a = gv * uv * rc;
        pk[r >> 1] = cvtpk(a[0], a[1]);
      }
      const int T = wn * 3 + j;
      st16<AUX4>(rw, ob + (T >> 1) * 4096 + (T & 1) * 512, (u32x4){pk[0], pk[1], pk[2], pk[3]});
    }
  }
  TS();
  wait_vm<0>();
  if (lds_add(ctl + 4 + wm * 4, lane) == 1 && lane < (8 >> CSH)) cnt_add(ws, 256 + ((mt * 2 + wm) * 8 >> CSH) + lane);
  TS();
  if (w == 0) { __builtin_amdgcn_s_sleep(PDLY); cnt_wait(ws, 256 + ((mt5 * 8 + ct) >> CSH), 16); if (lane == 0) *(volatile LAS unsigned*)(ctl + 12) = 1u; }
  else lds_wait(ctl + 12, 1);
  TS();
  constexpr int WIN = WIN5;
  bf16x8 a5[WIN][2], w5[2][5];
  const int ao = WS_ACT + mt5 * 196608 + (w & 1) * 4096 + w * 1024 + lane * 16;
#pragma unroll
  for (int t = 0; t < WIN; ++t) { a5[t][0] = ld16_sc1(rw, ao, t * 8192); a5[t][1] = ld16_sc1(rw, ao ^ 1024, t * 8192); }
  SB0();
  bar();
  TS();
  f32x4 ac5[5];
  {
    ldsp rp = (ldsp)smem + K4_SD + w * 3072 + lane * 16;
#pragma unroll
    for (int j = 0; j < 5; ++j) {
      const int e = odd ? (j == 0 ? 5 : j - 1) : j;
      const u32x2 r = *(const LAS u32x2*)(rp + (e >> 1) * 1024 + (e & 1) * 8);
      ac5[j] = (f32x4){bflo(r[0]), bfhi(r[0]), bflo(r[1]), bfhi(r[1])};
    }
  }
  f32x4 ad5[5];
#pragma unroll
  for (int j = 0; j < 5; ++j) ad5[j] = (f32x4){0.f, 0.f, 0.f, 0.f};
  ldsp wp = (ldsp)smem + (w & 1) * 5120 + lane * 16;
#pragma unroll
  for (int j = 0; j < 5; ++j) w5[0][j] = lds_ld16(wp + j * 1024);
  sfor<0, 24>([&](auto tc) {
    constexpr int t = decltype(tc)::value, tn = t + 1;
    if constexpr (tn >= 14 && tn < 24 && (tn & 1) == 0) { lds_wait4(ctl + 32, (tn - 14) / 2 + 1); SB0(); }
    if constexpr (tn == REL0 || tn == REL1 || tn == REL2 || tn == REL3 || tn == REL4) {
      asm volatile("" ::: "memory");
      lds_post(ctl + 16 + w * 4, tn == REL0 ? 1 : tn == REL1 ? 2 : tn == REL2 ? 3 : tn == REL3 ? 4 : 5, lane);
    }
#pragma unroll
    for (int j = 0; j < 5; ++j) {
      if constexpr (tn < 24) w5[tn & 1][j] = lds_ld16(wp + (tn % 14) * 10240 + j * 1024);
      ad5[j] = mfma16(w5[t & 1][j], a5[t % WIN][1], ad5[j]);
    }
#pragma unroll
    for (int j = 0; j < 5; ++j) ac5[j] = mfma16(w5[t & 1][j], a5[t % WIN][0], ac5[j]);
    if constexpr (t + WIN < 24) { a5[t % WIN][0] = ld16_sc1(rw, ao, (t + WIN) * 8192); a5[t % WIN][1] = ld16_sc1(rw, ao ^ 1024, (t + WIN) * 8192); }
    SB0();
  });
  TS();
#pragma unroll
  for (int j = 0; j < 5; ++j) *(LAS f32x4*)((ldsp)smem + 102400 + (w * 5 + j) * 1024 + lane * 16) = ad5[j];
  bar();
#pragma unroll
  for (int j = 0; j < 5; ++j) ac5[j] += *(const LAS f32x4*)((ldsp)smem + 102400 + ((w ^ 1) * 5 + j) * 1024 + lane * 16);
  TS();
  {
    const int g = lane >> 4;
    u32x2 hv[5];
#pragma unroll
    for (int j = 0; j < 5; ++j) hv[j] = (u32x2){cvtpk(ac5[j][0], ac5[j][1]), cvtpk(ac5[j][2], ac5[j][3])};
    const rsrc_t ro = mk_rsrc(out, S_ * D_ * 2);
    const int oo = (mt5 * 64 + w * 16 + (lane & 15)) * 1280 + ct * 160;
#pragma unroll
    for (int jp = 0; jp < 2; ++jp) {
      auto r0 = __builtin_amdgcn_permlane16_swap(hv[2 * jp][0], hv[2 * jp + 1][0], false, false);
      auto r1 = __builtin_amdgcn_permlane16_swap(hv[2 * jp][1], hv[2 * jp + 1][1], false, false);
      st16<AUX5>(ro, oo + jp * 64 + (g & 1) * 32 + (g >> 1) * 16, (u32x4){r0[0], r1[0], r0[1], r1[1]});
    }
    st8<AUX5>(ro, oo + 128 + g * 8, hv[4]);
  }
  TS();
  if (w == 0) { TS_END(3); }
}

extern "C" __global__ void k_text_end() {}

extern "C" void kernel_launch(void* const* d_in, const int* in_sizes, int n_in, void* d_out, int out_size, void* d_ws, size_t ws_size,
                              hipStream_t stream) {
  const u16* x = (const u16*)d_in[0];
  const u16* wqkv = (const u16*)d_in[1];
  const u16* wo = (const u16*)d_in[2];
  const u16* wg = (const u16*)d_in[4];
  const u16* wu = (const u16*)d_in[5];
  const u16* wd = (const u16*)d_in[6];
  char* ws = (char*)d_ws;
  u16* out = (u16*)d_out;
  (void)hipFuncSetAttribute((const void*)k_qkv, hipFuncAttributeMaxDynamicSharedMemorySize, K1_LDS);
  (void)hipFuncSetAttribute((const void*)k_attn, hipFuncAttributeMaxDynamicSharedMemorySize, K2_LDS);
  (void)hipFuncSetAttribute((const void*)k_mlp, hipFuncAttributeMaxDynamicSharedMemorySize, K4_LDS);
  k_qkv<<<256, 768, K1_LDS, stream>>>(x, wqkv, wo, wd, ws);
  k_attn<<<256, 768, K2_LDS, stream>>>(ws, x, wg, wu, wd);
  k_mlp<<<256, 512, K4_LDS, stream>>>(ws, out);
}
```

```cpp
#include <hip/hip_runtime.h>
#include <stdint.h>
#include <type_traits>

#define LAS __attribute__((address_space(3)))
#define DI __device__ __forceinline__
typedef unsigned short u16;
typedef short bf16x8 __attribute__((ext_vector_type(8)));
typedef short bf16x4 __attribute__((ext_vector_type(4)));
typedef __bf16 bf16x2_t __attribute__((ext_vector_type(2)));
typedef float f32x2 __attribute__((ext_vector_type(2)));
typedef float f32x4 __attribute__((ext_vector_type(4)));
typedef float f32x16 __attribute__((ext_vector_type(16)));
typedef unsigned u32x4 __attribute__((ext_vector_type(4)));
typedef unsigned u32x2 __attribute__((ext_vector_type(2)));
typedef LAS char* ldsp;
typedef __amdgpu_buffer_rsrc_t rsrc_t;

constexpr int S_ = 2048, D_ = 640, NH = 32, DF = 1536;
constexpr float LN_EPS = 1e-5f;
constexpr float QSCALE = 0.22360679774997896f * 1.4426950408889634f;
constexpr unsigned ONES2 = 0x3f803f80u;
#define AUX_ST 16

constexpr int PLANE = S_ * 16;
constexpr int WS_QK = 0;
constexpr int WS_ACT = WS_QK + 80 * PLANE;
constexpr int WS_H = WS_ACT + S_ * DF * 2;
constexpr int WS_WO = WS_H + S_ * D_ * 2;
constexpr int WS_ST = WS_WO + D_ * D_ * 2;
constexpr int WS_CS = WS_ST + S_ * 8 * 8;
constexpr int WS_WGU = WS_CS + 16 * 8192;
constexpr int WS_WD = WS_WGU + D_ * DF * 4;
static_assert(WS_WO >= 0 && WS_QK % 4096 == 0, "workspace map");
constexpr int WS_CNT = WS_WD + D_ * DF * 2;
#define CSTR 64
constexpr int WS_END = WS_CNT + 512 * CSTR * 4;

#define TS_DECL
#define TS()
#define TS_END(kid)

template <int I, int N, class F> DI void sfor(F&& f) {
  if constexpr (I < N) { f(std::integral_constant<int, I>{}); sfor<I + 1, N>(f); }
}
#define SB0() __builtin_amdgcn_sched_barrier(0)
DI unsigned cvtpk(float lo, float hi) { f32x2 v = {lo, hi}; return __builtin_bit_cast(unsigned, __builtin_convertvector(v, bf16x2_t)); }
DI float bflo(unsigned u) { return __uint_as_float(u << 16); }
DI float bfhi(unsigned u) { return __uint_as_float(u & 0xffff0000u); }
DI float dot2(unsigned a, unsigned b, float c) {
  return __builtin_amdgcn_fdot2_f32_bf16(__builtin_bit_cast(bf16x2_t, a), __builtin_bit_cast(bf16x2_t, b), c, false);
}
DI f32x4 mfma16(bf16x8 a, bf16x8 b, f32x4 c) { return __builtin_amdgcn_mfma_f32_16x16x32_bf16(a, b, c, 0, 0, 0); }
DI f32x16 mfma32(bf16x8 a, bf16x8 b, f32x16 c) { return __builtin_amdgcn_mfma_f32_32x32x16_bf16(a, b, c, 0, 0, 0); }
DI void set_m0(unsigned v) { asm volatile("s_mov_b32 m0, %0\n\ts_nop 0" ::"s"(v) : "memory"); }
template <int OFF> DI void dma(const void* sbase, unsigned voff) {
  asm volatile("global_load_lds_dwordx4 %0, %1 offset:%2" ::"v"(voff), "s"(sbase), "n"(OFF) : "memory");
}
template <int OFF> DI void dma_nt(const void* sbase, unsigned voff) {
  asm volatile("global_load_lds_dwordx4 %0, %1 offset:%2 nt" ::"v"(voff), "s"(sbase), "n"(OFF) : "memory");
}
template <int OFF> DI void gld16(u32x4& d, const void* sbase, unsigned voff) {
  asm volatile("global_load_dwordx4 %0, %1, %2 offset:%3" : "=v"(d) : "v"(voff), "s"(sbase), "n"(OFF) : "memory");
}
template <int N> DI void wait_vm() { asm volatile("s_waitcnt vmcnt(%0)" ::"n"(N) : "memory"); }
template <int N> DI void wait_vm_bar() { asm volatile("s_waitcnt vmcnt(%0) lgkmcnt(0)\n\ts_barrier" ::"n"(N) : "memory"); }
DI void bar() { asm volatile("s_waitcnt lgkmcnt(0)\n\ts_barrier" ::: "memory"); }
template <class T> DI void pin(T& v) { asm volatile("" : "+v"(v)); }
DI bf16x8 lds_ld16(ldsp p) { return *(const LAS bf16x8*)p; }
DI bf16x4 lds_tr(ldsp p) { return __builtin_amdgcn_ds_read_tr16_b64_v4i16((LAS bf16x4*)p); }
DI bf16x8 lds_tr2(ldsp p0, ldsp p1) {
  bf16x4 a = lds_tr(p0), b = lds_tr(p1);
  return __builtin_shufflevector(a, b, 0, 1, 2, 3, 4, 5, 6, 7);
}
DI float xsum32(float v) {
  auto r = __builtin_amdgcn_permlane32_swap(__float_as_uint(v), __float_as_uint(v), false, false);
  return __uint_as_float(r[0]) + __uint_as_float(r[1]);
}
DI float xsum16_32(float v) {
  auto r = __builtin_amdgcn_permlane16_swap(__float_as_uint(v), __float_as_uint(v), false, false);
  return xsum32(__uint_as_float(r[0]) + __uint_as_float(r[1]));
}
DI rsrc_t mk_rsrc(void* p, int bytes) { return __builtin_amdgcn_make_buffer_rsrc(p, 0, bytes, 0x00020000); }
template <int AUX = 16> DI void st16(rsrc_t r, int off, u32x4 v) { __builtin_amdgcn_raw_buffer_store_b128(v, r, off, 0, AUX); }
template <int AUX = 16> DI void st8(rsrc_t r, int off, u32x2 v) { __builtin_amdgcn_raw_buffer_store_b64(v, r, off, 0, AUX); }
#define CSH 2
#define PDLY 0
#define PSLP 2
#define REL0 2
#define REL1 6
#define REL2 10
#define REL3 13
#define REL4 16
#define SLP4 1
#define LDAUX 16
#define WIN5 5
#define AUX1 16
#define AUX3 0
#define AUX5 0
#define AUX4 16

template <int P, int NT, int NS, int X, class F, class G0, class G> DI void loader_loop(F&& issue, G0&& early, G&& extra) {
  static_assert(NS >= 4, "three stages in flight + one being read");
  issue(0); issue(1);
  wait_vm_bar<P>();
  early();
  issue(2); issue(3);
  int slot = 4 % NS;
#pragma unroll 1
  for (int t = 1; t < NT - 3; ++t) {
    wait_vm_bar<2 * P>();
    issue(slot);
    slot = slot == NS - 1 ? 0 : slot + 1;
  }
  extra();
  wait_vm_bar<2 * P + X>(); wait_vm_bar<P + X>(); wait_vm_bar<X>();
}
DI void touch(const char* p, int lane, int nlines, unsigned lds_scratch) {
  set_m0(lds_scratch);
  asm volatile("global_load_lds_dword %0, %1" ::"v"((unsigned)((lane < nlines ? lane : nlines - 1) * 128)), "s"(p) : "memory");
}
#define CODE_ADDR(dst, sym) asm volatile("s_getpc_b64 s[40:41]\n\ts_add_u32 s40, s40, " #sym "@rel32@lo+4\n\ts_addc_u32 s41, s41, " #sym "@rel32@hi+12\n\ts_mov_b64 %0, s[40:41]" : "=s"(dst) :: "s40", "s41", "scc")
template <int P> DI void dma_run(const char* src, unsigned dst, unsigned lane16) {
  static_assert(P <= 8, "offset range");
  set_m0(dst + 4096);
  sfor<0, P>([&](auto ic) { dma<decltype(ic)::value * 1024 - 4096>(src + 4096, lane16); });
}
template <int P, int NT, int NS, int SSTR, int LSTR, int X, class G0, class G> DI void linear_loader(const char* src, unsigned dst, unsigned lane16, G0&& early, G&& extra) {
  loader_loop<P, NT, NS, X>([&](int slot) { dma_run<P>(src, dst + slot * LSTR, lane16); src += SSTR; }, early, extra);
}

template <int N> DI void dma_long(const char* src, unsigned dst, unsigned lane16) {
  sfor<0, N / 5>([&](auto ic) { constexpr int i = decltype(ic)::value; dma_run<5>(src + i * 5120, dst + i * 5120, lane16); });
}
DI unsigned lds_add(ldsp p, int lane) {
  unsigned old = 0;
  if (lane == 0) old = __hip_atomic_fetch_add((LAS unsigned*)p, 1u, __ATOMIC_RELAXED, __HIP_MEMORY_SCOPE_WORKGROUP);
  return __builtin_amdgcn_readfirstlane(old);
}
DI void lds_wait(ldsp p, unsigned want) {
  for (int sp = 0; sp < (1 << 22); ++sp) {
    if ((unsigned)__builtin_amdgcn_readfirstlane(*(volatile LAS unsigned*)p) >= want) break;
    __builtin_amdgcn_s_sleep(1);
  }
  asm volatile("" ::: "memory");
}
DI void lds_post(ldsp p, unsigned v, int lane) { if (lane == 0) *(volatile LAS unsigned*)p = v; asm volatile("" ::: "memory"); }
DI void lds_wait4(ldsp p, unsigned want) {
  for (int sp = 0; sp < (1 << 22); ++sp) {
    const u32x4 v = *(volatile LAS u32x4*)p;
    if ((unsigned)__builtin_amdgcn_readfirstlane(min(min(v[0], v[1]), min(v[2], v[3]))) >= want) break;
    __builtin_amdgcn_s_sleep(SLP4);
  }
  asm volatile("" ::: "memory");
}
DI void cnt_add(char* ws, int idx) { __hip_atomic_fetch_add((unsigned*)(ws + WS_CNT) + idx * CSTR, 1u, __ATOMIC_RELAXED, __HIP_MEMORY_SCOPE_AGENT); }
DI void cnt_wait(char* ws, int idx, unsigned want) {
  unsigned* p = (unsigned*)(ws + WS_CNT) + idx * CSTR;
  for (int sp = 0; sp < (1 << 18); ++sp) {
    if ((unsigned)__builtin_amdgcn_readfirstlane(__hip_atomic_load(p, __ATOMIC_RELAXED, __HIP_MEMORY_SCOPE_AGENT)) >= want) break;
    __builtin_amdgcn_s_sleep(PSLP);
  }
  __builtin_amdgcn_fence(__ATOMIC_ACQUIRE, "wavefront");
}
DI bf16x8 ld16_sc1(rsrc_t r, int voff, int soff) { return __builtin_bit_cast(bf16x8, __builtin_amdgcn_raw_buffer_load_b128(r, voff, soff, LDAUX)); }

template <int KIND> DI void pack_issue(const u16* wsrc, const u16* wsrc2, int ks, int cg, unsigned ldsdst, int lane) {
  constexpr int PITCH = KIND == 1 ? DF * 2 : D_ * 2;
  set_m0(ldsdst);
  sfor<0, 4>([&](auto ic) {
    constexpr int g = decltype(ic)::value;
    const int t = (lane >> 5) & 1, q = (lane >> 3) & 3;
    int row;
    if (KIND == 0) { const int P = 4 * ks + g, hp = P / 5, r = P - 5 * hp; row = 40 * hp + q + (r < 2 ? 8 * t + 4 * r : r == 2 ? 16 + 4 * t : 12 + 8 * t + 4 * r); }
    else if (KIND == 1) row = 32 * (ks >> 1) + 16 * t + 4 * (2 * (ks & 1) + (g & 1)) + q;
    else row = 32 * ks + 16 * (g >> 1) + 8 * t + 4 * (g & 1) + q;
    dma_nt<g * 1024>((const char*)(KIND == 1 && g >= 2 ? wsrc2 : wsrc) + cg * 128 - 4096, (unsigned)(row * PITCH + (lane & 7) * 16 + 4096 - g * 1024));
  });
}
template <int KIND> DI void pack_finish(ldsp u, rsrc_t rw, int ks, int cg, int lane, LAS float* cs) {
  const int q = (lane & 15) >> 2, p = lane & 3;
  ldsp a = KIND == 1 ? u + ((lane >> 4) & 1) * 2048 + (8 * (lane >> 5) + q) * 128 + p * 8 : u + (8 * (lane >> 4) + q) * 128 + p * 8;
#pragma unroll
  for (int i = 0; i < 4; ++i) {
    const int nt = 4 * cg + i;
    const u32x4 f = __builtin_bit_cast(u32x4, lds_tr2(a + i * 32, a + i * 32 + 512));
    if (KIND == 1) {
      const int ft = nt / 6, j = nt - ft * 6;
      st16(rw, WS_WGU + ((ft * 40 + ks) * 6 + j) * 1024 + lane * 16, f);
      const unsigned sv = (lane & 15) == ((lane >> 4) & 1) ? ONES2 : 0u;
      const f32x4 c = mfma16(__builtin_bit_cast(bf16x8, (u32x4){sv, sv, sv, sv}), __builtin_bit_cast(bf16x8, f), (f32x4){0.f, 0.f, 0.f, 0.f});
      if (lane < 16) { cs[i * 32 + lane] = c[0]; cs[i * 32 + 16 + lane] = c[1]; }
    } else {
      const int ct = nt / 5, j = nt - ct * 5;
      st16(rw, (KIND == 0 ? WS_WO : WS_WD) + ((ct * (KIND == 0 ? 20 : 48) + ks) * 5 + j) * 1024 + lane * 16, f);
    }
  }
}

#define STATMODE 1
constexpr int NWD_A = 352;
#define WD0 8
#define WD1 24
constexpr int K1_NS = 4, K1_STG = 32768, K1_NT = 10, K1_CTL = K1_NS * K1_STG, K1_LDS = K1_CTL + 4096;

DI void qkv_compute(ldsp lds, char* __restrict__ ws, int mt, int nt, int w, int lane) {
  const int wm = w >> 1, wn = w & 1, r32 = lane & 31, hi = lane >> 5, q = (lane & 15) >> 2, p = lane & 3, blk = (lane >> 4) & 1;
  TS_DECL;
  ldsp ctl = lds + K1_CTL;
  int aoff[4], woff[2];
#pragma unroll
  for (int s = 0; s < 4; ++s) aoff[s] = (wm * 64 + r32) * 128 + (((2 * s + hi) ^ ((r32 >> 1) & 7)) * 16);
#pragma unroll
  for (int j = 0; j < 2; ++j) woff[j] = 16384 + (8 * hi + q) * 256 + (((wn * 2 + (j ^ wm)) ^ q) * 64) + blk * 32 + p * 8;
  f32x16 acc[2][2];
#pragma unroll
  for (int j = 0; j < 2; ++j)
#pragma unroll
    for (int i = 0; i < 2; ++i) { acc[j][i] = (f32x16){}; pin(acc[j][i]); }
  f32x4 cs = {};
  const unsigned sv = (lane & 15) == blk ? ONES2 : 0u;
  const bf16x8 sel = __builtin_bit_cast(bf16x8, (u32x4){sv, sv, sv, sv});
  bf16x8 xf[2][2], wf[2][2];
  auto rdx = [&](ldsp sb, int s, int i) { xf[s & 1][i] = lds_ld16(sb + aoff[s] + i * 4096); };
  auto rdw = [&](ldsp sb, int s, int j) { wf[s & 1][j] = lds_tr2(sb + woff[j] + s * 4096, sb + woff[j] + s * 4096 + 1024); };
  auto mm = [&](int s, int j, int i) { acc[j][i] = mfma32(wf[s & 1][j], xf[s & 1][i], acc[j][i]); };
  auto step = [&](ldsp nb, int s, int sn, bool rd) {
    cs = mfma16(sel, wf[s & 1][0], cs);
    mm(s, 0, 0); if (rd) { rdx(nb, sn, 0); rdw(nb, sn, 0); } SB0();
    mm(s, 0, 1); if (rd) { rdx(nb, sn, 1); rdw(nb, sn, 1); } SB0();
    mm(s, 1, 0); SB0();
    mm(s, 1, 1); SB0();
  };
  bar();
  TS();
  ldsp sb = lds;
  rdx(sb, 0, 0); rdw(sb, 0, 0); rdx(sb, 0, 1); rdw(sb, 0, 1); SB0();
  int slot = 0;
#pragma unroll 1
  for (int t = 0; t < K1_NT - 1; ++t) {
    step(sb, 0, 1, true); step(sb, 1, 2, true); step(sb, 2, 3, true);
    slot = slot == K1_NS - 1 ? 0 : slot + 1;
    sb = lds + slot * K1_STG;
    bar(); SB0();
    step(sb, 3, 0, true);
  }
  step(sb, 0, 1, true); step(sb, 1, 2, true); step(sb, 2, 3, true);
  const LAS f32x2* rst = (const LAS f32x2*)(ctl + 1024);
  LAS float* cst = (LAS float*)(ctl + 2048);
  cs = mfma16(sel, wf[1][0], cs);
  mm(3, 0, 0); mm(3, 0, 1); SB0();
  if (lane < 16) { cst[wn * 64 + wm * 32 + lane] = cs[0]; cst[wn * 64 + wm * 32 + 16 + lane] = cs[1]; }
  mm(3, 1, 0); mm(3, 1, 1); SB0();
  TS();
  bar();
  TS();
  const rsrc_t rw = mk_rsrc(ws, WS_END);
  const float sc = nt < 5 ? QSCALE : 1.f;
  f32x4 c4[2][4];
#pragma unroll
  for (int j = 0; j < 2; ++j)
#pragma unroll
    for (int b = 0; b < 4; ++b) c4[j][b] = *(const LAS f32x4*)(cst + wn * 64 + (j ^ wm) * 32 + 8 * b + 4 * hi);
#pragma unroll
  for (int i = 0; i < 2; ++i) {
    const int ml = wm * 64 + i * 32 + r32;
    const f32x2 st = rst[ml];
    const float a = st.y * sc, nm = -st.x * a;
    const int ob = WS_QK + ((nt * 16 + wn * 8 + hi) * S_ + mt * 128 + ml) * 16;
#pragma unroll
    for (int j = 0; j < 2; ++j)
#pragma unroll
      for (int bp = 0; bp < 2; ++bp) {
        unsigned pk[2][2];
#pragma unroll
        for (int e = 0; e < 2; ++e) {
          const int b = 2 * bp + e;
          const f32x2 v0 = (f32x2){acc[j][i][4 * b], acc[j][i][4 * b + 1]} * a + nm * (f32x2){c4[j][b][0], c4[j][b][1]};
          const f32x2 v1 = (f32x2){acc[j][i][4 * b + 2], acc[j][i][4 * b + 3]} * a + nm * (f32x2){c4[j][b][2], c4[j][b][3]};
          pk[e][0] = cvtpk(v0[0], v0[1]); pk[e][1] = cvtpk(v1[0], v1[1]);
        }
        auto r0 = __builtin_amdgcn_permlane32_swap(pk[0][0], pk[1][0], false, false);
        auto r1 = __builtin_amdgcn_permlane32_swap(pk[0][1], pk[1][1], false, false);
        st16<AUX1>(rw, ob + ((j ^ wm) * 4 + bp * 2) * S_ * 16, (u32x4){r0[0], r1[0], r0[1], r1[1]});
      }
  }
  TS();
  if (w == 0) { TS_END(0); }
}

DI void qkv_stats(ldsp lds, int sw, int lane) {
  const int fr = lane & 15, g = lane >> 4;
  const int off = (sw * 32 + fr) * 128, x = fr >> 1;
  const bf16x8 ones = __builtin_bit_cast(bf16x8, (u32x4){ONES2, ONES2, ONES2, ONES2});
  f32x4 sm[2] = {}, sq[2] = {};
  int slot = 0;
  __builtin_amdgcn_s_setprio(2);
#pragma unroll 1
  for (int t = 0; t < K1_NT; ++t) {
    bar();
    ldsp sb = lds + slot * K1_STG + off;
    slot = slot == K1_NS - 1 ? 0 : slot + 1;
    bf16x8 v[2][2];
#pragma unroll
    for (int ks = 0; ks < 2; ++ks)
#pragma unroll
      for (int i = 0; i < 2; ++i) v[ks][i] = lds_ld16(sb + i * 2048 + (((4 * ks + g) ^ x) * 16));
#pragma unroll
    for (int ks = 0; ks < 2; ++ks)
#pragma unroll
      for (int i = 0; i < 2; ++i) { sq[i] = mfma16(v[ks][i], v[ks][i], sq[i]); sm[i] = mfma16(ones, v[ks][i], sm[i]); }
  }
#pragma unroll
  for (int i = 0; i < 2; ++i) {
    const int p = lane & 3;
    const float q = p == 0 ? sq[i][0] : p == 1 ? sq[i][1] : p == 2 ? sq[i][2] : sq[i][3];
    const float mu = sm[i][0] * (1.f / D_), var = fmaxf(q * (1.f / D_) - mu * mu, 0.f);
    if ((fr >> 2) == g) ((LAS f32x2*)(lds + K1_CTL + 1024))[sw * 32 + i * 16 + fr] = (f32x2){mu, __builtin_amdgcn_rsqf(var + LN_EPS)};
  }
  asm volatile("s_waitcnt lgkmcnt(0)" ::: "memory");
}

DI void qkv_loader(ldsp lds, const u16* __restrict__ x, const u16* __restrict__ wqkv, int mt, int nt, int lw, int lane) {
  const bool isx = lw < 2;
  const int half = lw & 1;
  unsigned voff[8];
#pragma unroll
  for (int i = 0; i < 8; ++i) {
    const int r = (half * 8 + i) * 8 + (lane >> 3), c = (lane & 7) ^ ((r >> 1) & 7);
    const int k = (half * 8 + i) * 4 + (lane >> 4), cp = lane & 15;
    voff[i] = (isx ? r * 1280 + c * 16 : k * 3840 + (cp ^ ((k & 3) << 2)) * 16) + 8192 - i * 1024;
  }
  const char* src = (isx ? (const char*)x + (size_t)mt * 128 * 1280 : (const char*)wqkv + nt * 256) - 4096;
  const int sstr = isx ? 128 : 64 * 3840;
  const unsigned dst = (unsigned)(uintptr_t)lds + lw * 8192 + 4096;
  loader_loop<8, K1_NT, K1_NS, 0>([&](int slot) {
    set_m0(dst + slot * K1_STG);
    sfor<0, 8>([&](auto ic) { constexpr int i = decltype(ic)::value; dma<i * 1024 - 4096>(src, voff[i]); });
    src += sstr;
  }, [] {}, [] {});
}

extern "C" __global__ __launch_bounds__(768, 3) void k_qkv(const u16* __restrict__ x, const u16* __restrict__ wqkv, const u16* __restrict__ wo, const u16* __restrict__ wd, char* __restrict__ ws) {
  extern __shared__ __attribute__((aligned(16))) char smem[];
  ldsp lds = (ldsp)smem;
  const int lane = threadIdx.x & 63, w = __builtin_amdgcn_readfirstlane(threadIdx.x >> 6);
  const int xcd = blockIdx.x & 7, idx = blockIdx.x >> 3;
  const int nt = (xcd & 1) * 8 + (idx >> 2), mt = (xcd >> 1) * 4 + (idx & 3);
  if (w < 4 && nt < 15) { qkv_loader(lds, x, wqkv, mt, nt, w, lane); return; }
  if (w == 11 && lane < 2) ((unsigned*)(ws + WS_CNT))[(blockIdx.x * 2 + lane) * CSTR] = 0u;
  if (w == 10) {
    const char *c0, *c1;
    CODE_ADDR(c0, k_qkv); CODE_ADDR(c1, k_text_end);
    const unsigned lim = c1 > c0 ? (unsigned)(c1 - c0) : 0u, off = (idx * 16 + (lane & 15)) * 128;
    set_m0((unsigned)(uintptr_t)lds + K1_CTL + 3072 + 512);
    asm volatile("global_load_lds_dword %0, %1" ::"v"(off < lim ? off : lim), "s"(c0) : "memory");
  }
  if (w == 8 || w == 9) {
    const int id = blockIdx.x * 64 + lane, u = w - 8;
    const bool isx = blockIdx.x < 128;
    const int i2 = id - 8192 < 7680 ? id - 8192 : 7679;
    const unsigned off = isx ? (id >> 2) * 1280 + 256 + u * 512 + (id & 3) * 128 : (128 + u * 256) * 3840 + i2 * 128;
    set_m0((unsigned)(uintptr_t)lds + K1_CTL + 3072 + u * 256);
    const char* src = isx ? (const char*)x : (const char*)wqkv;
    asm volatile("" : "+s"(src));
    if (u == 0) __builtin_amdgcn_s_sleep(WD0); else __builtin_amdgcn_s_sleep(WD1);
    asm volatile("global_load_lds_dword %0, %1" ::"v"(off), "s"(src) : "memory");
  }
  if (nt == 15) {
    const rsrc_t rw = mk_rsrc(ws, WS_END);
    constexpr int NU = (200 + NWD_A + 191) / 192;
    auto iss = [&](int r) {
      const int u = min(mt * 12 + w + r * 192, 200 + NWD_A - 1), v = u - 200;
      if (u < 200) pack_issue<0>(wo, wo, u / 10, u % 10, (unsigned)(uintptr_t)lds + (w * 2 + (r & 1)) * 4096, lane);
      else pack_issue<2>(wd, wd, v / 10, v % 10, (unsigned)(uintptr_t)lds + (w * 2 + (r & 1)) * 4096, lane);
    };
    iss(0); iss(1);
#pragma unroll 1
    for (int r = 0; r < NU; ++r) {
      if (r == 0) wait_vm<4>(); else if (r + 1 < NU) wait_vm<8>(); else wait_vm<4>();
      const int u = min(mt * 12 + w + r * 192, 200 + NWD_A - 1), v = u - 200;
      if (u < 200) pack_finish<0>(lds + (w * 2 + (r & 1)) * 4096, rw, u / 10, u % 10, lane, nullptr);
      else pack_finish<2>(lds + (w * 2 + (r & 1)) * 4096, rw, v / 10, v % 10, lane, nullptr);
      asm volatile("s_waitcnt lgkmcnt(0)" ::: "memory");
      if (r + 2 < NU) iss(r + 2);
    }
    return;
  }
  if (w >= 8) qkv_stats(lds, w - 8, lane);
  else qkv_compute(lds, ws, mt, nt, w - 4, lane);
}

#define LAZY_LIM 1.0995116e12f
#define VLEAD 3
constexpr int K2_K = 102400, K2_V = K2_K + 24576, K2_VP = 8192 + 64, K2_CS = K2_V + 4 * K2_VP, K2_MSK = K2_CS + 2560, K2_FL = K2_MSK + 832, K2_LDS = K2_FL + 64 + 256;
static_assert(K2_LDS <= 163840 && K2_FL % 16 == 0, "LDS map");

DI void n80_epilogue_h(f32x4 (&acc)[5], rsrc_t rw, int mt, int ct, int w, int lane) {
  const int fr = lane & 15, g = lane >> 4, tok = mt * 64 + w * 16 + fr;
  const int odd = ct & 1, pb = (ct >> 1) * 5 + (odd ? 3 : 0), ps = (ct >> 1) * 5 + 2;
  const int hb = WS_H + (((mt >> 1) & 1) * 40 + (g >> 1)) * PLANE + (mt >> 2) * 4096 + ((mt & 1) * 2 + (w >> 1)) * 1024 + ((g & 1) * 32 + (w & 1) * 16 + fr) * 16;
  u32x2 hv[5];
  float s = 0.f;
#pragma unroll
  for (int j = 0; j < 5; ++j) hv[j] = (u32x2){cvtpk(acc[j][0], acc[j][1]), cvtpk(acc[j][2], acc[j][3])};
  const u32x2 a0 = odd ? hv[1] : hv[0], a1 = odd ? hv[2] : hv[1], b0 = odd ? hv[3] : hv[2], b1 = odd ? hv[4] : hv[3], s1 = odd ? hv[0] : hv[4];
  st16<AUX3>(rw, hb + pb * 2 * PLANE, (u32x4){a0[0], a0[1], a1[0], a1[1]});
  st16<AUX3>(rw, hb + (pb + 1) * 2 * PLANE, (u32x4){b0[0], b0[1], b1[0], b1[1]});
  st8<AUX3>(rw, hb + ps * 2 * PLANE + odd * 8, s1);
  f32x4 v[5];
#pragma unroll
  for (int j = 0; j < 5; ++j) { v[j] = (f32x4){bflo(hv[j][0]), bfhi(hv[j][0]), bflo(hv[j][1]), bfhi(hv[j][1])}; s += (v[j][0] + v[j][1]) + (v[j][2] + v[j][3]); }
  const float mean = xsum16_32(s) * (1.f / 80.f);
  float m2 = 0.f;
#pragma unroll
  for (int j = 0; j < 5; ++j) { const f32x4 d = v[j] - mean; m2 += (d[0] * d[0] + d[1] * d[1]) + (d[2] * d[2] + d[3] * d[3]); }
  m2 = xsum16_32(m2);
  if (g == 0) st8<AUX3>(rw, WS_ST + (((ct >> 2) * S_ + tok) * 4 + (ct & 3)) * 8, (u32x2){__float_as_uint(mean), __float_as_uint(m2)});
}

DI void attn_helper(ldsp lds, char* __restrict__ ws, int c, int h, int hw, int lane, const u16* __restrict__ x, const u16* __restrict__ wg, const u16* __restrict__ wu, const u16* __restrict__ wd) {
  const unsigned lds0 = (unsigned)(uintptr_t)lds;
  const int pm = 5 * (h >> 1) + 3 * (h & 1), pt = 5 * (h >> 1) + 2;
  const bool EDGE = c == 0;
  const int tok0 = 256 * c - 256 + 128 * hw + ((EDGE && hw < 2) ? 256 : 0);
  const char* kb = ws + WS_QK + (80 * S_ + tok0) * 16;
  {
    set_m0(lds0 + K2_K + hw * 2048); dma<0>(kb + pm * S_ * 16, lane * 16); dma<1024>(kb + pm * S_ * 16, lane * 16);
    set_m0(lds0 + K2_K + 8192 + hw * 2048); dma<0>(kb + (pm + 1) * S_ * 16, lane * 16); dma<1024>(kb + (pm + 1) * S_ * 16, lane * 16);
    set_m0(lds0 + K2_K + 16384 + hw * 2048); dma<0>(kb + pt * S_ * 16, lane * 16); dma<1024>(kb + pt * S_ * 16, lane * 16);
  }
  TS_DECL;
  const int b = blockIdx.x, xcd = b & 7, idx = b >> 3;
  if (hw == 0 && lane < 8) *(LAS unsigned*)(lds + K2_FL + lane * 4) = 0u;
  {
    const unsigned tm = (EDGE && hw < 2 ? 0xff80u : 0u) | 0xbf800000u;
    *(LAS u32x4*)(lds + K2_V + 3 * K2_VP + hw * 2048 + lane * 16) = (u32x4){tm, 0xbf80bf80u, ONES2, ONES2};
    *(LAS u32x4*)(lds + K2_V + 3 * K2_VP + hw * 2048 + 1024 + lane * 16) = (u32x4){tm, 0xbf80bf80u, ONES2, ONES2};
  }
  if (hw == 1 && lane < 52) {
    const int d = lane - 20;
    u32x4 mk;
#pragma unroll
    for (int e = 0; e < 4; ++e) { const int k0 = ((2 * e) & 3) + 8 * (e >> 1); mk[e] = (k0 > d ? 0xffffu : 0u) | (k0 + 1 > d ? 0xffff0000u : 0u); }
    *(LAS u32x4*)(lds + K2_MSK + lane * 16) = mk;
  }
  wait_vm<VLEAD>();
  {
    const char* vb = kb + 80 * S_ * 16;
    set_m0(lds0 + K2_V + hw * 2048); dma<0>(vb + pm * S_ * 16, lane * 16); dma<1024>(vb + pm * S_ * 16, lane * 16);
    set_m0(lds0 + K2_V + K2_VP + hw * 2048); dma<0>(vb + (pm + 1) * S_ * 16, lane * 16); dma<1024>(vb + (pm + 1) * S_ * 16, lane * 16);
    set_m0(lds0 + K2_V + 2 * K2_VP + hw * 2048); dma<0>(vb + pt * S_ * 16, lane * 16); dma<1024>(vb + pt * S_ * 16, lane * 16);
  }
  const rsrc_t rw = mk_rsrc(ws, WS_END);
  const int mt = xcd * 4 + (idx >> 3), ct = idx & 7;
  const int q0 = hw * 25600;
  const int bb = b, pcg = bb % 24, pks = (bb / 24) * 4 + hw, v0 = NWD_A + (b - 240) * 8 + hw * 2;
  wait_vm_bar<6>();
  if (b < 240) { pack_issue<1>(wg, wu, pks, pcg, lds0 + q0, lane); wait_vm<4>(); }
  else { pack_issue<2>(wd, wd, v0 / 10, v0 % 10, lds0 + q0, lane); pack_issue<2>(wd, wd, v0 / 10, v0 % 10 + 1, lds0 + q0 + 4096, lane); wait_vm<8>(); }
  if (lane == 0) *(volatile LAS unsigned*)(lds + K2_FL + 16 + hw * 4) = 1u;
  TS();
  u32x4 xr[2]; u32x2 xs;
  {
    const char* xp = (const char*)x + (size_t)(mt * 64 + hw * 16) * 1280 + ct * 160;
    const int g = lane >> 4;
    const unsigned o16 = (lane & 15) * 1280 + (g & 1) * 32 + (g >> 1) * 16, o8 = (lane & 15) * 1280 + g * 8;
    asm volatile("global_load_dwordx4 %0, %1, %2" : "=v"(xr[0]) : "v"(o16), "s"(xp) : "memory");
    asm volatile("global_load_dwordx4 %0, %1, %2 offset:64" : "=v"(xr[1]) : "v"(o16), "s"(xp) : "memory");
    asm volatile("global_load_dwordx2 %0, %1, %2 offset:128" : "=v"(xs) : "v"(o8), "s"(xp) : "memory");
  }
  dma_long<15>(ws + WS_WO + ct * 102400 + q0 + 10240, lds0 + q0 + 10240, lane * 16);
  wait_vm<15>();
  pin(xr[0]); pin(xr[1]); pin(xs);
  TS();
  if (b >= 240) {
    pack_finish<2>(lds + q0, rw, v0 / 10, v0 % 10, lane, nullptr); pack_finish<2>(lds + q0 + 4096, rw, v0 / 10, v0 % 10 + 1, lane, nullptr);
    asm volatile("s_waitcnt lgkmcnt(0)" ::: "memory");
  } else {
    LAS float* ct4 = (LAS float*)(lds + K2_CS);
    pack_finish<1>(lds + q0, rw, pks, pcg, lane, ct4 + hw * 128);
    asm volatile("s_waitcnt lgkmcnt(0)" ::: "memory");
    if (lds_add(lds + K2_FL, lane) == 3) {
      asm volatile("" ::: "memory");
      const LAS f32x2* c2 = (const LAS f32x2*)ct4 + lane;
      const f32x2 a = (c2[0] + c2[64]) + (c2[128] + c2[192]);
      const int e = pcg * 128 + lane * 2, ft = e / 192;
      st8(rw, WS_CS + ft * 8192 + ((bb / 24) * 192 + e - ft * 192) * 4, __builtin_bit_cast(u32x2, a));
    }
  }
  TS();
  dma_long<10>(ws + WS_WO + ct * 102400 + q0, lds0 + q0, lane * 16);
  if (hw == 3) {
    const char *c0, *c1;
    CODE_ADDR(c0, k_mlp); CODE_ADDR(c1, k_text_end);
    const unsigned lim = c1 > c0 ? (unsigned)(c1 - c0) : 0u, off = (idx * 8 + (lane & 7)) * 128;
    set_m0(lds0 + K2_FL + 64);
    asm volatile("global_load_lds_dword %0, %1" ::"v"(off < lim ? off : lim), "s"(c0) : "memory");
  }
  f32x4 acc[5];
#pragma unroll
  for (int jp = 0; jp < 2; ++jp) {
    auto r0 = __builtin_amdgcn_permlane16_swap(xr[jp][0], xr[jp][2], false, false);
    auto r1 = __builtin_amdgcn_permlane16_swap(xr[jp][1], xr[jp][3], false, false);
    acc[2 * jp] = (f32x4){bflo(r0[0]), bfhi(r0[0]), bflo(r1[0]), bfhi(r1[0])};
    acc[2 * jp + 1] = (f32x4){bflo(r0[1]), bfhi(r0[1]), bflo(r1[1]), bfhi(r1[1])};
  }
  acc[4] = (f32x4){bflo(xs[0]), bfhi(xs[0]), bflo(xs[1]), bfhi(xs[1])};
#pragma unroll
  for (int j = 0; j < 5; ++j) pin(acc[j]);
  wait_vm<0>();
  lds_add(lds + K2_FL + 8, lane);
  lds_wait(lds + K2_FL + 8, 4);
  TS();
  if (hw == 0) {
    lds_wait(lds + K2_FL + 4, 8);
    __builtin_amdgcn_s_sleep(PDLY);

    cnt_wait(ws, (mt * 8 + ct) >> CSH, 32);
 if (lane == 0) *(volatile LAS unsigned*)(lds + K2_FL + 12) = 1u; }
  else {
    lds_wait(lds + K2_FL + 12, 1);
  }
  TS();
  bf16x8 af[20], wf[3][5];
  const int ao = WS_QK + (lane >> 4) * PLANE + (mt * 64 + hw * 16 + (lane & 15)) * 16;
#pragma unroll
  for (int ks = 0; ks < 20; ++ks) af[ks] = ld16_sc1(rw, ao, ks * 4 * PLANE);
  SB0();
  ldsp wp = lds + lane * 16;
#pragma unroll
  for (int j = 0; j < 10; ++j) wf[j / 5][j % 5] = lds_ld16(wp + j * 1024);
#pragma unroll
  for (int ks = 0; ks < 20; ++ks) {
#pragma unroll
    for (int j = 0; j < 5; ++j) {
      if (ks + 2 < 20) wf[(ks + 2) % 3][j] = lds_ld16(wp + (ks + 2) * 5120 + j * 1024);
      acc[j] = mfma16(wf[ks % 3][j], af[ks], acc[j]);
    }
    SB0();
  }
  TS();
  n80_epilogue_h(acc, rw, mt, ct, hw, lane);
  TS();
  if (hw == 1) { TS_END(2); }
}

template <bool EXACT> DI f32x16 attn_rows(ldsp lds, ldsp kp, ldsp ktp, ldsp vp, bf16x8 qm, u32x2 qtr, int dq, int hi) {
  const float NEG = -__builtin_inff();
  auto mx3 = [](float a, float b, float c) { return __builtin_elementwise_maximum(__builtin_elementwise_maximum(a, b), c); };
  auto qk = [&](int j, u32x2 q2, f32x16 cin) {
    return __builtin_amdgcn_mfma_f32_32x32x8bf16_1k(*(const LAS bf16x4*)(ktp + j * 512), __builtin_bit_cast(bf16x4, q2), mfma32(lds_ld16(kp + j * 512), qm, cin), 0, 0, 0);
  };
  f32x16 s0, s8;
#pragma unroll
  for (int r = 0; r < 16; ++r) { const bool le = (r & 3) + 8 * (r >> 2) <= dq; s8[r] = le ? 0.f : NEG; s0[r] = le ? NEG : 0.f; }
  if (!EXACT) { pin(s0); pin(s8); bar(); }
  bf16x8 km[2]; bf16x4 kt[2];
  s8 = qk(8, qtr, s8); s0 = qk(0, qtr, s0);
  km[1] = lds_ld16(kp + 512); kt[1] = *(const LAS bf16x4*)(ktp + 512);
  km[0] = lds_ld16(kp + 1024); kt[0] = *(const LAS bf16x4*)(ktp + 1024);
  f32x16 sa = mfma32(km[1], qm, (f32x16){});
#pragma unroll
  for (int r = 0; r < 16; ++r) s0[r] = __builtin_elementwise_maximum(s0[r], s8[r]);
  float m = mx3(mx3(mx3(s0[0], s0[1], s0[2]), mx3(s0[3], s0[4], s0[5]), mx3(s0[6], s0[7], s0[8])), mx3(mx3(s0[9], s0[10], s0[11]), s0[12], s0[13]), __builtin_elementwise_maximum(s0[14], s0[15]));
  if (EXACT) {
#pragma unroll 1
    for (int j = 1; j < 8; ++j) {
      const f32x16 t = qk(j, qtr, (f32x16){});
#pragma unroll
      for (int r = 0; r < 16; r += 2) m = mx3(m, t[r], t[r + 1]);
    }
  }
  {
    auto rr = __builtin_amdgcn_permlane32_swap(__float_as_uint(m), __float_as_uint(m), false, false);
    m = __builtin_elementwise_maximum(__uint_as_float(rr[0]), __uint_as_float(rr[1]));
  }
  u32x2 q2 = qtr;
  {
    const unsigned u0 = __float_as_uint(m) & 0xffff0000u;
    const float r1 = m - __uint_as_float(u0);
    const unsigned u1 = __float_as_uint(r1) & 0xffff0000u;
    const float r2 = r1 - __uint_as_float(u1);
    if (hi) q2 = (u32x2){0x3f80u | u0, (u1 >> 16) | (__float_as_uint(r2) & 0xffff0000u)};
  }
  const bf16x4 qs = __builtin_bit_cast(bf16x4, q2);
  s0 = __builtin_amdgcn_mfma_f32_32x32x8bf16_1k(__builtin_bit_cast(bf16x4, hi ? (u32x2){0xbf800000u, 0xbf80bf80u} : (u32x2){0u, 0u}), qs, s0, 0, 0, 0);
  sa = __builtin_amdgcn_mfma_f32_32x32x8bf16_1k(kt[1], qs, sa, 0, 0, 0);
  f32x16 sb = __builtin_amdgcn_mfma_f32_32x32x8bf16_1k(kt[0], qs, mfma32(km[0], qm, (f32x16){}), 0, 0, 0);
  SB0();
  auto ex8 = [&](const f32x16& s, int ks) {
    u32x4 p;
#pragma unroll
    for (int e = 0; e < 4; ++e) p[e] = cvtpk(__builtin_amdgcn_exp2f(s[8 * ks + 2 * e]), __builtin_amdgcn_exp2f(s[8 * ks + 2 * e + 1]));
    return p;
  };
  auto vld = [&](int slot, int ks) { return lds_tr2(vp + slot * 512 + ks * 256, vp + slot * 512 + ks * 256 + 128); };
  f32x16 o = {};
  auto pv = [&](bf16x8 v, u32x4 p) { o = mfma32(v, __builtin_bit_cast(bf16x8, p), o); };
  const u32x4 mk0 = *(const LAS u32x4*)(lds + K2_MSK + (dq + 20) * 16), mk1 = *(const LAS u32x4*)(lds + K2_MSK + (dq + 4) * 16);
  u32x4 p0[2], pp[2], pc[2];
  p0[0] = ex8(s0, 0); p0[1] = ex8(s0, 1);
  const u32x4 vrdy = *(volatile LAS u32x4*)(lds + K2_FL + 16);
  km[1] = lds_ld16(kp + 3 * 512); kt[1] = *(const LAS bf16x4*)(ktp + 3 * 512);
  pp[0] = ex8(sa, 0); pp[1] = ex8(sa, 1);
  pin(p0[0]); pin(p0[1]); pin(pp[0]); pin(pp[1]);
  if (!EXACT && __builtin_expect(__builtin_amdgcn_readfirstlane((vrdy[0] & vrdy[1]) & (vrdy[2] & vrdy[3])) == 0, 0)) lds_wait4(lds + K2_FL + 16, 1);
  asm volatile("" ::: "memory");
  bf16x8 vf[2][2], vz[4];
  vf[1][0] = vld(1, 0); vf[1][1] = vld(1, 1);
  SB0();
  sfor<2, 8>([&](auto jc) {
    constexpr int j = decltype(jc)::value;
    f32x16& cur = (j & 1) ? sa : sb;
    f32x16& nxt = (j & 1) ? sb : sa;
    vf[j & 1][0] = vld(j, 0); vf[j & 1][1] = vld(j, 1);
    if constexpr (j + 2 < 8) { km[j & 1] = lds_ld16(kp + (j + 2) * 512); kt[j & 1] = *(const LAS bf16x4*)(ktp + (j + 2) * 512); }
    if constexpr (j == 6) { vz[0] = vld(0, 0); vz[1] = vld(8, 0); }
    if constexpr (j == 7) { vz[2] = vld(0, 1); vz[3] = vld(8, 1); }
    if constexpr (j + 1 < 8) nxt = mfma32(km[(j + 1) & 1], qm, (f32x16){});
    else { const u32x4 a = p0[0] & mk0; pv(vz[0], a); pv(vz[1], p0[0] ^ a); }
    SB0();
    pc[0] = ex8(cur, 0); pv(vf[(j - 1) & 1][0], pp[0]);
    if constexpr (j + 1 < 8) nxt = __builtin_amdgcn_mfma_f32_32x32x8bf16_1k(kt[(j + 1) & 1], qs, nxt, 0, 0, 0);
    SB0();
    pc[1] = ex8(cur, 1); pv(vf[(j - 1) & 1][1], pp[1]); SB0();
    pp[0] = pc[0]; pp[1] = pc[1];
  });
  { const u32x4 a = p0[1] & mk1; pv(vz[2], a); pv(vz[3], p0[1] ^ a); }
  pv(vf[1][0], pp[0]); pv(vf[1][1], pp[1]);
  return o;
}

DI void attn_body(ldsp lds, char* __restrict__ ws, int c, int h, int w, int lane) {
  const int r32 = lane & 31, hi = lane >> 5;
  TS_DECL;
  const int pm = 5 * (h >> 1) + 3 * (h & 1), pt = 5 * (h >> 1) + 2;
  const char* qb = ws + WS_QK + (256 * c + 32 * w) * 16;
  const bf16x8 qm = *(const bf16x8*)(qb + ((pm + hi) * S_ + r32) * 16);
  u32x2 qtr = *(const u32x2*)(qb + (pt * S_ + r32) * 16 + (h & 1) * 8);
  if (hi) qtr = (u32x2){0x3f80u, 0u};
  ldsp kp = lds + K2_K + hi * 8192 + (w * 32 + r32) * 16;
  ldsp ktp = lds + (hi ? K2_V + 3 * K2_VP + w * 512 : K2_K + 16384 + (w * 32 + r32) * 16 + (h & 1) * 8);
  const int vq = (lane & 15) >> 2, vpp = lane & 3, vblk = (lane >> 4) & 1;
  ldsp vp = lds + K2_V + (w * 32 + 4 * hi + vq) * 16 + (vblk == 0 ? (vpp >> 1) * K2_VP + (vpp & 1) * 8 : vpp == 0 ? 2 * K2_VP + (h & 1) * 8 : 3 * K2_VP + 8);
  f32x16 o = attn_rows<false>(lds, kp, ktp, vp, qm, qtr, r32 - 4 * hi, hi);
  float l = __uint_as_float(__builtin_amdgcn_permlane32_swap(__float_as_uint(o[8]), __float_as_uint(o[8]), false, false)[1]);
  if (__builtin_expect(__builtin_amdgcn_ballot_w64(!(l < LAZY_LIM)) != 0, 0)) {
    o = attn_rows<true>(lds, kp, ktp, vp, qm, qtr, r32 - 4 * hi, hi);
    l = __uint_as_float(__builtin_amdgcn_permlane32_swap(__float_as_uint(o[8]), __float_as_uint(o[8]), false, false)[1]);
  }
  TS();
  const float il = __builtin_amdgcn_rcpf(l);
  const int tok = 256 * c + 32 * w + r32;
  const rsrc_t rw = mk_rsrc(ws, WS_END);
  const u32x4 w0 = {cvtpk(o[0] * il, o[1] * il), cvtpk(o[2] * il, o[3] * il), cvtpk(o[4] * il, o[5] * il), cvtpk(o[6] * il, o[7] * il)};
  st16(rw, WS_QK + (pm + hi) * PLANE + tok * 16, w0);
  if (hi == 0) st8(rw, WS_QK + pt * PLANE + tok * 16 + (h & 1) * 8, (u32x2){cvtpk(o[8] * il, o[9] * il), cvtpk(o[10] * il, o[11] * il)});
  TS();
  wait_vm<0>();
  if (lds_add(lds + K2_FL + 4, lane) == 7 && lane < (32 >> CSH)) cnt_add(ws, (c * 32 >> CSH) + lane);
  if (w == 7 || w == 0) { TS_END(w == 7 ? 1 : 5); }
}

extern "C" __global__ __launch_bounds__(768, 3) void k_attn(char* __restrict__ ws, const u16* __restrict__ x, const u16* __restrict__ wg, const u16* __restrict__ wu, const u16* __restrict__ wd) {
  extern __shared__ __attribute__((aligned(16))) char smem[];
  const int lane = threadIdx.x & 63, w = __builtin_amdgcn_readfirstlane(threadIdx.x >> 6);
  const int xcd = blockIdx.x & 7, idx = blockIdx.x >> 3, h = xcd * 4 + (idx >> 3), c = idx & 7;
  if (w < 4) attn_helper((ldsp)smem, ws, c, h, w, lane, x, wg, wu, wd);
  else attn_body((ldsp)smem, ws, c, h, w - 4, lane);
}

constexpr int K4_NS = 4, K4_STG = 20480, K4_NT = 20, K4_RING = 61440, K4_CTL = K4_RING + K4_NS * K4_STG, K4_SD = K4_CTL + 2048, K4_LDS = K4_SD + 8192 + 8192;

extern "C" __global__ __launch_bounds__(512, 2) void k_mlp(char* __restrict__ ws, u16* __restrict__ out) {
  extern __shared__ __attribute__((aligned(16))) char smem[];
  ldsp lds = (ldsp)smem + K4_RING;
  ldsp ctl = (ldsp)smem + K4_CTL;
  const int lane = threadIdx.x & 63, w = __builtin_amdgcn_readfirstlane(threadIdx.x >> 6);
  const unsigned lds00 = (unsigned)(uintptr_t)(ldsp)smem, lds0 = lds00 + K4_RING;
  const int xcd = blockIdx.x & 7, idx = blockIdx.x >> 3, mt = (xcd >> 2) * 8 + (idx >> 2), ft = (xcd & 3) * 4 + (idx & 3);
  const int mt5 = (xcd >> 1) * 8 + (idx >> 2), ct = (xcd & 1) * 4 + (idx & 3);
  const int odd = ct & 1, pb = (ct >> 1) * 5 + (odd ? 3 : 0), ps = (ct >> 1) * 5 + 2;
  if (w >= 4) {
    const int lw = w - 4;
    const char* wd = ws + WS_WD + ct * 245760;
    {
      const bool isa = w < 6;
      const char* src = isa ? ws + WS_H + ((mt & 1) * 40 + lw) * PLANE + (mt >> 1) * 4096 : ws + WS_WGU + ft * K4_NT * 12288 + (w - 6) * 6144;
      const unsigned dst = isa ? lds0 + lw * 4096 : lds0 + 8192 + (w - 6) * 6144;
      auto one = [] {};
      constexpr int XP = 0;
      if (isa) loader_loop<4 + XP, K4_NT, K4_NS, 0>([&](int slot) { dma_run<4>(src, dst + slot * K4_STG, lane * 16); src += 2 * PLANE; one(); },
                                               [&] { dma_run<4>(ws + WS_ST + lw * S_ * 32 + mt * 4096, lds00 + K4_SD + lw * 4096, lane * 16); }, [] {});
      else loader_loop<6 + XP, K4_NT, K4_NS, 0>([&](int slot) { dma_run<6>(src, dst + slot * K4_STG, lane * 16); src += 12288; one(); },
                                           [&] { dma_run<4>(ws + WS_CS + ft * 8192 + (w - 6) * 4096, lds00 + K4_SD + 8192 + (w - 6) * 4096, lane * 16); }, [] {});
    }
    dma_long<15>(wd + lw * 15360, lds00 + lw * 15360, lane * 16);
    {
      set_m0(lds00 + K4_SD + lw * 3072);
      const char* hs = ws + WS_H + ((mt5 >> 1) & 1) * 40 * PLANE + (mt5 >> 2) * 4096 + ((mt5 & 1) * 2 + (lw >> 1)) * 1024;
      const unsigned vo = (lane >> 5) * PLANE + (((lane >> 4) & 1) * 32 + (lw & 1) * 16 + (lane & 15)) * 16 + 2048;
      dma<0>(hs + pb * 2 * PLANE - 2048, vo); dma<1024>(hs + (pb + 1) * 2 * PLANE - 3072, vo); dma<2048>(hs + ps * 2 * PLANE - 4096, vo);
    }
    lds_wait(ctl, 4);
    dma_long<20>(wd + 61440 + lw * 20480, lds0 + lw * 20480, lane * 16);
    wait_vm_bar<0>();
#pragma unroll 1
    for (int g = 0; g < 5; ++g) {
      lds_wait4(ctl + 16, g + 1);
      dma_run<5>(wd + 143360 + g * 20480 + lw * 5120, lds00 + g * 20480 + lw * 5120, lane * 16);
      wait_vm<0>();
      lds_post(ctl + 32 + lw * 4, g + 1, lane);
    }
    return;
  }
  if (threadIdx.x < 12) ((LAS unsigned*)ctl)[threadIdx.x] = 0u;
  const int r32 = lane & 31, hi = lane >> 5, wm = w >> 1, wn = w & 1;
  TS_DECL;
  f32x16 acc[2][3];
#pragma unroll
  for (int i = 0; i < 2; ++i)
#pragma unroll
    for (int j = 0; j < 3; ++j) { acc[i][j] = (f32x16){}; pin(acc[i][j]); }
  bf16x8 af[2][2], wf[2][3];
  const int aoff = wm * 2048 + lane * 16, woff = 8192 + wn * 3072 + lane * 16;
  auto rda = [&](ldsp sb, int s, int i) { af[s][i] = lds_ld16(sb + aoff + s * 4096 + i * 1024); };
  auto rdw = [&](ldsp sb, int s, int j) { wf[s][j] = lds_ld16(sb + woff + s * 6144 + j * 1024); };
  auto mm = [&](int s, int i, int j) { acc[i][j] = mfma32(wf[s][j], af[s][i], acc[i][j]); };
  bar();
  const int tok = mt * 128 + wm * 64 + r32;
  f32x4 sv[2][2];
  float cv[10];
  const LAS float* cp = (const LAS float*)((ldsp)smem + K4_SD + 8192) + w * 48 + (lane < 48 ? lane : 47);
  ldsp sp = (ldsp)smem + K4_SD + hi * 4096 + (wm * 64 + r32) * 32;
  TS();
  rda(lds, 0, 0); rdw(lds, 0, 0); rda(lds, 0, 1); rdw(lds, 0, 1); rdw(lds, 0, 2); SB0();
  constexpr float GC0 = -2.f * 0.7978845608028654f * 1.4426950408889634f, GC1 = GC0 * 0.044715f;
  float ca[2], cb[2], nmu[2], var[2];
  sfor<0, K4_NT>([&](auto tc) {
    constexpr int t = decltype(tc)::value;
    constexpr bool more = t + 1 < K4_NT;
    ldsp sb = lds + (t % K4_NS) * K4_STG;
    ldsp nb = lds + ((t + 1) % K4_NS) * K4_STG;
    mm(0, 0, 0); rda(sb, 1, 0); rdw(sb, 1, 0); SB0();
    mm(0, 1, 0); rda(sb, 1, 1); rdw(sb, 1, 1); SB0();
    mm(0, 0, 1); rdw(sb, 1, 2); SB0();
    mm(0, 1, 1); SB0();
    mm(0, 0, 2); SB0();
    mm(0, 1, 2); SB0();
    if (more) { bar(); SB0(); }
    else { asm volatile("s_waitcnt lgkmcnt(0)" ::: "memory"); lds_add(ctl, lane); SB0(); }
    mm(1, 0, 0); if (more) { rda(nb, 0, 0); rdw(nb, 0, 0); } SB0();
    mm(1, 1, 0); if (more) { rda(nb, 0, 1); rdw(nb, 0, 1); } SB0();
    mm(1, 0, 1); if (more) rdw(nb, 0, 2); SB0();
    if constexpr (t == 3) { cv[8] = cp[8 * 192]; cv[9] = cp[9 * 192]; }
    if constexpr (t >= 4 && t <= 7) { cv[2 * t - 8] = cp[(2 * t - 8) * 192]; cv[2 * t - 7] = cp[(2 * t - 7) * 192]; sv[(t - 4) >> 1][t & 1] = *(const LAS f32x4*)(sp + ((t - 4) >> 1) * 1024 + (t & 1) * 16); }
    if constexpr (t == 8) { if (lane < 48) ((LAS float*)(ctl + 1024))[w * 48 + lane] = (((cv[0] + cv[1]) + (cv[2] + cv[3])) + ((cv[4] + cv[5]) + (cv[6] + cv[7]))) + (cv[8] + cv[9]); }
    if constexpr (t == 9 || t == 10) {
      constexpr int i = t - 9;
      const f32x4 s0 = sv[i][0], s1 = sv[i][1];
      const float mu = xsum32((s0[0] + s0[2]) + (s1[0] + s1[2])) * 0.125f;
      const float d0 = s0[0] - mu, d1 = s0[2] - mu, d2 = s1[0] - mu, d3 = s1[2] - mu;
      var[i] = xsum32((s0[1] + s0[3]) + (s1[1] + s1[3]) + 80.f * ((d0 * d0 + d1 * d1) + (d2 * d2 + d3 * d3))) * (1.f / D_) + LN_EPS;
      const float rstd = __builtin_amdgcn_rsqf(var[i]);
      ca[i] = GC0 * rstd; cb[i] = GC1 * rstd * rstd * rstd; nmu[i] = -mu;
    }
    mm(1, 1, 1);
    mm(1, 0, 2);
    mm(1, 1, 2);
    if constexpr (t == 9 || t == 10) { pin(ca[t - 9]); pin(cb[t - 9]); pin(nmu[t - 9]); pin(var[t - 9]); }
    SB0();
  });
  TS();
  const rsrc_t rw = mk_rsrc(ws, WS_END);
  const LAS float* csb = (const LAS float*)(ctl + 1024) + wn * 96 + 4 * hi;
  f32x4 cq[3][4];
#pragma unroll
  for (int j = 0; j < 3; ++j)
#pragma unroll
    for (int b = 0; b < 4; ++b) cq[j][b] = *(const LAS f32x4*)(csb + j * 32 + 8 * b);
#pragma unroll
  for (int i = 0; i < 2; ++i) {
    const int tk = tok + 32 * i;
    const int ob = WS_ACT + (((tk >> 6) * 48 + ft * 3) * 4 + ((tk >> 4) & 3)) * 1024 + (hi * 16 + (tk & 15)) * 16;
#pragma unroll
    for (int j = 0; j < 3; ++j) {
      unsigned pk[4];
#pragma unroll
      for (int r = 0; r < 8; r += 2) {
        const f32x2 gv = (f32x2){acc[i][j][r], acc[i][j][r + 1]} + nmu[i] * (f32x2){cq[j][r >> 2][r & 3], cq[j][r >> 2][(r & 3) + 1]};
        const f32x2 uv = (f32x2){acc[i][j][8 + r], acc[i][j][9 + r]} + nmu[i] * (f32x2){cq[j][2 + (r >> 2)][r & 3], cq[j][2 + (r >> 2)][(r & 3) + 1]};
        const f32x2 arg = gv * (gv * gv * cb[i] + ca[i]);
        const f32x2 ex = {__builtin_amdgcn_exp2f(arg[0]), __builtin_amdgcn_exp2f(arg[1])};
        const f32x2 den = ex * var[i] + var[i];
        const f32x2 rc = {__builtin_amdgcn_rcpf(den[0]), __builtin_amdgcn_rcpf(den[1])};
        const f32x2 a = gv * uv * rc;
        pk[r >> 1] = cvtpk(a[0], a[1]);
      }
      const int T = wn * 3 + j;
      st16<AUX4>(rw, ob + (T >> 1) * 4096 + (T & 1) * 512, (u32x4){pk[0], pk[1], pk[2], pk[3]});
    }
  }
  TS();
  wait_vm<0>();
  if (lds_add(ctl + 4 + wm * 4, lane) == 1 && lane < (8 >> CSH)) cnt_add(ws, 256 + ((mt * 2 + wm) * 8 >> CSH) + lane);
  TS();
  if (w == 0) { __builtin_amdgcn_s_sleep(PDLY); cnt_wait(ws, 256 + ((mt5 * 8 + ct) >> CSH), 16); if (lane == 0) *(volatile LAS unsigned*)(ctl + 12) = 1u; }
  else lds_wait(ctl + 12, 1);
  TS();
  constexpr int WIN = 2 * WIN5;
  bf16x8 a5[WIN], w5[3][5];
  const int ao = WS_ACT + mt5 * 196608 + w * 1024 + lane * 16;
#pragma unroll
  for (int ks = 0; ks < WIN; ++ks) a5[ks] = ld16_sc1(rw, ao, ks * 4096);
  SB0();
  bar();
  TS();
  f32x4 ac5[5];
  {
    ldsp rp = (ldsp)smem + K4_SD + w * 3072 + lane * 16;
#pragma unroll
    for (int j = 0; j < 5; ++j) {
      const int e = odd ? (j == 0 ? 5 : j - 1) : j;
      const u32x2 r = *(const LAS u32x2*)(rp + (e >> 1) * 1024 + (e & 1) * 8);
      ac5[j] = (f32x4){bflo(r[0]), bfhi(r[0]), bflo(r[1]), bfhi(r[1])};
    }
  }
  ldsp wp = (ldsp)smem + lane * 16;
#pragma unroll
  for (int j = 0; j < 10; ++j) w5[j / 5][j % 5] = lds_ld16(wp + j * 1024);
  sfor<0, 48>([&](auto kc) {
    constexpr int ks = decltype(kc)::value, kn = ks + 2, tn = kn / 2;
    if constexpr ((kn & 3) == 0 && kn >= 28 && kn < 48) { lds_wait4(ctl + 32, (kn - 28) / 4 + 1); SB0(); }
    if constexpr ((kn & 1) == 0 && (tn == REL0 || tn == REL1 || tn == REL2 || tn == REL3 || tn == REL4)) {
      asm volatile("" ::: "memory");
      lds_post(ctl + 16 + w * 4, tn == REL0 ? 1 : tn == REL1 ? 2 : tn == REL2 ? 3 : tn == REL3 ? 4 : 5, lane);
    }
#pragma unroll
    for (int j = 0; j < 5; ++j) {
      if constexpr (kn < 48) w5[kn % 3][j] = lds_ld16(wp + (kn % 28) * 5120 + j * 1024);
      ac5[j] = mfma16(w5[ks % 3][j], a5[ks % WIN], ac5[j]);
    }
    if constexpr (ks + WIN < 48) a5[ks % WIN] = ld16_sc1(rw, ao, (ks + WIN) * 4096);
    SB0();
  });
  TS();
  {
    const int g = lane >> 4;
    u32x2 hv[5];
#pragma unroll
    for (int j = 0; j < 5; ++j) hv[j] = (u32x2){cvtpk(ac5[j][0], ac5[j][1]), cvtpk(ac5[j][2], ac5[j][3])};
    const rsrc_t ro = mk_rsrc(out, S_ * D_ * 2);
    const int oo = (mt5 * 64 + w * 16 + (lane & 15)) * 1280 + ct * 160;
#pragma unroll
    for (int jp = 0; jp < 2; ++jp) {
      auto r0 = __builtin_amdgcn_permlane16_swap(hv[2 * jp][0], hv[2 * jp + 1][0], false, false);
      auto r1 = __builtin_amdgcn_permlane16_swap(hv[2 * jp][1], hv[2 * jp + 1][1], false, false);
      st16<AUX5>(ro, oo + jp * 64 + (g & 1) * 32 + (g >> 1) * 16, (u32x4){r0[0], r1[0], r0[1], r1[1]});
    }
    st8<AUX5>(ro, oo + 128 + g * 8, hv[4]);
  }
  TS();
  if (w == 0) { TS_END(3); }
}

extern "C" __global__ void k_text_end() {}

extern "C" void kernel_launch(void* const* d_in, const int* in_sizes, int n_in, void* d_out, int out_size, void* d_ws, size_t ws_size,
                              hipStream_t stream) {
  const u16* x = (const u16*)d_in[0];
  const u16* wqkv = (const u16*)d_in[1];
  const u16* wo = (const u16*)d_in[2];
  const u16* wg = (const u16*)d_in[4];
  const u16* wu = (const u16*)d_in[5];
  const u16* wd = (const u16*)d_in[6];
  char* ws = (char*)d_ws;
  u16* out = (u16*)d_out;
  (void)hipFuncSetAttribute((const void*)k_qkv, hipFuncAttributeMaxDynamicSharedMemorySize, K1_LDS);
  (void)hipFuncSetAttribute((const void*)k_attn, hipFuncAttributeMaxDynamicSharedMemorySize, K2_LDS);
  (void)hipFuncSetAttribute((const void*)k_mlp, hipFuncAttributeMaxDynamicSharedMemorySize, K4_LDS);
  k_qkv<<<256, 768, K1_LDS, stream>>>(x, wqkv, wo, wd, ws);
  k_attn<<<256, 768, K2_LDS, stream>>>(ws, x, wg, wu, wd);
  k_mlp<<<256, 512, K4_LDS, stream>>>(ws, out);
}
```

```cpp
#include <hip/hip_runtime.h>
#include <stdint.h>
#include <type_traits>

#define LAS __attribute__((address_space(3)))
#define DI __device__ __forceinline__
typedef unsigned short u16;
typedef short bf16x8 __attribute__((ext_vector_type(8)));
typedef short bf16x4 __attribute__((ext_vector_type(4)));
typedef __bf16 bf16x2_t __attribute__((ext_vector_type(2)));
typedef float f32x2 __attribute__((ext_vector_type(2)));
typedef float f32x4 __attribute__((ext_vector_type(4)));
typedef float f32x16 __attribute__((ext_vector_type(16)));
typedef unsigned u32x4 __attribute__((ext_vector_type(4)));
typedef unsigned u32x2 __attribute__((ext_vector_type(2)));
typedef LAS char* ldsp;
typedef __amdgpu_buffer_rsrc_t rsrc_t;

constexpr int S_ = 2048, D_ = 640, DF = 1536;
constexpr float LN_EPS = 1e-5f;
constexpr float QSCALE = 0.22360679774997896f * 1.4426950408889634f;
constexpr unsigned ONES2 = 0x3f803f80u;

constexpr int PLANE = S_ * 16;
constexpr int WS_QK = 0;
constexpr int WS_ACT = WS_QK + 80 * PLANE;
constexpr int WS_H = WS_ACT + S_ * DF * 2;
constexpr int WS_WO = WS_H + S_ * D_ * 2;
constexpr int WS_ST = WS_WO + D_ * D_ * 2;
constexpr int WS_CS = WS_ST + S_ * 8 * 8;
constexpr int WS_WGU = WS_CS + 16 * 8192;
constexpr int WS_WD = WS_WGU + D_ * DF * 4;
static_assert(WS_WO >= 0 && WS_QK % 4096 == 0, "workspace map");
constexpr int WS_CNT = WS_WD + D_ * DF * 2;
constexpr int CSTR = 64;
constexpr int WS_END = WS_CNT + 512 * CSTR * 4;


template <int I, int N, class F> DI void sfor(F&& f) {
  if constexpr (I < N) { f(std::integral_constant<int, I>{}); sfor<I + 1, N>(f); }
}
#define SB0() __builtin_amdgcn_sched_barrier(0)
DI unsigned cvtpk(float lo, float hi) { f32x2 v = {lo, hi}; return __builtin_bit_cast(unsigned, __builtin_convertvector(v, bf16x2_t)); }
DI float bflo(unsigned u) { return __uint_as_float(u << 16); }
DI float bfhi(unsigned u) { return __uint_as_float(u & 0xffff0000u); }
DI f32x4 mfma16(bf16x8 a, bf16x8 b, f32x4 c) { return __builtin_amdgcn_mfma_f32_16x16x32_bf16(a, b, c, 0, 0, 0); }
DI f32x16 mfma32(bf16x8 a, bf16x8 b, f32x16 c) { return __builtin_amdgcn_mfma_f32_32x32x16_bf16(a, b, c, 0, 0, 0); }
DI void set_m0(unsigned v) { asm volatile("s_mov_b32 m0, %0\n\ts_nop 0" ::"s"(v) : "memory"); }
template <int OFF> DI void dma(const void* sbase, unsigned voff) {
  asm volatile("global_load_lds_dwordx4 %0, %1 offset:%2" ::"v"(voff), "s"(sbase), "n"(OFF) : "memory");
}
template <int OFF> DI void dma_nt(const void* sbase, unsigned voff) {
  asm volatile("global_load_lds_dwordx4 %0, %1 offset:%2 nt" ::"v"(voff), "s"(sbase), "n"(OFF) : "memory");
}
template <int N> DI void wait_vm() { asm volatile("s_waitcnt vmcnt(%0)" ::"n"(N) : "memory"); }
template <int N> DI void wait_vm_bar() { asm volatile("s_waitcnt vmcnt(%0) lgkmcnt(0)\n\ts_barrier" ::"n"(N) : "memory"); }
DI void bar() { asm volatile("s_waitcnt lgkmcnt(0)\n\ts_barrier" ::: "memory"); }
template <class T> DI void pin(T& v) { asm volatile("" : "+v"(v)); }
DI bf16x8 lds_ld16(ldsp p) { return *(const LAS bf16x8*)p; }
DI bf16x4 lds_tr(ldsp p) { return __builtin_amdgcn_ds_read_tr16_b64_v4i16((LAS bf16x4*)p); }
DI bf16x8 lds_tr2(ldsp p0, ldsp p1) {
  bf16x4 a = lds_tr(p0), b = lds_tr(p1);
  return __builtin_shufflevector(a, b, 0, 1, 2, 3, 4, 5, 6, 7);
}
DI float xsum32(float v) {
  auto r = __builtin_amdgcn_permlane32_swap(__float_as_uint(v), __float_as_uint(v), false, false);
  return __uint_as_float(r[0]) + __uint_as_float(r[1]);
}
DI float xsum16_32(float v) {
  auto r = __builtin_amdgcn_permlane16_swap(__float_as_uint(v), __float_as_uint(v), false, false);
  return xsum32(__uint_as_float(r[0]) + __uint_as_float(r[1]));
}
DI rsrc_t mk_rsrc(void* p, int bytes) { return __builtin_amdgcn_make_buffer_rsrc(p, 0, bytes, 0x00020000); }
template <int AUX = 16> DI void st16(rsrc_t r, int off, u32x4 v) { __builtin_amdgcn_raw_buffer_store_b128(v, r, off, 0, AUX); }
template <int AUX = 16> DI void st8(rsrc_t r, int off, u32x2 v) { __builtin_amdgcn_raw_buffer_store_b64(v, r, off, 0, AUX); }
constexpr int CSH = 2;

template <int P, int NT, int NS, int X, class F, class G> DI void loader_loop(F&& issue, G&& extra) {
  static_assert(NS >= 4, "three stages in flight + one being read");
  issue(0); issue(1);
  wait_vm_bar<P>();
  issue(2); issue(3);
  int slot = 4 % NS;
#pragma unroll 1
  for (int t = 1; t < NT - 3; ++t) {
    wait_vm_bar<2 * P>();
    issue(slot);
    slot = slot == NS - 1 ? 0 : slot + 1;
  }
  extra();
  wait_vm_bar<2 * P + X>(); wait_vm_bar<P + X>(); wait_vm_bar<X>();
}
DI void touch(const char* p, int lane, int nlines, unsigned lds_scratch) {
  set_m0(lds_scratch);
  asm volatile("global_load_lds_dword %0, %1" ::"v"((unsigned)((lane < nlines ? lane : nlines - 1) * 128)), "s"(p) : "memory");
}
#define CODE_ADDR(dst, sym) asm volatile("s_getpc_b64 s[40:41]\n\ts_add_u32 s40, s40, " #sym "@rel32@lo+4\n\ts_addc_u32 s41, s41, " #sym "@rel32@hi+12\n\ts_mov_b64 %0, s[40:41]" : "=s"(dst) :: "s40", "s41", "scc")
template <int P> DI void dma_run(const char* src, unsigned dst, unsigned lane16) {
  static_assert(P <= 8, "offset range");
  set_m0(dst + 4096);
  sfor<0, P>([&](auto ic) { dma<decltype(ic)::value * 1024 - 4096>(src + 4096, lane16); });
}
template <int N> DI void dma_long(const char* src, unsigned dst, unsigned lane16) {
  sfor<0, N / 5>([&](auto ic) { constexpr int i = decltype(ic)::value; dma_run<5>(src + i * 5120, dst + i * 5120, lane16); });
}
DI unsigned lds_add(ldsp p, int lane) {
  unsigned old = 0;
  if (lane == 0) old = __hip_atomic_fetch_add((LAS unsigned*)p, 1u, __ATOMIC_RELAXED, __HIP_MEMORY_SCOPE_WORKGROUP);
  return __builtin_amdgcn_readfirstlane(old);
}
DI void lds_wait(ldsp p, unsigned want) {
#pragma unroll 1
  for (int sp = 0; sp < (1 << 22); ++sp) {
    if ((unsigned)__builtin_amdgcn_readfirstlane(*(volatile LAS unsigned*)p) >= want) break;
    __builtin_amdgcn_s_sleep(1);
  }
  asm volatile("" ::: "memory");
}
DI void lds_post(ldsp p, unsigned v, int lane) { if (lane == 0) *(volatile LAS unsigned*)p = v; asm volatile("" ::: "memory"); }
DI void lds_wait4(ldsp p, unsigned want) {
#pragma unroll 1
  for (int sp = 0; sp < (1 << 22); ++sp) {
    const u32x4 v = *(volatile LAS u32x4*)p;
    if ((unsigned)__builtin_amdgcn_readfirstlane(min(min(v[0], v[1]), min(v[2], v[3]))) >= want) break;
    __builtin_amdgcn_s_sleep(1);
  }
  asm volatile("" ::: "memory");
}
DI void cnt_add(char* ws, int idx) { __hip_atomic_fetch_add((unsigned*)(ws + WS_CNT) + idx * CSTR, 1u, __ATOMIC_RELAXED, __HIP_MEMORY_SCOPE_AGENT); }
DI void cnt_wait(char* ws, int idx, unsigned want) {
  unsigned* p = (unsigned*)(ws + WS_CNT) + idx * CSTR;
#pragma unroll 1
  for (int sp = 0; sp < (1 << 18); ++sp) {
    if ((unsigned)__builtin_amdgcn_readfirstlane(__hip_atomic_load(p, __ATOMIC_RELAXED, __HIP_MEMORY_SCOPE_AGENT)) >= want) break;
    __builtin_amdgcn_s_sleep(2);
  }
  __builtin_amdgcn_fence(__ATOMIC_ACQUIRE, "wavefront");
}
DI bf16x8 ld16_sc1(rsrc_t r, int voff, int soff) { return __builtin_bit_cast(bf16x8, __builtin_amdgcn_raw_buffer_load_b128(r, voff, soff, 16)); }

template <int KIND> DI void pack_issue(const u16* wsrc, const u16* wsrc2, int ks, int cg, unsigned ldsdst, int lane) {
  constexpr int PITCH = KIND == 1 ? DF * 2 : D_ * 2;
  set_m0(ldsdst);
  sfor<0, 4>([&](auto ic) {
    constexpr int g = decltype(ic)::value;
    const int t = (lane >> 5) & 1, q = (lane >> 3) & 3;
    int row;
    if (KIND == 0) { const int P = 4 * ks + g, hp = P / 5, r = P - 5 * hp; row = 40 * hp + q + (r < 2 ? 8 * t + 4 * r : r == 2 ? 16 + 4 * t : 12 + 8 * t + 4 * r); }
    else if (KIND == 1) row = 32 * (ks >> 1) + 16 * t + 4 * (2 * (ks & 1) + (g & 1)) + q;
    else row = 32 * ks + 16 * (g >> 1) + 8 * t + 4 * (g & 1) + q;
    dma_nt<g * 1024>((const char*)(KIND == 1 && g >= 2 ? wsrc2 : wsrc) + cg * 128 - 4096, (unsigned)(row * PITCH + (lane & 7) * 16 + 4096 - g * 1024));
  });
}
template <int KIND> DI void pack_finish(ldsp u, rsrc_t rw, int ks, int cg, int lane, LAS float* cs) {
  const int q = (lane & 15) >> 2, p = lane & 3;
  ldsp a = KIND == 1 ? u + ((lane >> 4) & 1) * 2048 + (8 * (lane >> 5) + q) * 128 + p * 8 : u + (8 * (lane >> 4) + q) * 128 + p * 8;
#pragma unroll
  for (int i = 0; i < 4; ++i) {
    const int nt = 4 * cg + i;
    const u32x4 f = __builtin_bit_cast(u32x4, lds_tr2(a + i * 32, a + i * 32 + 512));
    if (KIND == 1) {
      const int ft = nt / 6, j = nt - ft * 6;
      st16(rw, WS_WGU + ((ft * 40 + ks) * 6 + j) * 1024 + lane * 16, f);
      const unsigned sv = (lane & 15) == ((lane >> 4) & 1) ? ONES2 : 0u;
      const f32x4 c = mfma16(__builtin_bit_cast(bf16x8, (u32x4){sv, sv, sv, sv}), __builtin_bit_cast(bf16x8, f), (f32x4){0.f, 0.f, 0.f, 0.f});
      if (lane < 16) { cs[i * 32 + lane] = c[0]; cs[i * 32 + 16 + lane] = c[1]; }
    } else {
      const int ct = nt / 5, j = nt - ct * 5;
      st16(rw, (KIND == 0 ? WS_WO : WS_WD) + ((ct * (KIND == 0 ? 20 : 48) + ks) * 5 + j) * 1024 + lane * 16, f);
    }
  }
}

constexpr int NWD_A = 352;
constexpr int K1_NS = 4, K1_STG = 32768, K1_NT = 10, K1_CTL = K1_NS * K1_STG, K1_LDS = K1_CTL + 4096;

DI void qkv_compute(ldsp lds, char* __restrict__ ws, int mt, int nt, int w, int lane) {
  const int wm = w >> 1, wn = w & 1, r32 = lane & 31, hi = lane >> 5, q = (lane & 15) >> 2, p = lane & 3, blk = (lane >> 4) & 1;
  ldsp ctl = lds + K1_CTL;
  int aoff[4], woff[2];
#pragma unroll
  for (int s = 0; s < 4; ++s) aoff[s] = (wm * 64 + r32) * 128 + (((2 * s + hi) ^ ((r32 >> 1) & 7)) * 16);
#pragma unroll
  for (int j = 0; j < 2; ++j) woff[j] = 16384 + (8 * hi + q) * 256 + (((wn * 2 + (j ^ wm)) ^ q) * 64) + blk * 32 + p * 8;
  f32x16 acc[2][2];
#pragma unroll
  for (int j = 0; j < 2; ++j)
#pragma unroll
    for (int i = 0; i < 2; ++i) { acc[j][i] = (f32x16){}; pin(acc[j][i]); }
  f32x4 cs = {};
  const unsigned sv = (lane & 15) == blk ? ONES2 : 0u;
  const bf16x8 sel = __builtin_bit_cast(bf16x8, (u32x4){sv, sv, sv, sv});
  bf16x8 xf[2][2], wf[2][2];
  auto rdx = [&](ldsp sb, int s, int i) { xf[s & 1][i] = lds_ld16(sb + aoff[s] + i * 4096); };
  auto rdw = [&](ldsp sb, int s, int j) { wf[s & 1][j] = lds_tr2(sb + woff[j] + s * 4096, sb + woff[j] + s * 4096 + 1024); };
  auto mm = [&](int s, int j, int i) { acc[j][i] = mfma32(wf[s & 1][j], xf[s & 1][i], acc[j][i]); };
  auto step = [&](ldsp nb, int s, int sn, bool rd) {
    cs = mfma16(sel, wf[s & 1][0], cs);
    mm(s, 0, 0); if (rd) { rdx(nb, sn, 0); rdw(nb, sn, 0); } SB0();
    mm(s, 0, 1); if (rd) { rdx(nb, sn, 1); rdw(nb, sn, 1); } SB0();
    mm(s, 1, 0); SB0();
    mm(s, 1, 1); SB0();
  };
  bar();
  ldsp sb = lds;
  rdx(sb, 0, 0); rdw(sb, 0, 0); rdx(sb, 0, 1); rdw(sb, 0, 1); SB0();
  int slot = 0;
#pragma unroll 1
  for (int t = 0; t < K1_NT - 1; ++t) {
    step(sb, 0, 1, true); step(sb, 1, 2, true); step(sb, 2, 3, true);
    slot = slot == K1_NS - 1 ? 0 : slot + 1;
    sb = lds + slot * K1_STG;
    bar(); SB0();
    step(sb, 3, 0, true);
  }
  step(sb, 0, 1, true); step(sb, 1, 2, true); step(sb, 2, 3, true);
  const LAS f32x2* rst = (const LAS f32x2*)(ctl + 1024);
  LAS float* cst = (LAS float*)(ctl + 2048);
  cs = mfma16(sel, wf[1][0], cs);
  mm(3, 0, 0); mm(3, 0, 1); SB0();
  if (lane < 16) { cst[wn * 64 + wm * 32 + lane] = cs[0]; cst[wn * 64 + wm * 32 + 16 + lane] = cs[1]; }
  mm(3, 1, 0); mm(3, 1, 1); SB0();
  bar();
  const rsrc_t rw = mk_rsrc(ws, WS_END);
  const float sc = nt < 5 ? QSCALE : 1.f;
  f32x4 c4[2][4];
#pragma unroll
  for (int j = 0; j < 2; ++j)
#pragma unroll
    for (int b = 0; b < 4; ++b) c4[j][b] = *(const LAS f32x4*)(cst + wn * 64 + (j ^ wm) * 32 + 8 * b + 4 * hi);
#pragma unroll
  for (int i = 0; i < 2; ++i) {
    const int ml = wm * 64 + i * 32 + r32;
    const f32x2 st = rst[ml];
    const float a = st.y * sc, nm = -st.x * a;
    const int ob = WS_QK + ((nt * 16 + wn * 8 + hi) * S_ + mt * 128 + ml) * 16;
#pragma unroll
    for (int j = 0; j < 2; ++j)
#pragma unroll
      for (int bp = 0; bp < 2; ++bp) {
        unsigned pk[2][2];
#pragma unroll
        for (int e = 0; e < 2; ++e) {
          const int b = 2 * bp + e;
          const f32x2 v0 = (f32x2){acc[j][i][4 * b], acc[j][i][4 * b + 1]} * a + nm * (f32x2){c4[j][b][0], c4[j][b][1]};
          const f32x2 v1 = (f32x2){acc[j][i][4 * b + 2], acc[j][i][4 * b + 3]} * a + nm * (f32x2){c4[j][b][2], c4[j][b][3]};
          pk[e][0] = cvtpk(v0[0], v0[1]); pk[e][1] = cvtpk(v1[0], v1[1]);
        }
        auto r0 = __builtin_amdgcn_permlane32_swap(pk[0][0], pk[1][0], false, false);
        auto r1 = __builtin_amdgcn_permlane32_swap(pk[0][1], pk[1][1], false, false);
        st16(rw, ob + ((j ^ wm) * 4 + bp * 2) * S_ * 16, (u32x4){r0[0], r1[0], r0[1], r1[1]});
      }
  }
}

DI void qkv_stats(ldsp lds, int sw, int lane, const char* wo_part) {
  const int fr = lane & 15, g = lane >> 4;
  const int off = (sw * 32 + fr) * 128, x = fr >> 1;
  const bf16x8 ones = __builtin_bit_cast(bf16x8, (u32x4){ONES2, ONES2, ONES2, ONES2});
  f32x4 sm[2] = {}, sq[2] = {};
  int slot = 0;
  __builtin_amdgcn_s_setprio(2);
#pragma unroll 1
  for (int t = 0; t < K1_NT; ++t) {
    bar();
    ldsp sb = lds + slot * K1_STG + off;
    slot = slot == K1_NS - 1 ? 0 : slot + 1;
    if (t == 8) touch(wo_part, lane, 58, (unsigned)(uintptr_t)lds + K1_CTL + 3072);
    bf16x8 v[2][2];
#pragma unroll
    for (int ks = 0; ks < 2; ++ks)
#pragma unroll
      for (int i = 0; i < 2; ++i) v[ks][i] = lds_ld16(sb + i * 2048 + (((4 * ks + g) ^ x) * 16));
#pragma unroll
    for (int ks = 0; ks < 2; ++ks)
#pragma unroll
      for (int i = 0; i < 2; ++i) { sq[i] = mfma16(v[ks][i], v[ks][i], sq[i]); sm[i] = mfma16(ones, v[ks][i], sm[i]); }
  }
#pragma unroll
  for (int i = 0; i < 2; ++i) {
    const int p = lane & 3;
    const float q = p == 0 ? sq[i][0] : p == 1 ? sq[i][1] : p == 2 ? sq[i][2] : sq[i][3];
    const float mu = sm[i][0] * (1.f / D_), var = fmaxf(q * (1.f / D_) - mu * mu, 0.f);
    if ((fr >> 2) == g) ((LAS f32x2*)(lds + K1_CTL + 1024))[sw * 32 + i * 16 + fr] = (f32x2){mu, __builtin_amdgcn_rsqf(var + LN_EPS)};
  }
  bar();
}

DI void qkv_loader(ldsp lds, const u16* __restrict__ x, const u16* __restrict__ wqkv, int mt, int nt, int lw, int lane) {
  const bool isx = lw < 2;
  const int half = lw & 1;
  unsigned voff[8];
#pragma unroll
  for (int i = 0; i < 8; ++i) {
    const int r = (half * 8 + i) * 8 + (lane >> 3), c = (lane & 7) ^ ((r >> 1) & 7);
    const int k = (half * 8 + i) * 4 + (lane >> 4), cp = lane & 15;
    voff[i] = (isx ? r * 1280 + c * 16 : k * 3840 + (cp ^ ((k & 3) << 2)) * 16) + 8192 - i * 1024;
  }
  const char* src = (isx ? (const char*)x + (size_t)mt * 128 * 1280 : (const char*)wqkv + nt * 256) - 4096;
  const int sstr = isx ? 128 : 64 * 3840;
  const unsigned dst = (unsigned)(uintptr_t)lds + lw * 8192 + 4096;
  loader_loop<8, K1_NT, K1_NS, 0>([&](int slot) {
    set_m0(dst + slot * K1_STG);
    sfor<0, 8>([&](auto ic) { constexpr int i = decltype(ic)::value; dma<i * 1024 - 4096>(src, voff[i]); });
    src += sstr;
  }, [] {});
}

extern "C" __global__ __launch_bounds__(768, 3) void k_qkv(const u16* __restrict__ x, const u16* __restrict__ wqkv, const u16* __restrict__ wo, const u16* __restrict__ wd, char* __restrict__ ws) {
  extern __shared__ __attribute__((aligned(16))) char smem[];
  ldsp lds = (ldsp)smem;
  const int lane = threadIdx.x & 63, w = __builtin_amdgcn_readfirstlane(threadIdx.x >> 6);
  const int xcd = blockIdx.x & 7, idx = blockIdx.x >> 3;
  const int nt = (xcd & 1) * 8 + (idx >> 2), mt = (xcd >> 1) * 4 + (idx & 3);
  if (w < 4 && nt < 15) { qkv_loader(lds, x, wqkv, mt, nt, w, lane); return; }
  if (w == 11 && lane < 2) ((unsigned*)(ws + WS_CNT))[(blockIdx.x * 2 + lane) * CSTR] = 0u;
  if (w == 10) {
    const char *c0, *c1;
    CODE_ADDR(c0, k_qkv); CODE_ADDR(c1, k_text_end);
    const unsigned lim = c1 > c0 ? (unsigned)(c1 - c0) : 0u, off = (idx * 16 + (lane & 15)) * 128;
    set_m0((unsigned)(uintptr_t)lds + K1_CTL + 3072 + 512);
    asm volatile("global_load_lds_dword %0, %1" ::"v"(off < lim ? off : lim), "s"(c0) : "memory");
  }
  if (w == 8 || w == 9) {
    const int id = blockIdx.x * 64 + lane, u = w - 8;
    const bool isx = blockIdx.x < 128;
    const int i2 = id - 8192 < 7680 ? id - 8192 : 7679;
    const unsigned off = isx ? (id >> 2) * 1280 + 256 + u * 512 + (id & 3) * 128 : (128 + u * 256) * 3840 + i2 * 128;
    set_m0((unsigned)(uintptr_t)lds + K1_CTL + 3072 + u * 256);
    const char* src = isx ? (const char*)x : (const char*)wqkv;
    asm volatile("" : "+s"(src));
    if (u == 0) __builtin_amdgcn_s_sleep(8); else __builtin_amdgcn_s_sleep(24);
    asm volatile("global_load_lds_dword %0, %1" ::"v"(off), "s"(src) : "memory");
  }
  if (nt == 15) {
    const rsrc_t rw = mk_rsrc(ws, WS_END);
    constexpr int NU = (200 + NWD_A + 191) / 192;
    auto iss = [&](int r) {
      const int u = min(mt * 12 + w + r * 192, 200 + NWD_A - 1), v = u - 200;
      if (u < 200) pack_issue<0>(wo, wo, u / 10, u % 10, (unsigned)(uintptr_t)lds + (w * 2 + (r & 1)) * 4096, lane);
      else pack_issue<2>(wd, wd, v / 10, v % 10, (unsigned)(uintptr_t)lds + (w * 2 + (r & 1)) * 4096, lane);
    };
    iss(0); iss(1);
#pragma unroll 1
    for (int r = 0; r < NU; ++r) {
      if (r == 0) wait_vm<4>(); else if (r + 1 < NU) wait_vm<8>(); else wait_vm<4>();
      const int u = min(mt * 12 + w + r * 192, 200 + NWD_A - 1), v = u - 200;
      if (u < 200) pack_finish<0>(lds + (w * 2 + (r & 1)) * 4096, rw, u / 10, u % 10, lane, nullptr);
      else pack_finish<2>(lds + (w * 2 + (r & 1)) * 4096, rw, v / 10, v % 10, lane, nullptr);
      asm volatile("s_waitcnt lgkmcnt(0)" ::: "memory");
      if (r + 2 < NU) iss(r + 2);
    }
    return;
  }
  if (w >= 8) qkv_stats(lds, w - 8, lane, ws + WS_WO + min((idx * 4 + w - 8) * 58, 6400 - 58) * 128);
  else qkv_compute(lds, ws, mt, nt, w - 4, lane);
}

constexpr float LAZY_LIM = 1.0995116e12f;
constexpr int K2_K = 102400, K2_V = K2_K + 24576, K2_VP = 8192 + 64, K2_CS = K2_V + 4 * K2_VP, K2_MSK = K2_CS + 2560, K2_FL = K2_MSK + 832, K2_LDS = K2_FL + 64 + 256;
static_assert(K2_LDS <= 163840 && K2_FL % 16 == 0, "LDS map");

DI void n80_epilogue_h(f32x4 (&acc)[5], rsrc_t rw, int mt, int ct, int w, int lane) {
  const int fr = lane & 15, g = lane >> 4, tok = mt * 64 + w * 16 + fr;
  const int odd = ct & 1, pb = (ct >> 1) * 5 + (odd ? 3 : 0), ps = (ct >> 1) * 5 + 2;
  const int hb = WS_H + (((mt >> 1) & 1) * 40 + (g >> 1)) * PLANE + (mt >> 2) * 4096 + ((mt & 1) * 2 + (w >> 1)) * 1024 + ((g & 1) * 32 + (w & 1) * 16 + fr) * 16;
  u32x2 hv[5];
  float s = 0.f;
#pragma unroll
  for (int j = 0; j < 5; ++j) hv[j] = (u32x2){cvtpk(acc[j][0], acc[j][1]), cvtpk(acc[j][2], acc[j][3])};
  const u32x2 a0 = odd ? hv[1] : hv[0], a1 = odd ? hv[2] : hv[1], b0 = odd ? hv[3] : hv[2], b1 = odd ? hv[4] : hv[3], s1 = odd ? hv[0] : hv[4];
  st16<0>(rw, hb + pb * 2 * PLANE, (u32x4){a0[0], a0[1], a1[0], a1[1]});
  st16<0>(rw, hb + (pb + 1) * 2 * PLANE, (u32x4){b0[0], b0[1], b1[0], b1[1]});
  st8<0>(rw, hb + ps * 2 * PLANE + odd * 8, s1);
  f32x4 v[5];
#pragma unroll
  for (int j = 0; j < 5; ++j) { v[j] = (f32x4){bflo(hv[j][0]), bfhi(hv[j][0]), bflo(hv[j][1]), bfhi(hv[j][1])}; s += (v[j][0] + v[j][1]) + (v[j][2] + v[j][3]); }
  const float mean = xsum16_32(s) * (1.f / 80.f);
  float m2 = 0.f;
#pragma unroll
  for (int j = 0; j < 5; ++j) { const f32x4 d = v[j] - mean; m2 += (d[0] * d[0] + d[1] * d[1]) + (d[2] * d[2] + d[3] * d[3]); }
  m2 = xsum16_32(m2);
  if (g == 0) st8<0>(rw, WS_ST + (((ct >> 2) * S_ + tok) * 4 + (ct & 3)) * 8, (u32x2){__float_as_uint(mean), __float_as_uint(m2)});
}

DI void attn_helper(ldsp lds, char* __restrict__ ws, int c, int h, int hw, int lane, const u16* __restrict__ x, const u16* __restrict__ wg, const u16* __restrict__ wu, const u16* __restrict__ wd) {
  const unsigned lds0 = (unsigned)(uintptr_t)lds;
  const int pm = 5 * (h >> 1) + 3 * (h & 1), pt = 5 * (h >> 1) + 2;
  const bool EDGE = c == 0;
  const int tok0 = 256 * c - 256 + 128 * hw + ((EDGE && hw < 2) ? 256 : 0);
  const char* kb = ws + WS_QK + (80 * S_ + tok0) * 16;
  {
    set_m0(lds0 + K2_K + hw * 2048); dma<0>(kb + pm * S_ * 16, lane * 16); dma<1024>(kb + pm * S_ * 16, lane * 16);
    set_m0(lds0 + K2_K + 8192 + hw * 2048); dma<0>(kb + (pm + 1) * S_ * 16, lane * 16); dma<1024>(kb + (pm + 1) * S_ * 16, lane * 16);
    set_m0(lds0 + K2_K + 16384 + hw * 2048); dma<0>(kb + pt * S_ * 16, lane * 16); dma<1024>(kb + pt * S_ * 16, lane * 16);
  }
  const int b = blockIdx.x, xcd = b & 7, idx = b >> 3;
  if (hw == 0 && lane < 8) *(LAS unsigned*)(lds + K2_FL + lane * 4) = 0u;
  {
    const unsigned tm = (EDGE && hw < 2 ? 0xff80u : 0u) | 0xbf800000u;
    *(LAS u32x4*)(lds + K2_V + 3 * K2_VP + hw * 2048 + lane * 16) = (u32x4){tm, 0xbf80bf80u, ONES2, ONES2};
    *(LAS u32x4*)(lds + K2_V + 3 * K2_VP + hw * 2048 + 1024 + lane * 16) = (u32x4){tm, 0xbf80bf80u, ONES2, ONES2};
  }
  if (hw == 1 && lane < 52) {
    const int d = lane - 20;
    u32x4 mk;
#pragma unroll
    for (int e = 0; e < 4; ++e) { const int k0 = ((2 * e) & 3) + 8 * (e >> 1); mk[e] = (k0 > d ? 0xffffu : 0u) | (k0 + 1 > d ? 0xffff0000u : 0u); }
    *(LAS u32x4*)(lds + K2_MSK + lane * 16) = mk;
  }
  wait_vm<3>();
  {
    const char* vb = kb + 80 * S_ * 16;
    set_m0(lds0 + K2_V + hw * 2048); dma<0>(vb + pm * S_ * 16, lane * 16); dma<1024>(vb + pm * S_ * 16, lane * 16);
    set_m0(lds0 + K2_V + K2_VP + hw * 2048); dma<0>(vb + (pm + 1) * S_ * 16, lane * 16); dma<1024>(vb + (pm + 1) * S_ * 16, lane * 16);
    set_m0(lds0 + K2_V + 2 * K2_VP + hw * 2048); dma<0>(vb + pt * S_ * 16, lane * 16); dma<1024>(vb + pt * S_ * 16, lane * 16);
  }
  const rsrc_t rw = mk_rsrc(ws, WS_END);
  const int mt = xcd * 4 + (idx >> 3), ct = idx & 7;
  const int q0 = hw * 25600;
  const int pcg = b % 24, pks = (b / 24) * 4 + hw, v0 = NWD_A + (b - 240) * 8 + hw * 2;
  wait_vm_bar<6>();
  if (b < 240) { pack_issue<1>(wg, wu, pks, pcg, lds0 + q0, lane); wait_vm<4>(); }
  else { pack_issue<2>(wd, wd, v0 / 10, v0 % 10, lds0 + q0, lane); pack_issue<2>(wd, wd, v0 / 10, v0 % 10 + 1, lds0 + q0 + 4096, lane); wait_vm<8>(); }
  if (lane == 0) *(volatile LAS unsigned*)(lds + K2_FL + 16 + hw * 4) = 1u;
  u32x4 xr[2]; u32x2 xs;
  {
    const char* xp = (const char*)x + (size_t)(mt * 64 + hw * 16) * 1280 + ct * 160;
    const int g = lane >> 4;
    const unsigned o16 = (lane & 15) * 1280 + (g & 1) * 32 + (g >> 1) * 16, o8 = (lane & 15) * 1280 + g * 8;
    asm volatile("global_load_dwordx4 %0, %1, %2" : "=v"(xr[0]) : "v"(o16), "s"(xp) : "memory");
    asm volatile("global_load_dwordx4 %0, %1, %2 offset:64" : "=v"(xr[1]) : "v"(o16), "s"(xp) : "memory");
    asm volatile("global_load_dwordx2 %0, %1, %2 offset:128" : "=v"(xs) : "v"(o8), "s"(xp) : "memory");
  }
  dma_long<15>(ws + WS_WO + ct * 102400 + q0 + 10240, lds0 + q0 + 10240, lane * 16);
  wait_vm<15>();
  pin(xr[0]); pin(xr[1]); pin(xs);
  if (b >= 240) {
    pack_finish<2>(lds + q0, rw, v0 / 10, v0 % 10, lane, nullptr); pack_finish<2>(lds + q0 + 4096, rw, v0 / 10, v0 % 10 + 1, lane, nullptr);
    asm volatile("s_waitcnt lgkmcnt(0)" ::: "memory");
  } else {
    LAS float* ct4 = (LAS float*)(lds + K2_CS);
    pack_finish<1>(lds + q0, rw, pks, pcg, lane, ct4 + hw * 128);
    asm volatile("s_waitcnt lgkmcnt(0)" ::: "memory");
    if (lds_add(lds + K2_FL, lane) == 3) {
      asm volatile("" ::: "memory");
      const LAS f32x2* c2 = (const LAS f32x2*)ct4 + lane;
      const f32x2 a = (c2[0] + c2[64]) + (c2[128] + c2[192]);
      const int e = pcg * 128 + lane * 2;
      const int hf = e / 96;
      st8(rw, WS_CS + hf * 4096 + ((b / 24) * 96 + e - hf * 96) * 4, __builtin_bit_cast(u32x2, a));
    }
  }
  dma_long<10>(ws + WS_WO + ct * 102400 + q0, lds0 + q0, lane * 16);
  if (hw == 3) {
    const char *c0, *c1;
    CODE_ADDR(c0, k_mlp); CODE_ADDR(c1, k_text_end);
    const unsigned lim = c1 > c0 ? (unsigned)(c1 - c0) : 0u, off = (idx * 8 + (lane & 7)) * 128;
    set_m0(lds0 + K2_FL + 64);
    asm volatile("global_load_lds_dword %0, %1" ::"v"(off < lim ? off : lim), "s"(c0) : "memory");
  }
  f32x4 acc[5];
#pragma unroll
  for (int jp = 0; jp < 2; ++jp) {
    auto r0 = __builtin_amdgcn_permlane16_swap(xr[jp][0], xr[jp][2], false, false);
    auto r1 = __builtin_amdgcn_permlane16_swap(xr[jp][1], xr[jp][3], false, false);
    acc[2 * jp] = (f32x4){bflo(r0[0]), bfhi(r0[0]), bflo(r1[0]), bfhi(r1[0])};
    acc[2 * jp + 1] = (f32x4){bflo(r0[1]), bfhi(r0[1]), bflo(r1[1]), bfhi(r1[1])};
  }
  acc[4] = (f32x4){bflo(xs[0]), bfhi(xs[0]), bflo(xs[1]), bfhi(xs[1])};
#pragma unroll
  for (int j = 0; j < 5; ++j) pin(acc[j]);
  wait_vm<0>();
  lds_add(lds + K2_FL + 8, lane);
  lds_wait(lds + K2_FL + 8, 4);
  if (hw == 0) {
    lds_wait(lds + K2_FL + 4, 8);
    cnt_wait(ws, (mt * 8 + ct) >> CSH, 32);
    lds_post(lds + K2_FL + 12, 1, lane);
  } else lds_wait(lds + K2_FL + 12, 1);
  bf16x8 af[20], wf[3][5];
  const int ao = WS_QK + (lane >> 4) * PLANE + (mt * 64 + hw * 16 + (lane & 15)) * 16;
#pragma unroll
  for (int ks = 0; ks < 20; ++ks) af[ks] = ld16_sc1(rw, ao, ks * 4 * PLANE);
  SB0();
  ldsp wp = lds + lane * 16;
#pragma unroll
  for (int j = 0; j < 10; ++j) wf[j / 5][j % 5] = lds_ld16(wp + j * 1024);
#pragma unroll
  for (int ks = 0; ks < 20; ++ks) {
#pragma unroll
    for (int j = 0; j < 5; ++j) {
      if (ks + 2 < 20) wf[(ks + 2) % 3][j] = lds_ld16(wp + (ks + 2) * 5120 + j * 1024);
      acc[j] = mfma16(wf[ks % 3][j], af[ks], acc[j]);
    }
    SB0();
  }
  n80_epilogue_h(acc, rw, mt, ct, hw, lane);
}

template <bool EXACT> DI f32x16 attn_rows(ldsp lds, ldsp kp, ldsp ktp, ldsp vp, bf16x8 qm, u32x2 qtr, int dq, int hi) {
  const float NEG = -__builtin_inff();
  auto mx3 = [](float a, float b, float c) { return __builtin_elementwise_maximum(__builtin_elementwise_maximum(a, b), c); };
  auto qk = [&](int j, u32x2 q2, f32x16 cin) {
    return __builtin_amdgcn_mfma_f32_32x32x8bf16_1k(*(const LAS bf16x4*)(ktp + j * 512), __builtin_bit_cast(bf16x4, q2), mfma32(lds_ld16(kp + j * 512), qm, cin), 0, 0, 0);
  };
  f32x16 s0, s8;
#pragma unroll
  for (int r = 0; r < 16; ++r) { const bool le = (r & 3) + 8 * (r >> 2) <= dq; s8[r] = le ? 0.f : NEG; s0[r] = le ? NEG : 0.f; }
  if (!EXACT) {
    pin(s0); pin(s8); bar();
  }
  bf16x8 km[2]; bf16x4 kt[2];
  s8 = qk(8, qtr, s8); s0 = qk(0, qtr, s0);
  km[1] = lds_ld16(kp + 512); kt[1] = *(const LAS bf16x4*)(ktp + 512);
  km[0] = lds_ld16(kp + 1024); kt[0] = *(const LAS bf16x4*)(ktp + 1024);
  f32x16 sa = mfma32(km[1], qm, (f32x16){});
#pragma unroll
  for (int r = 0; r < 16; ++r) s0[r] = __builtin_elementwise_maximum(s0[r], s8[r]);
  float m = mx3(mx3(mx3(s0[0], s0[1], s0[2]), mx3(s0[3], s0[4], s0[5]), mx3(s0[6], s0[7], s0[8])), mx3(mx3(s0[9], s0[10], s0[11]), s0[12], s0[13]), __builtin_elementwise_maximum(s0[14], s0[15]));
  if (EXACT) {
#pragma unroll 1
    for (int j = 1; j < 8; ++j) {
      const f32x16 t = qk(j, qtr, (f32x16){});
#pragma unroll
      for (int r = 0; r < 16; r += 2) m = mx3(m, t[r], t[r + 1]);
    }
  }
  {
    auto rr = __builtin_amdgcn_permlane32_swap(__float_as_uint(m), __float_as_uint(m), false, false);
    m = __builtin_elementwise_maximum(__uint_as_float(rr[0]), __uint_as_float(rr[1]));
  }
  u32x2 q2 = qtr;
  {
    const unsigned u0 = __float_as_uint(m) & 0xffff0000u;
    const float r1 = m - __uint_as_float(u0);
    const unsigned u1 = __float_as_uint(r1) & 0xffff0000u;
    const float r2 = r1 - __uint_as_float(u1);
    if (hi) q2 = (u32x2){0x3f80u | u0, (u1 >> 16) | (__float_as_uint(r2) & 0xffff0000u)};
  }
  const bf16x4 qs = __builtin_bit_cast(bf16x4, q2);
  s0 = __builtin_amdgcn_mfma_f32_32x32x8bf16_1k(__builtin_bit_cast(bf16x4, hi ? (u32x2){0xbf800000u, 0xbf80bf80u} : (u32x2){0u, 0u}), qs, s0, 0, 0, 0);
  sa = __builtin_amdgcn_mfma_f32_32x32x8bf16_1k(kt[1], qs, sa, 0, 0, 0);
  f32x16 sb = __builtin_amdgcn_mfma_f32_32x32x8bf16_1k(kt[0], qs, mfma32(km[0], qm, (f32x16){}), 0, 0, 0);
  SB0();
  auto ex8 = [&](const f32x16& s, int ks) {
    u32x4 p;
#pragma unroll
    for (int e = 0; e < 4; ++e) p[e] = cvtpk(__builtin_amdgcn_exp2f(s[8 * ks + 2 * e]), __builtin_amdgcn_exp2f(s[8 * ks + 2 * e + 1]));
    return p;
  };
  auto vld = [&](int slot, int ks) { return lds_tr2(vp + slot * 512 + ks * 256, vp + slot * 512 + ks * 256 + 128); };
  f32x16 o = {};
  auto pv = [&](bf16x8 v, u32x4 p) { o = mfma32(v, __builtin_bit_cast(bf16x8, p), o); };
  u32x4 mk0, mk1;
  u32x4 pk[8][2];
  pk[0][0] = ex8(s0, 0); pk[0][1] = ex8(s0, 1);
  km[1] = lds_ld16(kp + 3 * 512); kt[1] = *(const LAS bf16x4*)(ktp + 3 * 512);
  pk[1][0] = ex8(sa, 0); pk[1][1] = ex8(sa, 1);
  SB0();
  bf16x8 vf[2][4];
  u32x4 vrdy;
  sfor<2, 8>([&](auto jc) {
    constexpr int j = decltype(jc)::value;
    f32x16& cur = (j & 1) ? sa : sb;
    f32x16& nxt = (j & 1) ? sb : sa;
    constexpr int ta = j == 4 ? 0 : 2 * j - 9, tb = j == 4 ? 8 : 2 * j - 8;
    constexpr int na = 2 * j - 7, nb = j < 7 ? 2 * j - 6 : 7;
    if constexpr (j == 3) vrdy = *(volatile LAS u32x4*)(lds + K2_FL + 16);
    if constexpr (j >= 4) { vf[(j + 1) & 1][0] = vld(na, 0); vf[(j + 1) & 1][1] = vld(na, 1); if constexpr (j < 7) { vf[(j + 1) & 1][2] = vld(nb, 0); vf[(j + 1) & 1][3] = vld(nb, 1); } }
    if constexpr (j + 2 < 8) { km[j & 1] = lds_ld16(kp + (j + 2) * 512); kt[j & 1] = *(const LAS bf16x4*)(ktp + (j + 2) * 512); }
    if constexpr (j + 1 < 8) nxt = mfma32(km[(j + 1) & 1], qm, (f32x16){});
    SB0();
    pk[j][0] = ex8(cur, 0);
    if constexpr (j == 4) { const u32x4 a = pk[0][0] & mk0; pv(vf[0][0], a); pv(vf[0][2], pk[0][0] ^ a); }
    else if constexpr (j > 4) { pv(vf[j & 1][0], pk[ta][0]); pv(vf[j & 1][1], pk[ta][1]); }
    if constexpr (j + 1 < 8) nxt = __builtin_amdgcn_mfma_f32_32x32x8bf16_1k(kt[(j + 1) & 1], qs, nxt, 0, 0, 0);
    SB0();
    pk[j][1] = ex8(cur, 1);
    if constexpr (j == 4) { const u32x4 a = pk[0][1] & mk1; pv(vf[0][1], a); pv(vf[0][3], pk[0][1] ^ a); }
    else if constexpr (j > 4) { pv(vf[j & 1][2], pk[tb][0]); pv(vf[j & 1][3], pk[tb][1]); }
    if constexpr (j == 3) {
#pragma unroll
      for (int t = 0; t < 4; ++t) { pin(pk[t][0]); pin(pk[t][1]); }
      if (!EXACT && __builtin_expect(__builtin_amdgcn_readfirstlane((vrdy[0] & vrdy[1]) & (vrdy[2] & vrdy[3])) == 0, 0)) lds_wait4(lds + K2_FL + 16, 1);
      asm volatile("" ::: "memory");
      vf[0][0] = vld(0, 0); vf[0][1] = vld(0, 1); vf[0][2] = vld(8, 0); vf[0][3] = vld(8, 1);
      mk0 = *(const LAS u32x4*)(lds + K2_MSK + (dq + 20) * 16); mk1 = *(const LAS u32x4*)(lds + K2_MSK + (dq + 4) * 16);
    }
    SB0();
  });
  pv(vf[0][0], pk[7][0]); pv(vf[0][1], pk[7][1]);
  return o;
}

DI void attn_body(ldsp lds, char* __restrict__ ws, int c, int h, int w, int lane) {
  const int r32 = lane & 31, hi = lane >> 5;
  const int pm = 5 * (h >> 1) + 3 * (h & 1), pt = 5 * (h >> 1) + 2;
  const char* qb = ws + WS_QK + (256 * c + 32 * w) * 16;
  const bf16x8 qm = *(const bf16x8*)(qb + ((pm + hi) * S_ + r32) * 16);
  u32x2 qtr = *(const u32x2*)(qb + (pt * S_ + r32) * 16 + (h & 1) * 8);
  if (hi) qtr = (u32x2){0x3f80u, 0u};
  ldsp kp = lds + K2_K + hi * 8192 + (w * 32 + r32) * 16;
  ldsp ktp = lds + (hi ? K2_V + 3 * K2_VP + w * 512 : K2_K + 16384 + (w * 32 + r32) * 16 + (h & 1) * 8);
  const int vq = (lane & 15) >> 2, vpp = lane & 3, vblk = (lane >> 4) & 1;
  ldsp vp = lds + K2_V + (w * 32 + 4 * hi + vq) * 16 + (vblk == 0 ? (vpp >> 1) * K2_VP + (vpp & 1) * 8 : vpp == 0 ? 2 * K2_VP + (h & 1) * 8 : 3 * K2_VP + 8);
  f32x16 o = attn_rows<false>(lds, kp, ktp, vp, qm, qtr, r32 - 4 * hi, hi);
  float l = __uint_as_float(__builtin_amdgcn_permlane32_swap(__float_as_uint(o[8]), __float_as_uint(o[8]), false, false)[1]);
  if (__builtin_expect(__builtin_amdgcn_ballot_w64(!(l < LAZY_LIM)) != 0, 0)) {
    int dq2 = r32 - 4 * hi, hi2 = hi;
    pin(dq2); pin(hi2);
    o = attn_rows<true>(lds, kp, ktp, vp, qm, qtr, dq2, hi2);
    l = __uint_as_float(__builtin_amdgcn_permlane32_swap(__float_as_uint(o[8]), __float_as_uint(o[8]), false, false)[1]);
  }
  __builtin_amdgcn_s_setprio(3);
  const float il = __builtin_amdgcn_rcpf(l);
  const int tok = 256 * c + 32 * w + r32;
  const rsrc_t rw = mk_rsrc(ws, WS_END);
  const u32x4 w0 = {cvtpk(o[0] * il, o[1] * il), cvtpk(o[2] * il, o[3] * il), cvtpk(o[4] * il, o[5] * il), cvtpk(o[6] * il, o[7] * il)};
  st16(rw, WS_QK + (pm + hi) * PLANE + tok * 16, w0);
  if (hi == 0) st8(rw, WS_QK + pt * PLANE + tok * 16 + (h & 1) * 8, (u32x2){cvtpk(o[8] * il, o[9] * il), cvtpk(o[10] * il, o[11] * il)});
  wait_vm<0>();
  if (lds_add(lds + K2_FL + 4, lane) == 7 && lane < (32 >> CSH)) cnt_add(ws, (c * 32 >> CSH) + lane);
}

extern "C" __global__ __launch_bounds__(768, 3) void k_attn(char* __restrict__ ws, const u16* __restrict__ x, const u16* __restrict__ wg, const u16* __restrict__ wu, const u16* __restrict__ wd) {
  extern __shared__ __attribute__((aligned(16))) char smem[];
  const int lane = threadIdx.x & 63, w = __builtin_amdgcn_readfirstlane(threadIdx.x >> 6);
  const int xcd = blockIdx.x & 7, idx = blockIdx.x >> 3, h = xcd * 4 + (idx >> 3), c = idx & 7;
  if (w < 4) attn_helper((ldsp)smem, ws, c, h, w, lane, x, wg, wu, wd);
  else attn_body((ldsp)smem, ws, c, h, w - 4, lane);
}

constexpr int K4_NS = 4, K4_STG = 20480, K4_NT = 20, K4_RING = 61440, K4_CTL = K4_RING + K4_NS * K4_STG, K4_SD = K4_CTL + 4096, K4_LDS = K4_SD + 8192 + 8192;
static_assert(K4_LDS <= 163840, "LDS map");
constexpr float GC0 = -2.f * 0.7978845608028654f * 1.4426950408889634f, GC1 = GC0 * 0.044715f;

extern "C" __global__ __launch_bounds__(512, 2) void k_mlp(char* __restrict__ ws, u16* __restrict__ out) {
  extern __shared__ __attribute__((aligned(16))) char smem[];
  ldsp lds = (ldsp)smem + K4_RING;
  ldsp ctl = (ldsp)smem + K4_CTL;
  const int lane = threadIdx.x & 63, w = __builtin_amdgcn_readfirstlane(threadIdx.x >> 6);
  const unsigned lds00 = (unsigned)(uintptr_t)(ldsp)smem, lds0 = lds00 + K4_RING;
  const int xcd = blockIdx.x & 7, idx = blockIdx.x >> 3, mt = (xcd >> 2) * 8 + (idx >> 2), ft = (xcd & 3) * 4 + (idx & 3);
  const int mt5 = (xcd >> 1) * 8 + (idx >> 2), ct = (xcd & 1) * 4 + (idx & 3);
  const int odd = ct & 1, pb = (ct >> 1) * 5 + (odd ? 3 : 0), ps = (ct >> 1) * 5 + 2;
  if (w >= 4) {
    const int lw = w - 4;
    const char* wd = ws + WS_WD + ct * 245760;
    ldsp sd = (ldsp)smem + K4_SD + lw * 4096;
    if (w < 6) {
      const char* src = ws + WS_H + ((mt & 1) * 40 + lw) * PLANE + (mt >> 1) * 4096;
      const char* st = ws + WS_ST + mt * 4096 + lw * 2048;
      loader_loop<4, K4_NT, K4_NS, 4>([&](int slot) { dma_run<4>(src, lds0 + lw * 4096 + slot * K4_STG, lane * 16); src += 2 * PLANE; },
                                      [&] { dma_run<2>(st, lds00 + K4_SD + lw * 4096, lane * 16); dma_run<2>(st + S_ * 32, lds00 + K4_SD + lw * 4096 + 2048, lane * 16); });
      wait_vm<0>();
      const LAS f32x4* sp = (const LAS f32x4*)(sd + lane * 32);
      const f32x4 v[4] = {sp[0], sp[1], sp[128], sp[129]};
      const float mu = (((v[0][0] + v[0][2]) + (v[1][0] + v[1][2])) + ((v[2][0] + v[2][2]) + (v[3][0] + v[3][2]))) * 0.125f;
      float m2 = 0.f;
#pragma unroll
      for (int e = 0; e < 4; ++e) { const float d0 = v[e][0] - mu, d1 = v[e][2] - mu; m2 += (v[e][1] + v[e][3]) + 80.f * (d0 * d0 + d1 * d1); }
      const float var = m2 * (1.f / D_) + LN_EPS, rstd = __builtin_amdgcn_rsqf(var);
      ((LAS f32x4*)(ctl + 2048))[lw * 64 + lane] = (f32x4){-mu, GC0 * rstd, GC1 * rstd * rstd * rstd, var};
    } else {
      const char* src = ws + WS_WGU + ft * K4_NT * 12288 + (w - 6) * 6144;
      loader_loop<6, K4_NT, K4_NS, 4>([&](int slot) { dma_run<6>(src, lds0 + 8192 + (w - 6) * 6144 + slot * K4_STG, lane * 16); src += 12288; },
                                      [&] { dma_run<4>(ws + WS_CS + ft * 8192 + (w - 6) * 4096, lds00 + K4_SD + lw * 4096, lane * 16); });
      wait_vm<0>();
      const int e = lane < 48 ? lane : 47;
      const LAS f32x2* cp = (const LAS f32x2*)sd + e;
      ((LAS f32x2*)(ctl + 1024))[(w - 6) * 48 + e] = (((cp[0] + cp[48]) + (cp[96] + cp[144])) + ((cp[192] + cp[240]) + (cp[288] + cp[336]))) + (cp[384] + cp[432]);
    }
    asm volatile("s_waitcnt lgkmcnt(0)" ::: "memory");
    lds_post(ctl + 48 + lw * 4, 1, lane);
    dma_long<15>(wd + lw * 15360, lds00 + lw * 15360, lane * 16);
    {
      set_m0(lds00 + K4_SD + lw * 4096);
      const char* hs = ws + WS_H + ((mt5 >> 1) & 1) * 40 * PLANE + (mt5 >> 2) * 4096 + ((mt5 & 1) * 2 + (lw >> 1)) * 1024;
      const unsigned vo = (lane >> 5) * PLANE + (((lane >> 4) & 1) * 32 + (lw & 1) * 16 + (lane & 15)) * 16 + 2048;
      dma<0>(hs + pb * 2 * PLANE - 2048, vo); dma<1024>(hs + (pb + 1) * 2 * PLANE - 3072, vo); dma<2048>(hs + ps * 2 * PLANE - 4096, vo);
    }
    lds_wait(ctl, 4);
    dma_long<20>(wd + 61440 + lw * 20480, lds0 + lw * 20480, lane * 16);
    wait_vm_bar<0>();
#pragma unroll 1
    for (int g = 0; g < 5; ++g) {
      lds_wait4(ctl + 16, g + 1);
      dma_run<5>(wd + 143360 + g * 20480 + lw * 5120, lds00 + g * 20480 + lw * 5120, lane * 16);
      wait_vm<0>();
      lds_post(ctl + 32 + lw * 4, g + 1, lane);
    }
    return;
  }
  if (w == 0 && lane < 16) ((LAS unsigned*)ctl)[lane] = 0u;
  const int r32 = lane & 31, hi = lane >> 5, wm = w >> 1, wn = w & 1;
  f32x16 acc[2][3];
#pragma unroll
  for (int i = 0; i < 2; ++i)
#pragma unroll
    for (int j = 0; j < 3; ++j) { acc[i][j] = (f32x16){}; pin(acc[i][j]); }
  bf16x8 af[2][2], wf[2][3];
  const int aoff = wm * 2048 + lane * 16, woff = 8192 + wn * 3072 + lane * 16;
  auto rda = [&](ldsp sb, int s, int i) { af[s][i] = lds_ld16(sb + aoff + s * 4096 + i * 1024); };
  auto rdw = [&](ldsp sb, int s, int j) { wf[s][j] = lds_ld16(sb + woff + s * 6144 + j * 1024); };
  auto mm = [&](int s, int i, int j) { acc[i][j] = mfma32(wf[s][j], af[s][i], acc[i][j]); };
  bar();
  const int tok = mt * 128 + wm * 64 + r32;
  rda(lds, 0, 0); rdw(lds, 0, 0); rda(lds, 0, 1); rdw(lds, 0, 1); rdw(lds, 0, 2); SB0();
  f32x4 rc[2], cq[3][4];
  const LAS float* csb = (const LAS float*)(ctl + 1024) + wn * 96 + 4 * hi;
  sfor<0, K4_NT>([&](auto tc) {
    constexpr int t = decltype(tc)::value;
    constexpr bool more = t + 1 < K4_NT;
    ldsp sb = lds + (t % K4_NS) * K4_STG;
    ldsp nb = lds + ((t + 1) % K4_NS) * K4_STG;
    mm(0, 0, 0); rda(sb, 1, 0); rdw(sb, 1, 0); SB0();
    mm(0, 1, 0); rda(sb, 1, 1); rdw(sb, 1, 1); SB0();
    mm(0, 0, 1); rdw(sb, 1, 2); SB0();
    mm(0, 1, 1); SB0();
    mm(0, 0, 2); SB0();
    mm(0, 1, 2); SB0();
    if constexpr (more) { bar(); SB0(); }
    if constexpr (!more) { asm volatile("s_waitcnt lgkmcnt(0)" ::: "memory"); lds_add(ctl, lane); SB0(); }
    mm(1, 0, 0); if (more) { rda(nb, 0, 0); rdw(nb, 0, 0); } SB0();
    mm(1, 1, 0); if (more) { rda(nb, 0, 1); rdw(nb, 0, 1); } SB0();
    mm(1, 0, 1); if (more) rdw(nb, 0, 2); SB0();
    if constexpr (!more) {
      lds_wait4(ctl + 48, 1);
      rc[0] = ((const LAS f32x4*)(ctl + 2048))[wm * 64 + r32]; rc[1] = ((const LAS f32x4*)(ctl + 2048))[wm * 64 + 32 + r32];
#pragma unroll
      for (int j = 0; j < 3; ++j)
#pragma unroll
        for (int b = 0; b < 4; ++b) cq[j][b] = *(const LAS f32x4*)(csb + j * 32 + 8 * b);
    }
    mm(1, 1, 1); SB0();
    mm(1, 0, 2); SB0();
    mm(1, 1, 2); SB0();
  });
  const rsrc_t rw = mk_rsrc(ws, WS_END);
#pragma unroll
  for (int i = 0; i < 2; ++i) {
    const int tk = tok + 32 * i;
    const int ob = WS_ACT + (((tk >> 6) * 48 + ft * 3) * 4 + ((tk >> 4) & 3)) * 1024 + (hi * 16 + (tk & 15)) * 16;
#pragma unroll
    for (int j = 0; j < 3; ++j) {
      unsigned pk[4];
#pragma unroll
      for (int r = 0; r < 8; r += 2) {
        const f32x2 gv = (f32x2){acc[i][j][r], acc[i][j][r + 1]} + rc[i][0] * (f32x2){cq[j][r >> 2][r & 3], cq[j][r >> 2][(r & 3) + 1]};
        const f32x2 uv = (f32x2){acc[i][j][8 + r], acc[i][j][9 + r]} + rc[i][0] * (f32x2){cq[j][2 + (r >> 2)][r & 3], cq[j][2 + (r >> 2)][(r & 3) + 1]};
        const f32x2 arg = gv * (gv * gv * rc[i][2] + rc[i][1]);
        const f32x2 ex = {__builtin_amdgcn_exp2f(arg[0]), __builtin_amdgcn_exp2f(arg[1])};
        const f32x2 den = ex * rc[i][3] + rc[i][3];
        const f32x2 ri = {__builtin_amdgcn_rcpf(den[0]), __builtin_amdgcn_rcpf(den[1])};
        const f32x2 a = gv * uv * ri;
        pk[r >> 1] = cvtpk(a[0], a[1]);
      }
      const int T = wn * 3 + j;
      st16(rw, ob + (T >> 1) * 4096 + (T & 1) * 512, (u32x4){pk[0], pk[1], pk[2], pk[3]});
    }
  }
  wait_vm<0>();
  if (lds_add(ctl + 4 + wm * 4, lane) == 1 && lane < (8 >> CSH)) cnt_add(ws, 256 + ((mt * 2 + wm) * 8 >> CSH) + lane);
  if (w == 0) { cnt_wait(ws, 256 + ((mt5 * 8 + ct) >> CSH), 16); lds_post(ctl + 12, 1, lane); }
  else lds_wait(ctl + 12, 1);
  constexpr int WIN = 10, REL0 = 2, REL1 = 6, REL2 = 10, REL3 = 13, REL4 = 16;
  bf16x8 a5[WIN], w5[3][5];
  const int ao = WS_ACT + mt5 * 196608 + w * 1024 + lane * 16;
#pragma unroll
  for (int ks = 0; ks < WIN; ++ks) a5[ks] = ld16_sc1(rw, ao, ks * 4096);
  SB0();
  bar();
  f32x4 ac5[5];
  {
    ldsp rp = (ldsp)smem + K4_SD + w * 4096 + lane * 16;
#pragma unroll
    for (int j = 0; j < 5; ++j) {
      const int e = odd ? (j == 0 ? 5 : j - 1) : j;
      const u32x2 r = *(const LAS u32x2*)(rp + (e >> 1) * 1024 + (e & 1) * 8);
      ac5[j] = (f32x4){bflo(r[0]), bfhi(r[0]), bflo(r[1]), bfhi(r[1])};
    }
  }
  ldsp wp = (ldsp)smem + lane * 16;
#pragma unroll
  for (int j = 0; j < 10; ++j) w5[j / 5][j % 5] = lds_ld16(wp + j * 1024);
  sfor<0, 48>([&](auto kc) {
    constexpr int ks = decltype(kc)::value, kn = ks + 2, tn = kn / 2;
    if constexpr ((kn & 3) == 0 && kn >= 28 && kn < 48) { lds_wait4(ctl + 32, (kn - 28) / 4 + 1); SB0(); }
    if constexpr ((kn & 1) == 0 && (tn == REL0 || tn == REL1 || tn == REL2 || tn == REL3 || tn == REL4)) {
      asm volatile("" ::: "memory");
      lds_post(ctl + 16 + w * 4, tn == REL0 ? 1 : tn == REL1 ? 2 : tn == REL2 ? 3 : tn == REL3 ? 4 : 5, lane);
    }
#pragma unroll
    for (int j = 0; j < 5; ++j) {
      if constexpr (kn < 48) w5[kn % 3][j] = lds_ld16(wp + (kn % 28) * 5120 + j * 1024);
      ac5[j] = mfma16(w5[ks % 3][j], a5[ks % WIN], ac5[j]);
    }
    if constexpr (ks + WIN < 48) a5[ks % WIN] = ld16_sc1(rw, ao, (ks + WIN) * 4096);
    SB0();
  });
  {
    const int g = lane >> 4;
    u32x2 hv[5];
#pragma unroll
    for (int j = 0; j < 5; ++j) hv[j] = (u32x2){cvtpk(ac5[j][0], ac5[j][1]), cvtpk(ac5[j][2], ac5[j][3])};
    const rsrc_t ro = mk_rsrc(out, S_ * D_ * 2);
    const int oo = (mt5 * 64 + w * 16 + (lane & 15)) * 1280 + ct * 160;
#pragma unroll
    for (int jp = 0; jp < 2; ++jp) {
      auto r0 = __builtin_amdgcn_permlane16_swap(hv[2 * jp][0], hv[2 * jp + 1][0], false, false);
      auto r1 = __builtin_amdgcn_permlane16_swap(hv[2 * jp][1], hv[2 * jp + 1][1], false, false);
      st16<0>(ro, oo + jp * 64 + (g & 1) * 32 + (g >> 1) * 16, (u32x4){r0[0], r1[0], r0[1], r1[1]});
    }
    st8<0>(ro, oo + 128 + g * 8, hv[4]);
  }
}

extern "C" __global__ void k_text_end() {}

extern "C" void kernel_launch(void* const* d_in, const int* in_sizes, int n_in, void* d_out, int out_size, void* d_ws, size_t ws_size,
                              hipStream_t stream) {
  const u16* x = (const u16*)d_in[0];
  const u16* wqkv = (const u16*)d_in[1];
  const u16* wo = (const u16*)d_in[2];
  const u16* wg = (const u16*)d_in[4];
  const u16* wu = (const u16*)d_in[5];
  const u16* wd = (const u16*)d_in[6];
  char* ws = (char*)d_ws;
  u16* out = (u16*)d_out;
  (void)hipFuncSetAttribute((const void*)k_qkv, hipFuncAttributeMaxDynamicSharedMemorySize, K1_LDS);
  (void)hipFuncSetAttribute((const void*)k_attn, hipFuncAttributeMaxDynamicSharedMemorySize, K2_LDS);
  (void)hipFuncSetAttribute((const void*)k_mlp, hipFuncAttributeMaxDynamicSharedMemorySize, K4_LDS);
  k_qkv<<<256, 768, K1_LDS, stream>>>(x, wqkv, wo, wd, ws);
  k_attn<<<256, 768, K2_LDS, stream>>>(ws, x, wg, wu, wd);
  k_mlp<<<256, 512, K4_LDS, stream>>>(ws, out);
}
```

```cpp
#include <hip/hip_runtime.h>
typedef unsigned short u16;
__device__ __forceinline__ float bf(u16 b) { return __uint_as_float((unsigned)b << 16); }
__device__ __forceinline__ u16 tobf(float f) { unsigned u = __float_as_uint(f); u += 0x7fff + ((u >> 16) & 1); return u >> 16; }
#define NODES 1
__global__ void k_nop() {}
__global__ __launch_bounds__(1024) void k_one(const u16* x, const u16* wqkv, const u16* wo, const u16* wg, const u16* wu, const u16* wd, float* ws, u16* out) {
  const unsigned long long t0 = __builtin_amdgcn_s_memrealtime();
  const int b = blockIdx.x, tid = threadIdx.x, r0 = b * 8, lo = r0 - 255 < 0 ? 0 : r0 - 255, nr = r0 + 8 - lo;
  u16* kv = (u16*)ws + (size_t)b * (264 * 1280);
  __shared__ float xn[8][640], q[8][640], o[8][640], h[8][640], act[8][1536], st[8][2];
  for (int g0 = 0; g0 < nr; g0 += 8) {
    const int base = r0 + 8 - ((nr - g0 + 7) / 8) * 8;
    __syncthreads();
    if (tid < 8) { int r = base + tid; float s = 0, v = 0; if (r >= 0) { for (int k = 0; k < 640; ++k) s += bf(x[r * 640 + k]); s /= 640; for (int k = 0; k < 640; ++k) { float d = bf(x[r * 640 + k]) - s; v += d * d; } } st[tid][0] = s; st[tid][1] = rsqrtf(v / 640 + 1e-5f); }
    __syncthreads();
    for (int i = tid; i < 8 * 640; i += 1024) { int rr = i / 640, k = i % 640, r = base + rr; xn[rr][k] = r >= 0 ? (bf(x[r * 640 + k]) - st[rr][0]) * st[rr][1] : 0.f; }
    __syncthreads();
    const bool own = base == r0;
    for (int n = (own ? 0 : 640) + tid; n < 1920; n += 1024) {
      float a[8] = {0, 0, 0, 0, 0, 0, 0, 0};
      for (int k = 0; k < 640; ++k) { float w = bf(wqkv[k * 1920 + n]);
#pragma unroll
        for (int rr = 0; rr < 8; ++rr) a[rr] += xn[rr][k] * w; }
#pragma unroll
      for (int rr = 0; rr < 8; ++rr) { if (n < 640) q[rr][n] = a[rr]; else kv[(size_t)(base + rr - (r0 - 256)) * 1280 + n - 640] = tobf(a[rr]); }
    }
  }
  __syncthreads();
  if (tid < 256) {
    const int rr = tid >> 5, hh = tid & 31, r = r0 + rr, l0 = r - 255 < 0 ? 0 : r - 255;
    float mx = -1e30f;
    for (int k = l0; k <= r; ++k) { const u16* kp = kv + (size_t)(k - (r0 - 256)) * 1280 + hh * 20; float s = 0; for (int d = 0; d < 20; ++d) s += q[rr][hh * 20 + d] * bf(kp[d]); mx = fmaxf(mx, s); }
    float l = 0; for (int d = 0; d < 20; ++d) o[rr][hh * 20 + d] = 0;
    for (int k = l0; k <= r; ++k) { const u16* kp = kv + (size_t)(k - (r0 - 256)) * 1280 + hh * 20; float s = 0; for (int d = 0; d < 20; ++d) s += q[rr][hh * 20 + d] * bf(kp[d]);
      float p = __expf((s - mx) * 0.22360679774997896f); l += p; for (int d = 0; d < 20; ++d) o[rr][hh * 20 + d] += p * bf(kp[640 + d]); }
    for (int d = 0; d < 20; ++d) o[rr][hh * 20 + d] /= l;
  }
  __syncthreads();
  for (int i = tid; i < 8 * 640; i += 1024) { int rr = i / 640, n = i % 640; float a = bf(x[(r0 + rr) * 640 + n]); for (int k = 0; k < 640; ++k) a += o[rr][k] * bf(wo[k * 640 + n]); h[rr][n] = a; }
  __syncthreads();
  if (tid < 8) { float s = 0, v = 0; for (int k = 0; k < 640; ++k) s += h[tid][k]; s /= 640; for (int k = 0; k < 640; ++k) { float d = h[tid][k] - s; v += d * d; } st[tid][0] = s; st[tid][1] = rsqrtf(v / 640 + 1e-5f); }
  __syncthreads();
  for (int i = tid; i < 8 * 640; i += 1024) { int rr = i / 640, k = i % 640; xn[rr][k] = (h[rr][k] - st[rr][0]) * st[rr][1]; }
  __syncthreads();
  for (int i = tid; i < 8 * 1536; i += 1024) { int rr = i / 1536, n = i % 1536; float g = 0, u = 0; for (int k = 0; k < 640; ++k) { g += xn[rr][k] * bf(wg[k * 1536 + n]); u += xn[rr][k] * bf(wu[k * 1536 + n]); }
    act[rr][n] = 0.5f * g * (1.f + tanhf(0.7978845608028654f * (g + 0.044715f * g * g * g))) * u; }
  __syncthreads();
  for (int i = tid; i < 8 * 640; i += 1024) { int rr = i / 640, n = i % 640; float a = h[rr][n]; for (int k = 0; k < 1536; ++k) a += act[rr][k] * bf(wd[k * 640 + n]); out[(r0 + rr) * 640 + n] = tobf(a); }
  if (b == 0 && tid < 64) { for (int i = 0; i < 100000000; ++i) { if (__builtin_amdgcn_s_memrealtime() >= t0 + 800000) break; __builtin_amdgcn_s_sleep(1); } }
}
extern "C" void kernel_launch(void* const* d_in, const int* in_sizes, int n_in, void* d_out, int out_size, void* d_ws, size_t ws_size, hipStream_t stream) {
  k_one<<<256, 1024, 0, stream>>>((const u16*)d_in[0], (const u16*)d_in[1], (const u16*)d_in[2], (const u16*)d_in[4], (const u16*)d_in[5], (const u16*)d_in[6], (float*)d_ws, (u16*)d_out);
  for (int i = 1; i < NODES; ++i) k_nop<<<256, 64, 0, stream>>>();
}
```

```cpp
#define APRIO2 3
#define PA 0
#define PB 8
#define PN 4
#include <hip/hip_runtime.h>
#include <stdint.h>
#include <type_traits>

#define LAS __attribute__((address_space(3)))
#define DI __device__ __forceinline__
typedef unsigned short u16;
typedef short bf16x8 __attribute__((ext_vector_type(8)));
typedef short bf16x4 __attribute__((ext_vector_type(4)));
typedef __bf16 bf16x2_t __attribute__((ext_vector_type(2)));
typedef float f32x2 __attribute__((ext_vector_type(2)));
typedef float f32x4 __attribute__((ext_vector_type(4)));
typedef float f32x16 __attribute__((ext_vector_type(16)));
typedef unsigned u32x4 __attribute__((ext_vector_type(4)));
typedef unsigned u32x2 __attribute__((ext_vector_type(2)));
typedef LAS char* ldsp;
typedef __amdgpu_buffer_rsrc_t rsrc_t;

constexpr int S_ = 2048, D_ = 640, NH = 32, DF = 1536;
constexpr float LN_EPS = 1e-5f;
constexpr float QSCALE = 0.22360679774997896f * 1.4426950408889634f;
constexpr unsigned ONES2 = 0x3f803f80u;
#define AUX_ST 16

constexpr int PLANE = S_ * 16;
constexpr int WS_QK = 0;
constexpr int WS_ACT = WS_QK + 80 * PLANE;
constexpr int WS_H = WS_ACT + S_ * DF * 2;
constexpr int WS_WO = WS_H + S_ * D_ * 2;
constexpr int WS_ST = WS_WO + D_ * D_ * 2;
constexpr int WS_CS = WS_ST + S_ * 8 * 8;
constexpr int WS_WGU = WS_CS + 16 * 8192;
constexpr int WS_WD = WS_WGU + D_ * DF * 4;
static_assert(WS_WO >= 0 && WS_QK % 4096 == 0, "workspace map");
constexpr int WS_CNT = WS_WD + D_ * DF * 2;
#define CSTR 64
constexpr int WS_END = WS_CNT + 512 * CSTR * 4;

#define TS_DECL
#define TS()
#define TS_END(kid)

template <int I, int N, class F> DI void sfor(F&& f) {
  if constexpr (I < N) { f(std::integral_constant<int, I>{}); sfor<I + 1, N>(f); }
}
#define SB0() __builtin_amdgcn_sched_barrier(0)
DI unsigned cvtpk(float lo, float hi) { f32x2 v = {lo, hi}; return __builtin_bit_cast(unsigned, __builtin_convertvector(v, bf16x2_t)); }
DI float bflo(unsigned u) { return __uint_as_float(u << 16); }
DI float bfhi(unsigned u) { return __uint_as_float(u & 0xffff0000u); }
DI float dot2(unsigned a, unsigned b, float c) {
  return __builtin_amdgcn_fdot2_f32_bf16(__builtin_bit_cast(bf16x2_t, a), __builtin_bit_cast(bf16x2_t, b), c, false);
}
DI f32x4 mfma16(bf16x8 a, bf16x8 b, f32x4 c) { return __builtin_amdgcn_mfma_f32_16x16x32_bf16(a, b, c, 0, 0, 0); }
DI f32x16 mfma32(bf16x8 a, bf16x8 b, f32x16 c) { return __builtin_amdgcn_mfma_f32_32x32x16_bf16(a, b, c, 0, 0, 0); }
DI void set_m0(unsigned v) { asm volatile("s_mov_b32 m0, %0\n\ts_nop 0" ::"s"(v) : "memory"); }
template <int OFF> DI void dma(const void* sbase, unsigned voff) {
  asm volatile("global_load_lds_dwordx4 %0, %1 offset:%2" ::"v"(voff), "s"(sbase), "n"(OFF) : "memory");
}
template <int OFF> DI void dma_nt(const void* sbase, unsigned voff) {
  asm volatile("global_load_lds_dwordx4 %0, %1 offset:%2 nt" ::"v"(voff), "s"(sbase), "n"(OFF) : "memory");
}
template <int OFF> DI void gld16(u32x4& d, const void* sbase, unsigned voff) {
  asm volatile("global_load_dwordx4 %0, %1, %2 offset:%3" : "=v"(d) : "v"(voff), "s"(sbase), "n"(OFF) : "memory");
}
template <int N> DI void wait_vm() { asm volatile("s_waitcnt vmcnt(%0)" ::"n"(N) : "memory"); }
template <int N> DI void wait_vm_bar() { asm volatile("s_waitcnt vmcnt(%0) lgkmcnt(0)\n\ts_barrier" ::"n"(N) : "memory"); }
DI void bar() { asm volatile("s_waitcnt lgkmcnt(0)\n\ts_barrier" ::: "memory"); }
template <class T> DI void pin(T& v) { asm volatile("" : "+v"(v)); }
DI bf16x8 lds_ld16(ldsp p) { return *(const LAS bf16x8*)p; }
DI bf16x4 lds_tr(ldsp p) { return __builtin_amdgcn_ds_read_tr16_b64_v4i16((LAS bf16x4*)p); }
DI bf16x8 lds_tr2(ldsp p0, ldsp p1) {
  bf16x4 a = lds_tr(p0), b = lds_tr(p1);
  return __builtin_shufflevector(a, b, 0, 1, 2, 3, 4, 5, 6, 7);
}
DI float xsum32(float v) {
  auto r = __builtin_amdgcn_permlane32_swap(__float_as_uint(v), __float_as_uint(v), false, false);
  return __uint_as_float(r[0]) + __uint_as_float(r[1]);
}
DI float xsum16_32(float v) {
  auto r = __builtin_amdgcn_permlane16_swap(__float_as_uint(v), __float_as_uint(v), false, false);
  return xsum32(__uint_as_float(r[0]) + __uint_as_float(r[1]));
}
DI rsrc_t mk_rsrc(void* p, int bytes) { return __builtin_amdgcn_make_buffer_rsrc(p, 0, bytes, 0x00020000); }
template <int AUX = 16> DI void st16(rsrc_t r, int off, u32x4 v) { __builtin_amdgcn_raw_buffer_store_b128(v, r, off, 0, AUX); }
template <int AUX = 16> DI void st8(rsrc_t r, int off, u32x2 v) { __builtin_amdgcn_raw_buffer_store_b64(v, r, off, 0, AUX); }
#define CSH 2
#define PDLY 0
#define PSLP 2
#define REL0 2
#define REL1 6
#define REL2 10
#define REL3 13
#define REL4 16
#define SLP4 1
#define PSTG 3
#define PCHK 3
#define PMODE 0
#define PGAP 4
#define LDAUX 16
#define WIN5 5
#define AUX1 16
#define AUX3 0
#define AUX5 0
#define AUX4 16

template <int P, int NT, int NS, int X, class F, class G> DI void loader_loop(F&& issue, G&& extra) {
  static_assert(NS >= 4, "three stages in flight + one being read");
  issue(0); issue(1);
  wait_vm_bar<P>();
  issue(2); issue(3);
  int slot = 4 % NS;
#pragma unroll 1
  for (int t = 1; t < NT - 3; ++t) {
    wait_vm_bar<2 * P>();
    issue(slot);
    slot = slot == NS - 1 ? 0 : slot + 1;
  }
  extra();
  wait_vm_bar<2 * P + X>(); wait_vm_bar<P + X>(); wait_vm_bar<X>();
}
DI void touch(const char* p, int lane, int nlines, unsigned lds_scratch) {
  set_m0(lds_scratch);
  asm volatile("global_load_lds_dword %0, %1" ::"v"((unsigned)((lane < nlines ? lane : nlines - 1) * 128)), "s"(p) : "memory");
}
#define CODE_ADDR(dst, sym) asm volatile("s_getpc_b64 s[40:41]\n\ts_add_u32 s40, s40, " #sym "@rel32@lo+4\n\ts_addc_u32 s41, s41, " #sym "@rel32@hi+12\n\ts_mov_b64 %0, s[40:41]" : "=s"(dst) :: "s40", "s41", "scc")
template <int P> DI void dma_run(const char* src, unsigned dst, unsigned lane16) {
  static_assert(P <= 8, "offset range");
  set_m0(dst + 4096);
  sfor<0, P>([&](auto ic) { dma<decltype(ic)::value * 1024 - 4096>(src + 4096, lane16); });
}
template <int N> DI void dma_long(const char* src, unsigned dst, unsigned lane16) {
  sfor<0, N / 5>([&](auto ic) { constexpr int i = decltype(ic)::value; dma_run<5>(src + i * 5120, dst + i * 5120, lane16); });
}
DI unsigned lds_add(ldsp p, int lane) {
  unsigned old = 0;
  if (lane == 0) old = __hip_atomic_fetch_add((LAS unsigned*)p, 1u, __ATOMIC_RELAXED, __HIP_MEMORY_SCOPE_WORKGROUP);
  return __builtin_amdgcn_readfirstlane(old);
}
DI void lds_wait(ldsp p, unsigned want) {
  for (int sp = 0; sp < (1 << 22); ++sp) {
    if ((unsigned)__builtin_amdgcn_readfirstlane(*(volatile LAS unsigned*)p) >= want) break;
    __builtin_amdgcn_s_sleep(1);
  }
  asm volatile("" ::: "memory");
}
DI void lds_post(ldsp p, unsigned v, int lane) { if (lane == 0) *(volatile LAS unsigned*)p = v; asm volatile("" ::: "memory"); }
DI void lds_wait4(ldsp p, unsigned want) {
  for (int sp = 0; sp < (1 << 22); ++sp) {
    const u32x4 v = *(volatile LAS u32x4*)p;
    if ((unsigned)__builtin_amdgcn_readfirstlane(min(min(v[0], v[1]), min(v[2], v[3]))) >= want) break;
    __builtin_amdgcn_s_sleep(SLP4);
  }
  asm volatile("" ::: "memory");
}
DI void cnt_add(char* ws, int idx) { __hip_atomic_fetch_add((unsigned*)(ws + WS_CNT) + idx * CSTR, 1u, __ATOMIC_RELAXED, __HIP_MEMORY_SCOPE_AGENT); }
DI void cnt_wait(char* ws, int idx, unsigned want) {
  unsigned* p = (unsigned*)(ws + WS_CNT) + idx * CSTR;
  for (int sp = 0; sp < (1 << 18); ++sp) {
    if ((unsigned)__builtin_amdgcn_readfirstlane(__hip_atomic_load(p, __ATOMIC_RELAXED, __HIP_MEMORY_SCOPE_AGENT)) >= want) break;
    __builtin_amdgcn_s_sleep(PSLP);
  }
  __builtin_amdgcn_fence(__ATOMIC_ACQUIRE, "wavefront");
}
#define PP_CL "memory", "v100", "v101", "v102"
#define PP_ISSUE(R) asm volatile("s_mov_b64 exec, 1\n\tglobal_atomic_or " R ", %0, %0, %1 sc0\n\ts_mov_b64 exec, -1" ::"v"(0u), "s"(p) : PP_CL)
#define PP_CHECK(R) { unsigned v_; asm volatile("s_waitcnt vmcnt(2)\n\tv_readfirstlane_b32 %0, " R : "=s"(v_)::PP_CL); if (v_ >= want) break; }
DI void cnt_poll_post(char* ws, int idx, unsigned want, ldsp word) {
  unsigned* p = (unsigned*)(ws + WS_CNT) + idx * CSTR;
  PP_ISSUE("v100"); __builtin_amdgcn_s_sleep(4); PP_ISSUE("v101"); __builtin_amdgcn_s_sleep(4);
#pragma unroll 1
  for (int sp = 0; sp < (1 << 17); ++sp) { PP_ISSUE("v102"); PP_CHECK("v100"); PP_ISSUE("v100"); PP_CHECK("v101"); PP_ISSUE("v101"); PP_CHECK("v102"); }
  asm volatile("ds_write_b32 %0, %1\n\ts_waitcnt vmcnt(0)" ::"v"((unsigned)(uintptr_t)word), "v"(1u) : PP_CL);
}
DI bf16x8 ld16_sc1(rsrc_t r, int voff, int soff) { return __builtin_bit_cast(bf16x8, __builtin_amdgcn_raw_buffer_load_b128(r, voff, soff, LDAUX)); }

template <int KIND> DI void pack_issue(const u16* wsrc, const u16* wsrc2, int ks, int cg, unsigned ldsdst, int lane) {
  constexpr int PITCH = KIND == 1 ? DF * 2 : D_ * 2;
  set_m0(ldsdst);
  sfor<0, 4>([&](auto ic) {
    constexpr int g = decltype(ic)::value;
    const int t = (lane >> 5) & 1, q = (lane >> 3) & 3;
    int row;
    if (KIND == 0) { const int P = 4 * ks + g, hp = P / 5, r = P - 5 * hp; row = 40 * hp + q + (r < 2 ? 8 * t + 4 * r : r == 2 ? 16 + 4 * t : 12 + 8 * t + 4 * r); }
    else if (KIND == 1) row = 32 * (ks >> 1) + 16 * t + 4 * (2 * (ks & 1) + (g & 1)) + q;
    else row = 32 * ks + 16 * (g >> 1) + 8 * t + 4 * (g & 1) + q;
    dma_nt<g * 1024>((const char*)(KIND == 1 && g >= 2 ? wsrc2 : wsrc) + cg * 128 - 4096, (unsigned)(row * PITCH + (lane & 7) * 16 + 4096 - g * 1024));
  });
}
template <int KIND> DI void pack_finish(ldsp u, rsrc_t rw, int ks, int cg, int lane, LAS float* cs) {
  const int q = (lane & 15) >> 2, p = lane & 3;
  ldsp a = KIND == 1 ? u + ((lane >> 4) & 1) * 2048 + (8 * (lane >> 5) + q) * 128 + p * 8 : u + (8 * (lane >> 4) + q) * 128 + p * 8;
#pragma unroll
  for (int i = 0; i < 4; ++i) {
    const int nt = 4 * cg + i;
    const u32x4 f = __builtin_bit_cast(u32x4, lds_tr2(a + i * 32, a + i * 32 + 512));
    if (KIND == 1) {
      const int ft = nt / 6, j = nt - ft * 6;
      st16(rw, WS_WGU + ((ft * 40 + ks) * 6 + j) * 1024 + lane * 16, f);
      const unsigned sv = (lane & 15) == ((lane >> 4) & 1) ? ONES2 : 0u;
      const f32x4 c = mfma16(__builtin_bit_cast(bf16x8, (u32x4){sv, sv, sv, sv}), __builtin_bit_cast(bf16x8, f), (f32x4){0.f, 0.f, 0.f, 0.f});
      if (lane < 16) { cs[i * 32 + lane] = c[0]; cs[i * 32 + 16 + lane] = c[1]; }
    } else {
      const int ct = nt / 5, j = nt - ct * 5;
      st16(rw, (KIND == 0 ? WS_WO : WS_WD) + ((ct * (KIND == 0 ? 20 : 48) + ks) * 5 + j) * 1024 + lane * 16, f);
    }
  }
}

#define TLN 58
#define TWO 8
constexpr int NWD_A = 352;
#define WD0 8
#define WD1 24
constexpr int K1_NS = 4, K1_STG = 32768, K1_NT = 10, K1_CTL = K1_NS * K1_STG, K1_LDS = K1_CTL + 4096;

DI void qkv_compute(ldsp lds, char* __restrict__ ws, int mt, int nt, int w, int lane) {
  const int wm = w >> 1, wn = w & 1, r32 = lane & 31, hi = lane >> 5, q = (lane & 15) >> 2, p = lane & 3, blk = (lane >> 4) & 1;
  TS_DECL;
  ldsp ctl = lds + K1_CTL;
  int aoff[4], woff[2];
#pragma unroll
  for (int s = 0; s < 4; ++s) aoff[s] = (wm * 64 + r32) * 128 + (((2 * s + hi) ^ ((r32 >> 1) & 7)) * 16);
#pragma unroll
  for (int j = 0; j < 2; ++j) woff[j] = 16384 + (8 * hi + q) * 256 + (((wn * 2 + (j ^ wm)) ^ q) * 64) + blk * 32 + p * 8;
  f32x16 acc[2][2];
#pragma unroll
  for (int j = 0; j < 2; ++j)
#pragma unroll
    for (int i = 0; i < 2; ++i) { acc[j][i] = (f32x16){}; pin(acc[j][i]); }
  f32x4 cs = {};
  const unsigned sv = (lane & 15) == blk ? ONES2 : 0u;
  const bf16x8 sel = __builtin_bit_cast(bf16x8, (u32x4){sv, sv, sv, sv});
  bf16x8 xf[2][2], wf[2][2];
  auto rdx = [&](ldsp sb, int s, int i) { xf[s & 1][i] = lds_ld16(sb + aoff[s] + i * 4096); };
  auto rdw = [&](ldsp sb, int s, int j) { wf[s & 1][j] = lds_tr2(sb + woff[j] + s * 4096, sb + woff[j] + s * 4096 + 1024); };
  auto mm = [&](int s, int j, int i) { acc[j][i] = mfma32(wf[s & 1][j], xf[s & 1][i], acc[j][i]); };
  auto step = [&](ldsp nb, int s, int sn, bool rd) {
    cs = mfma16(sel, wf[s & 1][0], cs);
    mm(s, 0, 0); if (rd) { rdx(nb, sn, 0); rdw(nb, sn, 0); } SB0();
    mm(s, 0, 1); if (rd) { rdx(nb, sn, 1); rdw(nb, sn, 1); } SB0();
    mm(s, 1, 0); SB0();
    mm(s, 1, 1); SB0();
  };
  bar();
  TS();
  ldsp sb = lds;
  rdx(sb, 0, 0); rdw(sb, 0, 0); rdx(sb, 0, 1); rdw(sb, 0, 1); SB0();
  int slot = 0;
#pragma unroll 1
  for (int t = 0; t < K1_NT - 1; ++t) {
    step(sb, 0, 1, true); step(sb, 1, 2, true); step(sb, 2, 3, true);
    slot = slot == K1_NS - 1 ? 0 : slot + 1;
    sb = lds + slot * K1_STG;
    bar(); SB0();
    step(sb, 3, 0, true);
  }
  step(sb, 0, 1, true); step(sb, 1, 2, true); step(sb, 2, 3, true);
  const LAS f32x2* rst = (const LAS f32x2*)(ctl + 1024);
  LAS float* cst = (LAS float*)(ctl + 2048);
  cs = mfma16(sel, wf[1][0], cs);
  mm(3, 0, 0); mm(3, 0, 1); SB0();
  if (lane < 16) { cst[wn * 64 + wm * 32 + lane] = cs[0]; cst[wn * 64 + wm * 32 + 16 + lane] = cs[1]; }
  mm(3, 1, 0); mm(3, 1, 1); SB0();
  TS();
  bar();
  TS();
  const rsrc_t rw = mk_rsrc(ws, WS_END);
  const float sc = nt < 5 ? QSCALE : 1.f;
  f32x4 c4[2][4];
#pragma unroll
  for (int j = 0; j < 2; ++j)
#pragma unroll
    for (int b = 0; b < 4; ++b) c4[j][b] = *(const LAS f32x4*)(cst + wn * 64 + (j ^ wm) * 32 + 8 * b + 4 * hi);
#pragma unroll
  for (int i = 0; i < 2; ++i) {
    const int ml = wm * 64 + i * 32 + r32;
    const f32x2 st = rst[ml];
    const float a = st.y * sc, nm = -st.x * a;
    const int ob = WS_QK + ((nt * 16 + wn * 8 + hi) * S_ + mt * 128 + ml) * 16;
#pragma unroll
    for (int j = 0; j < 2; ++j)
#pragma unroll
      for (int bp = 0; bp < 2; ++bp) {
        unsigned pk[2][2];
#pragma unroll
        for (int e = 0; e < 2; ++e) {
          const int b = 2 * bp + e;
          const f32x2 v0 = (f32x2){acc[j][i][4 * b], acc[j][i][4 * b + 1]} * a + nm * (f32x2){c4[j][b][0], c4[j][b][1]};
          const f32x2 v1 = (f32x2){acc[j][i][4 * b + 2], acc[j][i][4 * b + 3]} * a + nm * (f32x2){c4[j][b][2], c4[j][b][3]};
          pk[e][0] = cvtpk(v0[0], v0[1]); pk[e][1] = cvtpk(v1[0], v1[1]);
        }
        auto r0 = __builtin_amdgcn_permlane32_swap(pk[0][0], pk[1][0], false, false);
        auto r1 = __builtin_amdgcn_permlane32_swap(pk[0][1], pk[1][1], false, false);
        st16<AUX1>(rw, ob + ((j ^ wm) * 4 + bp * 2) * S_ * 16, (u32x4){r0[0], r1[0], r0[1], r1[1]});
      }
  }
  TS();
  if (w == 0) { TS_END(0); }
}

DI void qkv_stats(ldsp lds, int sw, int lane, const char* wo_part) {
  const int fr = lane & 15, g = lane >> 4;
  const int off = (sw * 32 + fr) * 128, x = fr >> 1;
  const bf16x8 ones = __builtin_bit_cast(bf16x8, (u32x4){ONES2, ONES2, ONES2, ONES2});
  f32x4 sm[2] = {}, sq[2] = {};
  int slot = 0;
  __builtin_amdgcn_s_setprio(2);
#pragma unroll 1
  for (int t = 0; t < K1_NT; ++t) {
    bar();
    ldsp sb = lds + slot * K1_STG + off;
    slot = slot == K1_NS - 1 ? 0 : slot + 1;
    if (t == TWO) touch(wo_part, lane, TLN, (unsigned)(uintptr_t)lds + K1_CTL + 3072);
    bf16x8 v[2][2];
#pragma unroll
    for (int ks = 0; ks < 2; ++ks)
#pragma unroll
      for (int i = 0; i < 2; ++i) v[ks][i] = lds_ld16(sb + i * 2048 + (((4 * ks + g) ^ x) * 16));
#pragma unroll
    for (int ks = 0; ks < 2; ++ks)
#pragma unroll
      for (int i = 0; i < 2; ++i) { sq[i] = mfma16(v[ks][i], v[ks][i], sq[i]); sm[i] = mfma16(ones, v[ks][i], sm[i]); }
  }
#pragma unroll
  for (int i = 0; i < 2; ++i) {
    const int p = lane & 3;
    const float q = p == 0 ? sq[i][0] : p == 1 ? sq[i][1] : p == 2 ? sq[i][2] : sq[i][3];
    const float mu = sm[i][0] * (1.f / D_), var = fmaxf(q * (1.f / D_) - mu * mu, 0.f);
    if ((fr >> 2) == g) ((LAS f32x2*)(lds + K1_CTL + 1024))[sw * 32 + i * 16 + fr] = (f32x2){mu, __builtin_amdgcn_rsqf(var + LN_EPS)};
  }
  bar();
  if (TWO == 10) touch(wo_part, lane, 58, (unsigned)(uintptr_t)lds + K1_CTL + 3072);
}

DI void qkv_loader(ldsp lds, const u16* __restrict__ x, const u16* __restrict__ wqkv, int mt, int nt, int lw, int lane) {
  const bool isx = lw < 2;
  const int half = lw & 1;
  unsigned voff[8];
#pragma unroll
  for (int i = 0; i < 8; ++i) {
    const int r = (half * 8 + i) * 8 + (lane >> 3), c = (lane & 7) ^ ((r >> 1) & 7);
    const int k = (half * 8 + i) * 4 + (lane >> 4), cp = lane & 15;
    voff[i] = (isx ? r * 1280 + c * 16 : k * 3840 + (cp ^ ((k & 3) << 2)) * 16) + 8192 - i * 1024;
  }
  const char* src = (isx ? (const char*)x + (size_t)mt * 128 * 1280 : (const char*)wqkv + nt * 256) - 4096;
  const int sstr = isx ? 128 : 64 * 3840;
  const unsigned dst = (unsigned)(uintptr_t)lds + lw * 8192 + 4096;
  loader_loop<8, K1_NT, K1_NS, 0>([&](int slot) {
    set_m0(dst + slot * K1_STG);
    sfor<0, 8>([&](auto ic) { constexpr int i = decltype(ic)::value; dma<i * 1024 - 4096>(src, voff[i]); });
    src += sstr;
  }, [] {});
}

extern "C" __global__ __launch_bounds__(768, 3) void k_qkv(const u16* __restrict__ x, const u16* __restrict__ wqkv, const u16* __restrict__ wo, const u16* __restrict__ wd, char* __restrict__ ws) {
  extern __shared__ __attribute__((aligned(16))) char smem[];
  ldsp lds = (ldsp)smem;
  const int lane = threadIdx.x & 63, w = __builtin_amdgcn_readfirstlane(threadIdx.x >> 6);
  const int xcd = blockIdx.x & 7, idx = blockIdx.x >> 3;
  const int nt = (xcd & 1) * 8 + (idx >> 2), mt = (xcd >> 1) * 4 + (idx & 3);
  if (w < 4 && nt < 15) { qkv_loader(lds, x, wqkv, mt, nt, w, lane); return; }
  if (w == 11 && lane < 2) ((unsigned*)(ws + WS_CNT))[(blockIdx.x * 2 + lane) * CSTR] = 0u;
  if (w == 10) {
    const char *c0, *c1;
    CODE_ADDR(c0, k_qkv); CODE_ADDR(c1, k_text_end);
    const unsigned lim = c1 > c0 ? (unsigned)(c1 - c0) : 0u, off = (idx * 16 + (lane & 15)) * 128;
    set_m0((unsigned)(uintptr_t)lds + K1_CTL + 3072 + 512);
    asm volatile("global_load_lds_dword %0, %1" ::"v"(off < lim ? off : lim), "s"(c0) : "memory");
  }
  if (w == 8 || w == 9) {
    const int id = blockIdx.x * 64 + lane, u = w - 8;
    const bool isx = blockIdx.x < 128;
    const int i2 = id - 8192 < 7680 ? id - 8192 : 7679;
    const unsigned off = isx ? (id >> 2) * 1280 + 256 + u * 512 + (id & 3) * 128 : (128 + u * 256) * 3840 + i2 * 128;
    set_m0((unsigned)(uintptr_t)lds + K1_CTL + 3072 + u * 256);
    const char* src = isx ? (const char*)x : (const char*)wqkv;
    asm volatile("" : "+s"(src));
    if (u == 0) __builtin_amdgcn_s_sleep(WD0); else __builtin_amdgcn_s_sleep(WD1);
    asm volatile("global_load_lds_dword %0, %1" ::"v"(off), "s"(src) : "memory");
  }
  if (nt == 15) {
    const rsrc_t rw = mk_rsrc(ws, WS_END);
    constexpr int NU = (200 + NWD_A + 191) / 192;
    auto iss = [&](int r) {
      const int u = min(mt * 12 + w + r * 192, 200 + NWD_A - 1), v = u - 200;
      if (u < 200) pack_issue<0>(wo, wo, u / 10, u % 10, (unsigned)(uintptr_t)lds + (w * 2 + (r & 1)) * 4096, lane);
      else pack_issue<2>(wd, wd, v / 10, v % 10, (unsigned)(uintptr_t)lds + (w * 2 + (r & 1)) * 4096, lane);
    };
    iss(0); iss(1);
#pragma unroll 1
    for (int r = 0; r < NU; ++r) {
      if (r == 0) wait_vm<4>(); else if (r + 1 < NU) wait_vm<8>(); else wait_vm<4>();
      const int u = min(mt * 12 + w + r * 192, 200 + NWD_A - 1), v = u - 200;
      if (u < 200) pack_finish<0>(lds + (w * 2 + (r & 1)) * 4096, rw, u / 10, u % 10, lane, nullptr);
      else pack_finish<2>(lds + (w * 2 + (r & 1)) * 4096, rw, v / 10, v % 10, lane, nullptr);
      asm volatile("s_waitcnt lgkmcnt(0)" ::: "memory");
      if (r + 2 < NU) iss(r + 2);
    }
    return;
  }
  if (w >= 8) qkv_stats(lds, w - 8, lane, ws + WS_WO + min((idx * 4 + w - 8) * 58, 6400 - 58) * 128);
  else qkv_compute(lds, ws, mt, nt, w - 4, lane);
}

#define LAZY_LIM 1.0995116e12f
#define VLEAD 3
constexpr int K2_K = 102400, K2_V = K2_K + 24576, K2_VP = 8192 + 64, K2_CS = K2_V + 4 * K2_VP, K2_MSK = K2_CS + 2560, K2_FL = K2_MSK + 832, K2_LDS = K2_FL + 64 + 256;
static_assert(K2_LDS <= 163840 && K2_FL % 16 == 0, "LDS map");

DI void n80_epilogue_h(f32x4 (&acc)[5], rsrc_t rw, int mt, int ct, int w, int lane) {
  const int fr = lane & 15, g = lane >> 4, tok = mt * 64 + w * 16 + fr;
  const int odd = ct & 1, pb = (ct >> 1) * 5 + (odd ? 3 : 0), ps = (ct >> 1) * 5 + 2;
  const int hb = WS_H + (((mt >> 1) & 1) * 40 + (g >> 1)) * PLANE + (mt >> 2) * 4096 + ((mt & 1) * 2 + (w >> 1)) * 1024 + ((g & 1) * 32 + (w & 1) * 16 + fr) * 16;
  u32x2 hv[5];
  float s = 0.f;
#pragma unroll
  for (int j = 0; j < 5; ++j) hv[j] = (u32x2){cvtpk(acc[j][0], acc[j][1]), cvtpk(acc[j][2], acc[j][3])};
  const u32x2 a0 = odd ? hv[1] : hv[0], a1 = odd ? hv[2] : hv[1], b0 = odd ? hv[3] : hv[2], b1 = odd ? hv[4] : hv[3], s1 = odd ? hv[0] : hv[4];
  st16<AUX3>(rw, hb + pb * 2 * PLANE, (u32x4){a0[0], a0[1], a1[0], a1[1]});
  st16<AUX3>(rw, hb + (pb + 1) * 2 * PLANE, (u32x4){b0[0], b0[1], b1[0], b1[1]});
  st8<AUX3>(rw, hb + ps * 2 * PLANE + odd * 8, s1);
  f32x4 v[5];
#pragma unroll
  for (int j = 0; j < 5; ++j) { v[j] = (f32x4){bflo(hv[j][0]), bfhi(hv[j][0]), bflo(hv[j][1]), bfhi(hv[j][1])}; s += (v[j][0] + v[j][1]) + (v[j][2] + v[j][3]); }
  const float mean = xsum16_32(s) * (1.f / 80.f);
  float m2 = 0.f;
#pragma unroll
  for (int j = 0; j < 5; ++j) { const f32x4 d = v[j] - mean; m2 += (d[0] * d[0] + d[1] * d[1]) + (d[2] * d[2] + d[3] * d[3]); }
  m2 = xsum16_32(m2);
  if (g == 0) st8<AUX3>(rw, WS_ST + (((ct >> 2) * S_ + tok) * 4 + (ct & 3)) * 8, (u32x2){__float_as_uint(mean), __float_as_uint(m2)});
}

#define KVDMA dma
DI void attn_helper(ldsp lds, char* __restrict__ ws, int c, int h, int hw, int lane, const u16* __restrict__ x, const u16* __restrict__ wg, const u16* __restrict__ wu, const u16* __restrict__ wd) {
  const unsigned lds0 = (unsigned)(uintptr_t)lds;
  const int pm = 5 * (h >> 1) + 3 * (h & 1), pt = 5 * (h >> 1) + 2;
  const bool EDGE = c == 0;
  const int tok0 = 256 * c - 256 + 128 * hw + ((EDGE && hw < 2) ? 256 : 0);
  const char* kb = ws + WS_QK + (80 * S_ + tok0) * 16;
  {
    set_m0(lds0 + K2_K + hw * 2048); KVDMA<0>(kb + pm * S_ * 16, lane * 16); KVDMA<1024>(kb + pm * S_ * 16, lane * 16);
    set_m0(lds0 + K2_K + 8192 + hw * 2048); KVDMA<0>(kb + (pm + 1) * S_ * 16, lane * 16); KVDMA<1024>(kb + (pm + 1) * S_ * 16, lane * 16);
    set_m0(lds0 + K2_K + 16384 + hw * 2048); KVDMA<0>(kb + pt * S_ * 16, lane * 16); KVDMA<1024>(kb + pt * S_ * 16, lane * 16);
  }
  TS_DECL;
  const int b = blockIdx.x, xcd = b & 7, idx = b >> 3;
  if (hw == 0 && lane < 8) *(LAS unsigned*)(lds + K2_FL + lane * 4) = 0u;
  {
    const unsigned tm = (EDGE && hw < 2 ? 0xff80u : 0u) | 0xbf800000u;
    *(LAS u32x4*)(lds + K2_V + 3 * K2_VP + hw * 2048 + lane * 16) = (u32x4){tm, 0xbf80bf80u, ONES2, ONES2};
    *(LAS u32x4*)(lds + K2_V + 3 * K2_VP + hw * 2048 + 1024 + lane * 16) = (u32x4){tm, 0xbf80bf80u, ONES2, ONES2};
  }
  if (hw == 1 && lane < 52) {
    const int d = lane - 20;
    u32x4 mk;
#pragma unroll
    for (int e = 0; e < 4; ++e) { const int k0 = ((2 * e) & 3) + 8 * (e >> 1); mk[e] = (k0 > d ? 0xffffu : 0u) | (k0 + 1 > d ? 0xffff0000u : 0u); }
    *(LAS u32x4*)(lds + K2_MSK + lane * 16) = mk;
  }
  wait_vm<VLEAD>();
  {
    const char* vb = kb + 80 * S_ * 16;
    set_m0(lds0 + K2_V + hw * 2048); KVDMA<0>(vb + pm * S_ * 16, lane * 16); KVDMA<1024>(vb + pm * S_ * 16, lane * 16);
    set_m0(lds0 + K2_V + K2_VP + hw * 2048); KVDMA<0>(vb + (pm + 1) * S_ * 16, lane * 16); KVDMA<1024>(vb + (pm + 1) * S_ * 16, lane * 16);
    set_m0(lds0 + K2_V + 2 * K2_VP + hw * 2048); KVDMA<0>(vb + pt * S_ * 16, lane * 16); KVDMA<1024>(vb + pt * S_ * 16, lane * 16);
  }
  const rsrc_t rw = mk_rsrc(ws, WS_END);
  const int mt = xcd * 4 + (idx >> 3), ct = idx & 7;
  const int q0 = hw * 25600;
  const int bb = b, pcg = bb % 24, pks = (bb / 24) * 4 + hw, v0 = NWD_A + (b - 240) * 8 + hw * 2;
  wait_vm_bar<6>();
  if (b < 240) { pack_issue<1>(wg, wu, pks, pcg, lds0 + q0, lane); wait_vm<4>(); }
  else { pack_issue<2>(wd, wd, v0 / 10, v0 % 10, lds0 + q0, lane); pack_issue<2>(wd, wd, v0 / 10, v0 % 10 + 1, lds0 + q0 + 4096, lane); wait_vm<8>(); }
  if (lane == 0) *(volatile LAS unsigned*)(lds + K2_FL + 16 + hw * 4) = 1u;
  TS();
  u32x4 xr[2]; u32x2 xs;
  {
    const char* xp = (const char*)x + (size_t)(mt * 64 + hw * 16) * 1280 + ct * 160;
    const int g = lane >> 4;
    const unsigned o16 = (lane & 15) * 1280 + (g & 1) * 32 + (g >> 1) * 16, o8 = (lane & 15) * 1280 + g * 8;
    asm volatile("global_load_dwordx4 %0, %1, %2" : "=v"(xr[0]) : "v"(o16), "s"(xp) : "memory");
    asm volatile("global_load_dwordx4 %0, %1, %2 offset:64" : "=v"(xr[1]) : "v"(o16), "s"(xp) : "memory");
    asm volatile("global_load_dwordx2 %0, %1, %2 offset:128" : "=v"(xs) : "v"(o8), "s"(xp) : "memory");
  }
  dma_long<15>(ws + WS_WO + ct * 102400 + q0 + 10240, lds0 + q0 + 10240, lane * 16);
  wait_vm<15>();
  pin(xr[0]); pin(xr[1]); pin(xs);
  TS();
  if (b >= 240) {
    pack_finish<2>(lds + q0, rw, v0 / 10, v0 % 10, lane, nullptr); pack_finish<2>(lds + q0 + 4096, rw, v0 / 10, v0 % 10 + 1, lane, nullptr);
    asm volatile("s_waitcnt lgkmcnt(0)" ::: "memory");
  } else {
    LAS float* ct4 = (LAS float*)(lds + K2_CS);
    pack_finish<1>(lds + q0, rw, pks, pcg, lane, ct4 + hw * 128);
    asm volatile("s_waitcnt lgkmcnt(0)" ::: "memory");
    if (lds_add(lds + K2_FL, lane) == 3) {
      asm volatile("" ::: "memory");
      const LAS f32x2* c2 = (const LAS f32x2*)ct4 + lane;
      const f32x2 a = (c2[0] + c2[64]) + (c2[128] + c2[192]);
      const int e = pcg * 128 + lane * 2;
      const int hf = e / 96;
      st8(rw, WS_CS + hf * 4096 + ((bb / 24) * 96 + e - hf * 96) * 4, __builtin_bit_cast(u32x2, a));
    }
  }
  TS();
  dma_long<10>(ws + WS_WO + ct * 102400 + q0, lds0 + q0, lane * 16);
  if (hw == 3) {
    const char *c0, *c1;
    CODE_ADDR(c0, k_mlp); CODE_ADDR(c1, k_text_end);
    const unsigned lim = c1 > c0 ? (unsigned)(c1 - c0) : 0u, off = (idx * 8 + (lane & 7)) * 128;
    set_m0(lds0 + K2_FL + 64);
    asm volatile("global_load_lds_dword %0, %1" ::"v"(off < lim ? off : lim), "s"(c0) : "memory");
  }
  f32x4 acc[5];
#pragma unroll
  for (int jp = 0; jp < 2; ++jp) {
    auto r0 = __builtin_amdgcn_permlane16_swap(xr[jp][0], xr[jp][2], false, false);
    auto r1 = __builtin_amdgcn_permlane16_swap(xr[jp][1], xr[jp][3], false, false);
    acc[2 * jp] = (f32x4){bflo(r0[0]), bfhi(r0[0]), bflo(r1[0]), bfhi(r1[0])};
    acc[2 * jp + 1] = (f32x4){bflo(r0[1]), bfhi(r0[1]), bflo(r1[1]), bfhi(r1[1])};
  }
  acc[4] = (f32x4){bflo(xs[0]), bfhi(xs[0]), bflo(xs[1]), bfhi(xs[1])};
#pragma unroll
  for (int j = 0; j < 5; ++j) pin(acc[j]);
  wait_vm<0>();
  lds_add(lds + K2_FL + 8, lane);
  lds_wait(lds + K2_FL + 8, 4);
  TS();
  lds_wait(lds + K2_FL + 12, 1);
  TS();
  bf16x8 af[20], wf[3][5];
  const int ao = WS_QK + (lane >> 4) * PLANE + (mt * 64 + hw * 16 + (lane & 15)) * 16;
#pragma unroll
  for (int ks = 0; ks < 20; ++ks) af[ks] = ld16_sc1(rw, ao, ks * 4 * PLANE);
  SB0();
  ldsp wp = lds + lane * 16;
#pragma unroll
  for (int j = 0; j < 10; ++j) wf[j / 5][j % 5] = lds_ld16(wp + j * 1024);
#pragma unroll
  for (int ks = 0; ks < 20; ++ks) {
#pragma unroll
    for (int j = 0; j < 5; ++j) {
      if (ks + 2 < 20) wf[(ks + 2) % 3][j] = lds_ld16(wp + (ks + 2) * 5120 + j * 1024);
      acc[j] = mfma16(wf[ks % 3][j], af[ks], acc[j]);
    }
    SB0();
  }
  TS();
  n80_epilogue_h(acc, rw, mt, ct, hw, lane);
  TS();
  if (hw == 1) { TS_END(2); }
}

template <bool EXACT> DI f32x16 attn_rows(ldsp lds, ldsp kp, ldsp ktp, ldsp vp, bf16x8 qm, u32x2 qtr, int dq, int hi) {
  const float NEG = -__builtin_inff();
  auto mx3 = [](float a, float b, float c) { return __builtin_elementwise_maximum(__builtin_elementwise_maximum(a, b), c); };
  auto qk = [&](int j, u32x2 q2, f32x16 cin) {
    return __builtin_amdgcn_mfma_f32_32x32x8bf16_1k(*(const LAS bf16x4*)(ktp + j * 512), __builtin_bit_cast(bf16x4, q2), mfma32(lds_ld16(kp + j * 512), qm, cin), 0, 0, 0);
  };
  f32x16 s0, s8;
#pragma unroll
  for (int r = 0; r < 16; ++r) { const bool le = (r & 3) + 8 * (r >> 2) <= dq; s8[r] = le ? 0.f : NEG; s0[r] = le ? NEG : 0.f; }
  if (!EXACT) {
    pin(s0); pin(s8); bar();
  }
  bf16x8 km[2]; bf16x4 kt[2];
  s8 = qk(8, qtr, s8); s0 = qk(0, qtr, s0);
  km[1] = lds_ld16(kp + 512); kt[1] = *(const LAS bf16x4*)(ktp + 512);
  km[0] = lds_ld16(kp + 1024); kt[0] = *(const LAS bf16x4*)(ktp + 1024);
  f32x16 sa = mfma32(km[1], qm, (f32x16){});
#pragma unroll
  for (int r = 0; r < 16; ++r) s0[r] = __builtin_elementwise_maximum(s0[r], s8[r]);
  float m = mx3(mx3(mx3(s0[0], s0[1], s0[2]), mx3(s0[3], s0[4], s0[5]), mx3(s0[6], s0[7], s0[8])), mx3(mx3(s0[9], s0[10], s0[11]), s0[12], s0[13]), __builtin_elementwise_maximum(s0[14], s0[15]));
  if (EXACT) {
#pragma unroll 1
    for (int j = 1; j < 8; ++j) {
      const f32x16 t = qk(j, qtr, (f32x16){});
#pragma unroll
      for (int r = 0; r < 16; r += 2) m = mx3(m, t[r], t[r + 1]);
    }
  }
  {
    auto rr = __builtin_amdgcn_permlane32_swap(__float_as_uint(m), __float_as_uint(m), false, false);
    m = __builtin_elementwise_maximum(__uint_as_float(rr[0]), __uint_as_float(rr[1]));
  }
  u32x2 q2 = qtr;
  {
    const unsigned u0 = __float_as_uint(m) & 0xffff0000u;
    const float r1 = m - __uint_as_float(u0);
    const unsigned u1 = __float_as_uint(r1) & 0xffff0000u;
    const float r2 = r1 - __uint_as_float(u1);
    if (hi) q2 = (u32x2){0x3f80u | u0, (u1 >> 16) | (__float_as_uint(r2) & 0xffff0000u)};
  }
  const bf16x4 qs = __builtin_bit_cast(bf16x4, q2);
  s0 = __builtin_amdgcn_mfma_f32_32x32x8bf16_1k(__builtin_bit_cast(bf16x4, hi ? (u32x2){0xbf800000u, 0xbf80bf80u} : (u32x2){0u, 0u}), qs, s0, 0, 0, 0);
  sa = __builtin_amdgcn_mfma_f32_32x32x8bf16_1k(kt[1], qs, sa, 0, 0, 0);
  f32x16 sb = __builtin_amdgcn_mfma_f32_32x32x8bf16_1k(kt[0], qs, mfma32(km[0], qm, (f32x16){}), 0, 0, 0);
  SB0();
  auto ex8 = [&](const f32x16& s, int ks) {
    u32x4 p;
#pragma unroll
    for (int e = 0; e < 4; ++e) p[e] = cvtpk(__builtin_amdgcn_exp2f(s[8 * ks + 2 * e]), __builtin_amdgcn_exp2f(s[8 * ks + 2 * e + 1]));
    return p;
  };
  auto vld = [&](int slot, int ks) { return lds_tr2(vp + slot * 512 + ks * 256, vp + slot * 512 + ks * 256 + 128); };
  f32x16 o = {};
  auto pv = [&](bf16x8 v, u32x4 p) { o = mfma32(v, __builtin_bit_cast(bf16x8, p), o); };
  u32x4 mk0, mk1;
  u32x4 pk[8][2];
  pk[0][0] = ex8(s0, 0); pk[0][1] = ex8(s0, 1);
  km[1] = lds_ld16(kp + 3 * 512); kt[1] = *(const LAS bf16x4*)(ktp + 3 * 512);
  pk[1][0] = ex8(sa, 0); pk[1][1] = ex8(sa, 1);
  SB0();
  bf16x8 vf[2][4];
  u32x4 vrdy;
  sfor<2, 8>([&](auto jc) {
    constexpr int j = decltype(jc)::value;
    f32x16& cur = (j & 1) ? sa : sb;
    f32x16& nxt = (j & 1) ? sb : sa;
    constexpr int ta = j == 4 ? 0 : 2 * j - 9, tb = j == 4 ? 8 : 2 * j - 8;
    constexpr int na = 2 * j - 7, nb = j < 7 ? 2 * j - 6 : 7;
    if constexpr (j == 3) vrdy = *(volatile LAS u32x4*)(lds + K2_FL + 16);
    if constexpr (j >= 4) { vf[(j + 1) & 1][0] = vld(na, 0); vf[(j + 1) & 1][1] = vld(na, 1); if constexpr (j < 7) { vf[(j + 1) & 1][2] = vld(nb, 0); vf[(j + 1) & 1][3] = vld(nb, 1); } }
    if constexpr (j + 2 < 8) { km[j & 1] = lds_ld16(kp + (j + 2) * 512); kt[j & 1] = *(const LAS bf16x4*)(ktp + (j + 2) * 512); }
    if constexpr (j + 1 < 8) nxt = mfma32(km[(j + 1) & 1], qm, (f32x16){});
    SB0();
    pk[j][0] = ex8(cur, 0);
    if constexpr (j == 4) { const u32x4 a = pk[0][0] & mk0; pv(vf[0][0], a); pv(vf[0][2], pk[0][0] ^ a); }
    else if constexpr (j > 4) { pv(vf[j & 1][0], pk[ta][0]); pv(vf[j & 1][1], pk[ta][1]); }
    if constexpr (j + 1 < 8) nxt = __builtin_amdgcn_mfma_f32_32x32x8bf16_1k(kt[(j + 1) & 1], qs, nxt, 0, 0, 0);
    SB0();
    pk[j][1] = ex8(cur, 1);
    if constexpr (j == 4) { const u32x4 a = pk[0][1] & mk1; pv(vf[0][1], a); pv(vf[0][3], pk[0][1] ^ a); }
    else if constexpr (j > 4) { pv(vf[j & 1][2], pk[tb][0]); pv(vf[j & 1][3], pk[tb][1]); }
    if constexpr (j == 3) {
#pragma unroll
      for (int t = 0; t < 4; ++t) { pin(pk[t][0]); pin(pk[t][1]); }
      if (!EXACT && __builtin_expect(__builtin_amdgcn_readfirstlane((vrdy[0] & vrdy[1]) & (vrdy[2] & vrdy[3])) == 0, 0)) lds_wait4(lds + K2_FL + 16, 1);
      asm volatile("" ::: "memory");
      vf[0][0] = vld(0, 0); vf[0][1] = vld(0, 1); vf[0][2] = vld(8, 0); vf[0][3] = vld(8, 1);
      mk0 = *(const LAS u32x4*)(lds + K2_MSK + (dq + 20) * 16); mk1 = *(const LAS u32x4*)(lds + K2_MSK + (dq + 4) * 16);
    }
    SB0();
  });
  pv(vf[0][0], pk[7][0]); pv(vf[0][1], pk[7][1]);
  return o;
}

DI void attn_body(ldsp lds, char* __restrict__ ws, int c, int h, int w, int lane) {
  const int r32 = lane & 31, hi = lane >> 5;
  TS_DECL;
  const int pm = 5 * (h >> 1) + 3 * (h & 1), pt = 5 * (h >> 1) + 2;
  const char* qb = ws + WS_QK + (256 * c + 32 * w) * 16;
  const bf16x8 qm = *(const bf16x8*)(qb + ((pm + hi) * S_ + r32) * 16);
  u32x2 qtr = *(const u32x2*)(qb + (pt * S_ + r32) * 16 + (h & 1) * 8);
  if (hi) qtr = (u32x2){0x3f80u, 0u};
  ldsp kp = lds + K2_K + hi * 8192 + (w * 32 + r32) * 16;
  ldsp ktp = lds + (hi ? K2_V + 3 * K2_VP + w * 512 : K2_K + 16384 + (w * 32 + r32) * 16 + (h & 1) * 8);
  const int vq = (lane & 15) >> 2, vpp = lane & 3, vblk = (lane >> 4) & 1;
  ldsp vp = lds + K2_V + (w * 32 + 4 * hi + vq) * 16 + (vblk == 0 ? (vpp >> 1) * K2_VP + (vpp & 1) * 8 : vpp == 0 ? 2 * K2_VP + (h & 1) * 8 : 3 * K2_VP + 8);
  f32x16 o = attn_rows<false>(lds, kp, ktp, vp, qm, qtr, r32 - 4 * hi, hi);
  float l = __uint_as_float(__builtin_amdgcn_permlane32_swap(__float_as_uint(o[8]), __float_as_uint(o[8]), false, false)[1]);
  if (__builtin_expect(__builtin_amdgcn_ballot_w64(!(l < LAZY_LIM)) != 0, 0)) {
    int dq2 = r32 - 4 * hi, hi2 = hi;
    pin(dq2); pin(hi2);
    o = attn_rows<true>(lds, kp, ktp, vp, qm, qtr, dq2, hi2);
    l = __uint_as_float(__builtin_amdgcn_permlane32_swap(__float_as_uint(o[8]), __float_as_uint(o[8]), false, false)[1]);
  }
  TS();
  __builtin_amdgcn_s_setprio(APRIO2);
  const float il = __builtin_amdgcn_rcpf(l);
  const int tok = 256 * c + 32 * w + r32;
  const rsrc_t rw = mk_rsrc(ws, WS_END);
  const u32x4 w0 = {cvtpk(o[0] * il, o[1] * il), cvtpk(o[2] * il, o[3] * il), cvtpk(o[4] * il, o[5] * il), cvtpk(o[6] * il, o[7] * il)};
  st16(rw, WS_QK + (pm + hi) * PLANE + tok * 16, w0);
  if (hi == 0) st8(rw, WS_QK + pt * PLANE + tok * 16 + (h & 1) * 8, (u32x2){cvtpk(o[8] * il, o[9] * il), cvtpk(o[10] * il, o[11] * il)});
  TS();
  wait_vm<0>();
  if (lds_add(lds + K2_FL + 4, lane) == 7) {
    if (lane < (32 >> CSH)) cnt_add(ws, (c * 32 >> CSH) + lane);
    cnt_poll_post(ws, ((blockIdx.x & 7) * 32 + (blockIdx.x >> 3)) >> CSH, 32, lds + K2_FL + 12);
  }
  if (w == 7 || w == 0) { TS_END(w == 7 ? 1 : 5); }
}

extern "C" __global__ __launch_bounds__(768, 3) void k_attn(char* __restrict__ ws, const u16* __restrict__ x, const u16* __restrict__ wg, const u16* __restrict__ wu, const u16* __restrict__ wd) {
  extern __shared__ __attribute__((aligned(16))) char smem[];
  const int lane = threadIdx.x & 63, w = __builtin_amdgcn_readfirstlane(threadIdx.x >> 6);
  const int xcd = blockIdx.x & 7, idx = blockIdx.x >> 3, h = xcd * 4 + (idx >> 3), c = idx & 7;
  if (w < 4) attn_helper((ldsp)smem, ws, c, h, w, lane, x, wg, wu, wd);
  else attn_body((ldsp)smem, ws, c, h, w - 4, lane);
}

constexpr int K4_NS = 4, K4_STG = 20480, K4_NT = 20, K4_RING = 61440, K4_CTL = K4_RING + K4_NS * K4_STG, K4_SD = K4_CTL + 4096, K4_LDS = K4_SD + 8192 + 8192;
static_assert(K4_LDS <= 163840, "LDS map");
constexpr float GC0 = -2.f * 0.7978845608028654f * 1.4426950408889634f, GC1 = GC0 * 0.044715f;

#define SWAPW 0
extern "C" __global__ __launch_bounds__(512, 2) void k_mlp(char* __restrict__ ws, u16* __restrict__ out) {
  extern __shared__ __attribute__((aligned(16))) char smem[];
  ldsp lds = (ldsp)smem + K4_RING;
  ldsp ctl = (ldsp)smem + K4_CTL;
  const int lane = threadIdx.x & 63, w = __builtin_amdgcn_readfirstlane(threadIdx.x >> 6) ^ SWAPW;
  const unsigned lds00 = (unsigned)(uintptr_t)(ldsp)smem, lds0 = lds00 + K4_RING;
  const int xcd = blockIdx.x & 7, idx = blockIdx.x >> 3, mt = (xcd >> 2) * 8 + (idx >> 2), ft = (xcd & 3) * 4 + (idx & 3);
  const int mt5 = (xcd >> 1) * 8 + (idx >> 2), ct = (xcd & 1) * 4 + (idx & 3);
  const int odd = ct & 1, pb = (ct >> 1) * 5 + (odd ? 3 : 0), ps = (ct >> 1) * 5 + 2;
  if (w >= 4) {
    const int lw = w - 4;
    const char* wd = ws + WS_WD + ct * 245760;
    ldsp sd = (ldsp)smem + K4_SD + lw * 4096;
    auto front = [] {};
    if (w < 6) {
      const char* src = ws + WS_H + ((mt & 1) * 40 + lw) * PLANE + (mt >> 1) * 4096;
      const char* st = ws + WS_ST + mt * 4096 + lw * 2048;
      loader_loop<4, K4_NT, K4_NS, 4>([&](int slot) { front(); dma_run<4>(src, lds0 + lw * 4096 + slot * K4_STG, lane * 16); src += 2 * PLANE; },
                                      [&] { dma_run<2>(st, lds00 + K4_SD + lw * 4096, lane * 16); dma_run<2>(st + S_ * 32, lds00 + K4_SD + lw * 4096 + 2048, lane * 16); });
      wait_vm<0>();
      const LAS f32x4* sp = (const LAS f32x4*)(sd + lane * 32);
      const f32x4 v[4] = {sp[0], sp[1], sp[128], sp[129]};
      const float mu = (((v[0][0] + v[0][2]) + (v[1][0] + v[1][2])) + ((v[2][0] + v[2][2]) + (v[3][0] + v[3][2]))) * 0.125f;
      float m2 = 0.f;
#pragma unroll
      for (int e = 0; e < 4; ++e) { const float d0 = v[e][0] - mu, d1 = v[e][2] - mu; m2 += (v[e][1] + v[e][3]) + 80.f * (d0 * d0 + d1 * d1); }
      const float var = m2 * (1.f / D_) + LN_EPS, rstd = __builtin_amdgcn_rsqf(var);
      ((LAS f32x4*)(ctl + 2048))[lw * 64 + lane] = (f32x4){-mu, GC0 * rstd, GC1 * rstd * rstd * rstd, var};
    } else {
      const char* src = ws + WS_WGU + ft * K4_NT * 12288 + (w - 6) * 6144;
      loader_loop<6, K4_NT, K4_NS, 4>([&](int slot) { front(); dma_run<6>(src, lds0 + 8192 + (w - 6) * 6144 + slot * K4_STG, lane * 16); src += 12288; },
                                      [&] { dma_run<4>(ws + WS_CS + ft * 8192 + (w - 6) * 4096, lds00 + K4_SD + lw * 4096, lane * 16); });
      wait_vm<0>();
      const int e = lane < 48 ? lane : 47;
      const LAS f32x2* cp = (const LAS f32x2*)sd + e;
      ((LAS f32x2*)(ctl + 1024))[(w - 6) * 48 + e] = (((cp[0] + cp[48]) + (cp[96] + cp[144])) + ((cp[192] + cp[240]) + (cp[288] + cp[336]))) + (cp[384] + cp[432]);
    }
    asm volatile("s_waitcnt lgkmcnt(0)" ::: "memory");
    lds_post(ctl + 48 + lw * 4, 1, lane);
    dma_long<15>(wd + lw * 15360, lds00 + lw * 15360, lane * 16);
    {
      set_m0(lds00 + K4_SD + lw * 4096);
      const char* hs = ws + WS_H + ((mt5 >> 1) & 1) * 40 * PLANE + (mt5 >> 2) * 4096 + ((mt5 & 1) * 2 + (lw >> 1)) * 1024;
      const unsigned vo = (lane >> 5) * PLANE + (((lane >> 4) & 1) * 32 + (lw & 1) * 16 + (lane & 15)) * 16 + 2048;
      dma<0>(hs + pb * 2 * PLANE - 2048, vo); dma<1024>(hs + (pb + 1) * 2 * PLANE - 3072, vo); dma<2048>(hs + ps * 2 * PLANE - 4096, vo);
    }
    lds_wait(ctl, 4);
    dma_long<20>(wd + 61440 + lw * 20480, lds0 + lw * 20480, lane * 16);
    if (lw == 0) {
      lds_wait(ctl + 4, 2); lds_wait(ctl + 8, 2);
      cnt_poll_post(ws, 256 + ((mt5 * 8 + ct) >> CSH), 16, ctl + 12);
    }
    wait_vm_bar<0>();
#pragma unroll 1
    for (int g = 0; g < 5; ++g) {
      lds_wait4(ctl + 16, g + 1);
      dma_run<5>(wd + 143360 + g * 20480 + lw * 5120, lds00 + g * 20480 + lw * 5120, lane * 16);
      wait_vm<0>();
      lds_post(ctl + 32 + lw * 4, g + 1, lane);
    }
    return;
  }
  if (w == 0 && lane < 16) ((LAS unsigned*)ctl)[lane] = 0u;
  const int r32 = lane & 31, hi = lane >> 5, wm = w >> 1, wn = w & 1;
  TS_DECL;
  f32x16 acc[2][3];
#pragma unroll
  for (int i = 0; i < 2; ++i)
#pragma unroll
    for (int j = 0; j < 3; ++j) { acc[i][j] = (f32x16){}; pin(acc[i][j]); }
  bf16x8 af[2][2], wf[2][3];
  const int aoff = wm * 2048 + lane * 16, woff = 8192 + wn * 3072 + lane * 16;
  auto rda = [&](ldsp sb, int s, int i) { af[s][i] = lds_ld16(sb + aoff + s * 4096 + i * 1024); };
  auto rdw = [&](ldsp sb, int s, int j) { wf[s][j] = lds_ld16(sb + woff + s * 6144 + j * 1024); };
  auto mm = [&](int s, int i, int j) { acc[i][j] = mfma32(wf[s][j], af[s][i], acc[i][j]); };
  bar();
  const int tok = mt * 128 + wm * 64 + r32;
  TS();
  rda(lds, 0, 0); rdw(lds, 0, 0); rda(lds, 0, 1); rdw(lds, 0, 1); rdw(lds, 0, 2); SB0();
  f32x4 rc[2], cq[3][4];
  const LAS float* csb = (const LAS float*)(ctl + 1024) + wn * 96 + 4 * hi;
  sfor<0, K4_NT>([&](auto tc) {
    constexpr int t = decltype(tc)::value;
    constexpr bool more = t + 1 < K4_NT;
    ldsp sb = lds + (t % K4_NS) * K4_STG;
    ldsp nb = lds + ((t + 1) % K4_NS) * K4_STG;
    mm(0, 0, 0); rda(sb, 1, 0); rdw(sb, 1, 0); SB0();
    mm(0, 1, 0); rda(sb, 1, 1); rdw(sb, 1, 1); SB0();
    mm(0, 0, 1); rdw(sb, 1, 2); SB0();
    mm(0, 1, 1); SB0();
    mm(0, 0, 2); SB0();
    mm(0, 1, 2); SB0();
    if constexpr (t >= PA && t < PB) { __builtin_amdgcn_s_sleep(PN); SB0(); }
    if constexpr (more) { bar(); SB0(); }
    if constexpr (!more) { asm volatile("s_waitcnt lgkmcnt(0)" ::: "memory"); lds_add(ctl, lane); SB0(); }
    mm(1, 0, 0); if (more) { rda(nb, 0, 0); rdw(nb, 0, 0); } SB0();
    mm(1, 1, 0); if (more) { rda(nb, 0, 1); rdw(nb, 0, 1); } SB0();
    mm(1, 0, 1); if (more) rdw(nb, 0, 2); SB0();
    if constexpr (!more) {
      lds_wait4(ctl + 48, 1);
      rc[0] = ((const LAS f32x4*)(ctl + 2048))[wm * 64 + r32]; rc[1] = ((const LAS f32x4*)(ctl + 2048))[wm * 64 + 32 + r32];
#pragma unroll
      for (int j = 0; j < 3; ++j)
#pragma unroll
        for (int b = 0; b < 4; ++b) cq[j][b] = *(const LAS f32x4*)(csb + j * 32 + 8 * b);
    }
    mm(1, 1, 1); SB0();
    mm(1, 0, 2); SB0();
    mm(1, 1, 2); SB0();
  });
  TS();
  const rsrc_t rw = mk_rsrc(ws, WS_END);
#pragma unroll
  for (int i = 0; i < 2; ++i) {
    const int tk = tok + 32 * i;
    const int ob = WS_ACT + (((tk >> 6) * 48 + ft * 3) * 4 + ((tk >> 4) & 3)) * 1024 + (hi * 16 + (tk & 15)) * 16;
#pragma unroll
    for (int j = 0; j < 3; ++j) {
      unsigned pk[4];
#pragma unroll
      for (int r = 0; r < 8; r += 2) {
        const f32x2 gv = (f32x2){acc[i][j][r], acc[i][j][r + 1]} + rc[i][0] * (f32x2){cq[j][r >> 2][r & 3], cq[j][r >> 2][(r & 3) + 1]};
        const f32x2 uv = (f32x2){acc[i][j][8 + r], acc[i][j][9 + r]} + rc[i][0] * (f32x2){cq[j][2 + (r >> 2)][r & 3], cq[j][2 + (r >> 2)][(r & 3) + 1]};
        const f32x2 arg = gv * (gv * gv * rc[i][2] + rc[i][1]);
        const f32x2 ex = {__builtin_amdgcn_exp2f(arg[0]), __builtin_amdgcn_exp2f(arg[1])};
        const f32x2 den = ex * rc[i][3] + rc[i][3];
        const f32x2 ri = {__builtin_amdgcn_rcpf(den[0]), __builtin_amdgcn_rcpf(den[1])};
        const f32x2 a = gv * uv * ri;
        pk[r >> 1] = cvtpk(a[0], a[1]);
      }
      const int T = wn * 3 + j;
      st16<AUX4>(rw, ob + (T >> 1) * 4096 + (T & 1) * 512, (u32x4){pk[0], pk[1], pk[2], pk[3]});
    }
  }
  TS();
  wait_vm<0>();
  if (lds_add(ctl + 4 + wm * 4, lane) == 1 && lane < (8 >> CSH)) cnt_add(ws, 256 + ((mt * 2 + wm) * 8 >> CSH) + lane);
  TS();
  lds_wait(ctl + 12, 1);
  TS();
  constexpr int WIN = 2 * WIN5;
  bf16x8 a5[WIN], w5[3][5];
  const int ao = WS_ACT + mt5 * 196608 + w * 1024 + lane * 16;
#pragma unroll
  for (int ks = 0; ks < WIN; ++ks) a5[ks] = ld16_sc1(rw, ao, ks * 4096);
  SB0();
  bar();
  TS();
  f32x4 ac5[5];
  {
    ldsp rp = (ldsp)smem + K4_SD + w * 4096 + lane * 16;
#pragma unroll
    for (int j = 0; j < 5; ++j) {
      const int e = odd ? (j == 0 ? 5 : j - 1) : j;
      const u32x2 r = *(const LAS u32x2*)(rp + (e >> 1) * 1024 + (e & 1) * 8);
      ac5[j] = (f32x4){bflo(r[0]), bfhi(r[0]), bflo(r[1]), bfhi(r[1])};
    }
  }
  ldsp wp = (ldsp)smem + lane * 16;
#pragma unroll
  for (int j = 0; j < 10; ++j) w5[j / 5][j % 5] = lds_ld16(wp + j * 1024);
  sfor<0, 48>([&](auto kc) {
    constexpr int ks = decltype(kc)::value, kn = ks + 2, tn = kn / 2;
    if constexpr ((kn & 3) == 0 && kn >= 28 && kn < 48) { lds_wait4(ctl + 32, (kn - 28) / 4 + 1); SB0(); }
    if constexpr ((kn & 1) == 0 && (tn == REL0 || tn == REL1 || tn == REL2 || tn == REL3 || tn == REL4)) {
      asm volatile("" ::: "memory");
      lds_post(ctl + 16 + w * 4, tn == REL0 ? 1 : tn == REL1 ? 2 : tn == REL2 ? 3 : tn == REL3 ? 4 : 5, lane);
    }
#pragma unroll
    for (int j = 0; j < 5; ++j) {
      if constexpr (kn < 48) w5[kn % 3][j] = lds_ld16(wp + (kn % 28) * 5120 + j * 1024);
      ac5[j] = mfma16(w5[ks % 3][j], a5[ks % WIN], ac5[j]);
    }
    if constexpr (ks + WIN < 48) a5[ks % WIN] = ld16_sc1(rw, ao, (ks + WIN) * 4096);
    SB0();
  });
  TS();
  {
    const int g = lane >> 4;
    u32x2 hv[5];
#pragma unroll
    for (int j = 0; j < 5; ++j) hv[j] = (u32x2){cvtpk(ac5[j][0], ac5[j][1]), cvtpk(ac5[j][2], ac5[j][3])};
    const rsrc_t ro = mk_rsrc(out, S_ * D_ * 2);
    const int oo = (mt5 * 64 + w * 16 + (lane & 15)) * 1280 + ct * 160;
#pragma unroll
    for (int jp = 0; jp < 2; ++jp) {
      auto r0 = __builtin_amdgcn_permlane16_swap(hv[2 * jp][0], hv[2 * jp + 1][0], false, false);
      auto r1 = __builtin_amdgcn_permlane16_swap(hv[2 * jp][1], hv[2 * jp + 1][1], false, false);
      st16<AUX5>(ro, oo + jp * 64 + (g & 1) * 32 + (g >> 1) * 16, (u32x4){r0[0], r1[0], r0[1], r1[1]});
    }
    st8<AUX5>(ro, oo + 128 + g * 8, hv[4]);
  }
  TS();
  if (w == 0) { TS_END(3); }
}

extern "C" __global__ void k_text_end() {}

extern "C" void kernel_launch(void* const* d_in, const int* in_sizes, int n_in, void* d_out, int out_size, void* d_ws, size_t ws_size,
                              hipStream_t stream) {
  const u16* x = (const u16*)d_in[0];
  const u16* wqkv = (const u16*)d_in[1];
  const u16* wo = (const u16*)d_in[2];
  const u16* wg = (const u16*)d_in[4];
  const u16* wu = (const u16*)d_in[5];
  const u16* wd = (const u16*)d_in[6];
  char* ws = (char*)d_ws;
  u16* out = (u16*)d_out;
  (void)hipFuncSetAttribute((const void*)k_qkv, hipFuncAttributeMaxDynamicSharedMemorySize, K1_LDS);
  (void)hipFuncSetAttribute((const void*)k_attn, hipFuncAttributeMaxDynamicSharedMemorySize, K2_LDS);
  (void)hipFuncSetAttribute((const void*)k_mlp, hipFuncAttributeMaxDynamicSharedMemorySize, K4_LDS);
  k_qkv<<<256, 768, K1_LDS, stream>>>(x, wqkv, wo, wd, ws);
  k_attn<<<256, 768, K2_LDS, stream>>>(ws, x, wg, wu, wd);
  k_mlp<<<256, 512, K4_LDS, stream>>>(ws, out);
}
```

```cpp
#include <hip/hip_runtime.h>
#include <stdint.h>
#include <type_traits>

#define LAS __attribute__((address_space(3)))
#define DI __device__ __forceinline__
typedef unsigned short u16;
typedef short bf16x8 __attribute__((ext_vector_type(8)));
typedef short bf16x4 __attribute__((ext_vector_type(4)));
typedef __bf16 bf16x2_t __attribute__((ext_vector_type(2)));
typedef float f32x2 __attribute__((ext_vector_type(2)));
typedef float f32x4 __attribute__((ext_vector_type(4)));
typedef float f32x16 __attribute__((ext_vector_type(16)));
typedef unsigned u32x4 __attribute__((ext_vector_type(4)));
typedef unsigned u32x2 __attribute__((ext_vector_type(2)));
typedef LAS char* ldsp;
typedef __amdgpu_buffer_rsrc_t rsrc_t;

constexpr int S_ = 2048, D_ = 640, DF = 1536;
constexpr float LN_EPS = 1e-5f;
constexpr float QSCALE = 0.22360679774997896f * 1.4426950408889634f;
constexpr unsigned ONES2 = 0x3f803f80u;

constexpr int PLANE = S_ * 16;
constexpr int WS_QK = 0;
constexpr int WS_ACT = WS_QK + 80 * PLANE;
constexpr int WS_H = WS_ACT + S_ * DF * 2;
constexpr int WS_WO = WS_H + S_ * D_ * 2;
constexpr int WS_ST = WS_WO + D_ * D_ * 2;
constexpr int WS_CS = WS_ST + S_ * 8 * 8;
constexpr int WS_WGU = WS_CS + 16 * 8192;
constexpr int WS_WD = WS_WGU + D_ * DF * 4;
static_assert(WS_WO >= 0 && WS_QK % 4096 == 0, "workspace map");
constexpr int WS_CNT = WS_WD + D_ * DF * 2;
constexpr int CSTR = 64;
constexpr int WS_END = WS_CNT + 512 * CSTR * 4;


template <int I, int N, class F> DI void sfor(F&& f) {
  if constexpr (I < N) { f(std::integral_constant<int, I>{}); sfor<I + 1, N>(f); }
}
#define SB0() __builtin_amdgcn_sched_barrier(0)
DI unsigned cvtpk(float lo, float hi) { f32x2 v = {lo, hi}; return __builtin_bit_cast(unsigned, __builtin_convertvector(v, bf16x2_t)); }
DI float bflo(unsigned u) { return __uint_as_float(u << 16); }
DI float bfhi(unsigned u) { return __uint_as_float(u & 0xffff0000u); }
DI f32x4 mfma16(bf16x8 a, bf16x8 b, f32x4 c) { return __builtin_amdgcn_mfma_f32_16x16x32_bf16(a, b, c, 0, 0, 0); }
DI f32x16 mfma32(bf16x8 a, bf16x8 b, f32x16 c) { return __builtin_amdgcn_mfma_f32_32x32x16_bf16(a, b, c, 0, 0, 0); }
DI void set_m0(unsigned v) { asm volatile("s_mov_b32 m0, %0\n\ts_nop 0" ::"s"(v) : "memory"); }
template <int OFF> DI void dma(const void* sbase, unsigned voff) {
  asm volatile("global_load_lds_dwordx4 %0, %1 offset:%2" ::"v"(voff), "s"(sbase), "n"(OFF) : "memory");
}
template <int OFF> DI void dma_nt(const void* sbase, unsigned voff) {
  asm volatile("global_load_lds_dwordx4 %0, %1 offset:%2 nt" ::"v"(voff), "s"(sbase), "n"(OFF) : "memory");
}
template <int N> DI void wait_vm() { asm volatile("s_waitcnt vmcnt(%0)" ::"n"(N) : "memory"); }
template <int N> DI void wait_vm_bar() { asm volatile("s_waitcnt vmcnt(%0) lgkmcnt(0)\n\ts_barrier" ::"n"(N) : "memory"); }
DI void bar() { asm volatile("s_waitcnt lgkmcnt(0)\n\ts_barrier" ::: "memory"); }
template <class T> DI void pin(T& v) { asm volatile("" : "+v"(v)); }
DI bf16x8 lds_ld16(ldsp p) { return *(const LAS bf16x8*)p; }
DI bf16x4 lds_tr(ldsp p) { return __builtin_amdgcn_ds_read_tr16_b64_v4i16((LAS bf16x4*)p); }
DI bf16x8 lds_tr2(ldsp p0, ldsp p1) {
  bf16x4 a = lds_tr(p0), b = lds_tr(p1);
  return __builtin_shufflevector(a, b, 0, 1, 2, 3, 4, 5, 6, 7);
}
DI float xsum32(float v) {
  auto r = __builtin_amdgcn_permlane32_swap(__float_as_uint(v), __float_as_uint(v), false, false);
  return __uint_as_float(r[0]) + __uint_as_float(r[1]);
}
DI float xsum16_32(float v) {
  auto r = __builtin_amdgcn_permlane16_swap(__float_as_uint(v), __float_as_uint(v), false, false);
  return xsum32(__uint_as_float(r[0]) + __uint_as_float(r[1]));
}
DI rsrc_t mk_rsrc(void* p, int bytes) { return __builtin_amdgcn_make_buffer_rsrc(p, 0, bytes, 0x00020000); }
template <int AUX = 16> DI void st16(rsrc_t r, int off, u32x4 v) { __builtin_amdgcn_raw_buffer_store_b128(v, r, off, 0, AUX); }
template <int AUX = 16> DI void st8(rsrc_t r, int off, u32x2 v) { __builtin_amdgcn_raw_buffer_store_b64(v, r, off, 0, AUX); }
constexpr int CSH = 2;

template <int P, int NT, int NS, int X, class F, class G> DI void loader_loop(F&& issue, G&& extra) {
  static_assert(NS >= 4, "three stages in flight + one being read");
  issue(0); issue(1);
  wait_vm_bar<P>();
  issue(2); issue(3);
  int slot = 4 % NS;
#pragma unroll 1
  for (int t = 1; t < NT - 3; ++t) {
    wait_vm_bar<2 * P>();
    issue(slot);
    slot = slot == NS - 1 ? 0 : slot + 1;
  }
  extra();
  wait_vm_bar<2 * P + X>(); wait_vm_bar<P + X>(); wait_vm_bar<X>();
}
DI void touch(const char* p, int lane, int nlines, unsigned lds_scratch) {
  set_m0(lds_scratch);
  asm volatile("global_load_lds_dword %0, %1" ::"v"((unsigned)((lane < nlines ? lane : nlines - 1) * 128)), "s"(p) : "memory");
}
#define CODE_ADDR(dst, sym) asm volatile("s_getpc_b64 s[40:41]\n\ts_add_u32 s40, s40, " #sym "@rel32@lo+4\n\ts_addc_u32 s41, s41, " #sym "@rel32@hi+12\n\ts_mov_b64 %0, s[40:41]" : "=s"(dst) :: "s40", "s41", "scc")
template <int P> DI void dma_run(const char* src, unsigned dst, unsigned lane16) {
  static_assert(P <= 8, "offset range");
  set_m0(dst + 4096);
  sfor<0, P>([&](auto ic) { dma<decltype(ic)::value * 1024 - 4096>(src + 4096, lane16); });
}
template <int N> DI void dma_long(const char* src, unsigned dst, unsigned lane16) {
  sfor<0, N / 5>([&](auto ic) { constexpr int i = decltype(ic)::value; dma_run<5>(src + i * 5120, dst + i * 5120, lane16); });
}
DI unsigned lds_add(ldsp p, int lane) {
  unsigned old = 0;
  if (lane == 0) old = __hip_atomic_fetch_add((LAS unsigned*)p, 1u, __ATOMIC_RELAXED, __HIP_MEMORY_SCOPE_WORKGROUP);
  return __builtin_amdgcn_readfirstlane(old);
}
DI void lds_wait(ldsp p, unsigned want) {
#pragma unroll 1
  for (int sp = 0; sp < (1 << 22); ++sp) {
    if ((unsigned)__builtin_amdgcn_readfirstlane(*(volatile LAS unsigned*)p) >= want) break;
    __builtin_amdgcn_s_sleep(1);
  }
  asm volatile("" ::: "memory");
}
DI void lds_post(ldsp p, unsigned v, int lane) { if (lane == 0) *(volatile LAS unsigned*)p = v; asm volatile("" ::: "memory"); }
DI void lds_wait4(ldsp p, unsigned want) {
#pragma unroll 1
  for (int sp = 0; sp < (1 << 22); ++sp) {
    const u32x4 v = *(volatile LAS u32x4*)p;
    if ((unsigned)__builtin_amdgcn_readfirstlane(min(min(v[0], v[1]), min(v[2], v[3]))) >= want) break;
    __builtin_amdgcn_s_sleep(1);
  }
  asm volatile("" ::: "memory");
}
DI void cnt_add(char* ws, int idx) { __hip_atomic_fetch_add((unsigned*)(ws + WS_CNT) + idx * CSTR, 1u, __ATOMIC_RELAXED, __HIP_MEMORY_SCOPE_AGENT); }
#define PP_CL "memory", "v100", "v101", "v102"
#define PP_ISSUE(R) asm volatile("s_mov_b64 exec, 1\n\tglobal_atomic_or " R ", %0, %0, %1 sc0\n\ts_mov_b64 exec, -1" ::"v"(0u), "s"(p) : PP_CL)
#define PP_CHECK(R) { unsigned v_; asm volatile("s_waitcnt vmcnt(2)\n\tv_readfirstlane_b32 %0, " R : "=s"(v_)::PP_CL); if (v_ >= want) break; }
DI void cnt_poll_post(char* ws, int idx, unsigned want, ldsp word) {
  unsigned* p = (unsigned*)(ws + WS_CNT) + idx * CSTR;
  PP_ISSUE("v100"); __builtin_amdgcn_s_sleep(4); PP_ISSUE("v101"); __builtin_amdgcn_s_sleep(4);
#pragma unroll 1
  for (int sp = 0; sp < (1 << 17); ++sp) { PP_ISSUE("v102"); PP_CHECK("v100"); PP_ISSUE("v100"); PP_CHECK("v101"); PP_ISSUE("v101"); PP_CHECK("v102"); }
  asm volatile("ds_write_b32 %0, %1\n\ts_waitcnt vmcnt(0)" ::"v"((unsigned)(uintptr_t)word), "v"(1u) : PP_CL);
}
DI bf16x8 ld16_sc1(rsrc_t r, int voff, int soff) { return __builtin_bit_cast(bf16x8, __builtin_amdgcn_raw_buffer_load_b128(r, voff, soff, 16)); }

template <int KIND> DI void pack_issue(const u16* wsrc, const u16* wsrc2, int ks, int cg, unsigned ldsdst, int lane) {
  constexpr int PITCH = KIND == 1 ? DF * 2 : D_ * 2;
  set_m0(ldsdst);
  sfor<0, 4>([&](auto ic) {
    constexpr int g = decltype(ic)::value;
    const int t = (lane >> 5) & 1, q = (lane >> 3) & 3;
    int row;
    if (KIND == 0) { const int P = 4 * ks + g, hp = P / 5, r = P - 5 * hp; row = 40 * hp + q + (r < 2 ? 8 * t + 4 * r : r == 2 ? 16 + 4 * t : 12 + 8 * t + 4 * r); }
    else if (KIND == 1) row = 32 * (ks >> 1) + 16 * t + 4 * (2 * (ks & 1) + (g & 1)) + q;
    else row = 32 * ks + 16 * (g >> 1) + 8 * t + 4 * (g & 1) + q;
    dma_nt<g * 1024>((const char*)(KIND == 1 && g >= 2 ? wsrc2 : wsrc) + cg * 128 - 4096, (unsigned)(row * PITCH + (lane & 7) * 16 + 4096 - g * 1024));
  });
}
template <int KIND> DI void pack_finish(ldsp u, rsrc_t rw, int ks, int cg, int lane, LAS float* cs) {
  const int q = (lane & 15) >> 2, p = lane & 3;
  ldsp a = KIND == 1 ? u + ((lane >> 4) & 1) * 2048 + (8 * (lane >> 5) + q) * 128 + p * 8 : u + (8 * (lane >> 4) + q) * 128 + p * 8;
#pragma unroll
  for (int i = 0; i < 4; ++i) {
    const int nt = 4 * cg + i;
    const u32x4 f = __builtin_bit_cast(u32x4, lds_tr2(a + i * 32, a + i * 32 + 512));
    if (KIND == 1) {
      const int ft = nt / 6, j = nt - ft * 6;
      st16(rw, WS_WGU + ((ft * 40 + ks) * 6 + j) * 1024 + lane * 16, f);
      const unsigned sv = (lane & 15) == ((lane >> 4) & 1) ? ONES2 : 0u;
      const f32x4 c = mfma16(__builtin_bit_cast(bf16x8, (u32x4){sv, sv, sv, sv}), __builtin_bit_cast(bf16x8, f), (f32x4){0.f, 0.f, 0.f, 0.f});
      if (lane < 16) { cs[i * 32 + lane] = c[0]; cs[i * 32 + 16 + lane] = c[1]; }
    } else {
      const int ct = nt / 5, j = nt - ct * 5;
      st16(rw, (KIND == 0 ? WS_WO : WS_WD) + ((ct * (KIND == 0 ? 20 : 48) + ks) * 5 + j) * 1024 + lane * 16, f);
    }
  }
}

constexpr int NWD_A = 352;
constexpr int K1_NS = 4, K1_STG = 32768, K1_NT = 10, K1_CTL = K1_NS * K1_STG, K1_LDS = K1_CTL + 4096;

DI void qkv_compute(ldsp lds, char* __restrict__ ws, int mt, int nt, int w, int lane) {
  const int wm = w >> 1, wn = w & 1, r32 = lane & 31, hi = lane >> 5, q = (lane & 15) >> 2, p = lane & 3, blk = (lane >> 4) & 1;
  ldsp ctl = lds + K1_CTL;
  int aoff[4], woff[2];
#pragma unroll
  for (int s = 0; s < 4; ++s) aoff[s] = (wm * 64 + r32) * 128 + (((2 * s + hi) ^ ((r32 >> 1) & 7)) * 16);
#pragma unroll
  for (int j = 0; j < 2; ++j) woff[j] = 16384 + (8 * hi + q) * 256 + (((wn * 2 + (j ^ wm)) ^ q) * 64) + blk * 32 + p * 8;
  f32x16 acc[2][2];
#pragma unroll
  for (int j = 0; j < 2; ++j)
#pragma unroll
    for (int i = 0; i < 2; ++i) { acc[j][i] = (f32x16){}; pin(acc[j][i]); }
  f32x4 cs = {};
  const unsigned sv = (lane & 15) == blk ? ONES2 : 0u;
  const bf16x8 sel = __builtin_bit_cast(bf16x8, (u32x4){sv, sv, sv, sv});
  bf16x8 xf[2][2], wf[2][2];
  auto rdx = [&](ldsp sb, int s, int i) { xf[s & 1][i] = lds_ld16(sb + aoff[s] + i * 4096); };
  auto rdw = [&](ldsp sb, int s, int j) { wf[s & 1][j] = lds_tr2(sb + woff[j] + s * 4096, sb + woff[j] + s * 4096 + 1024); };
  auto mm = [&](int s, int j, int i) { acc[j][i] = mfma32(wf[s & 1][j], xf[s & 1][i], acc[j][i]); };
  auto step = [&](ldsp nb, int s, int sn, bool rd) {
    cs = mfma16(sel, wf[s & 1][0], cs);
    mm(s, 0, 0); if (rd) { rdx(nb, sn, 0); rdw(nb, sn, 0); } SB0();
    mm(s, 0, 1); if (rd) { rdx(nb, sn, 1); rdw(nb, sn, 1); } SB0();
    mm(s, 1, 0); SB0();
    mm(s, 1, 1); SB0();
  };
  bar();
  ldsp sb = lds;
  rdx(sb, 0, 0); rdw(sb, 0, 0); rdx(sb, 0, 1); rdw(sb, 0, 1); SB0();
  int slot = 0;
#pragma unroll 1
  for (int t = 0; t < K1_NT - 1; ++t) {
    step(sb, 0, 1, true); step(sb, 1, 2, true); step(sb, 2, 3, true);
    slot = slot == K1_NS - 1 ? 0 : slot + 1;
    sb = lds + slot * K1_STG;
    bar(); SB0();
    step(sb, 3, 0, true);
  }
  step(sb, 0, 1, true); step(sb, 1, 2, true); step(sb, 2, 3, true);
  const LAS f32x2* rst = (const LAS f32x2*)(ctl + 1024);
  LAS float* cst = (LAS float*)(ctl + 2048);
  cs = mfma16(sel, wf[1][0], cs);
  mm(3, 0, 0); mm(3, 0, 1); SB0();
  if (lane < 16) { cst[wn * 64 + wm * 32 + lane] = cs[0]; cst[wn * 64 + wm * 32 + 16 + lane] = cs[1]; }
  mm(3, 1, 0); mm(3, 1, 1); SB0();
  bar();
  const rsrc_t rw = mk_rsrc(ws, WS_END);
  const float sc = nt < 5 ? QSCALE : 1.f;
  f32x4 c4[2][4];
#pragma unroll
  for (int j = 0; j < 2; ++j)
#pragma unroll
    for (int b = 0; b < 4; ++b) c4[j][b] = *(const LAS f32x4*)(cst + wn * 64 + (j ^ wm) * 32 + 8 * b + 4 * hi);
#pragma unroll
  for (int i = 0; i < 2; ++i) {
    const int ml = wm * 64 + i * 32 + r32;
    const f32x2 st = rst[ml];
    const float a = st.y * sc, nm = -st.x * a;
    const int ob = WS_QK + ((nt * 16 + wn * 8 + hi) * S_ + mt * 128 + ml) * 16;
#pragma unroll
    for (int j = 0; j < 2; ++j)
#pragma unroll
      for (int bp = 0; bp < 2; ++bp) {
        unsigned pk[2][2];
#pragma unroll
        for (int e = 0; e < 2; ++e) {
          const int b = 2 * bp + e;
          const f32x2 v0 = (f32x2){acc[j][i][4 * b], acc[j][i][4 * b + 1]} * a + nm * (f32x2){c4[j][b][0], c4[j][b][1]};
          const f32x2 v1 = (f32x2){acc[j][i][4 * b + 2], acc[j][i][4 * b + 3]} * a + nm * (f32x2){c4[j][b][2], c4[j][b][3]};
          pk[e][0] = cvtpk(v0[0], v0[1]); pk[e][1] = cvtpk(v1[0], v1[1]);
        }
        auto r0 = __builtin_amdgcn_permlane32_swap(pk[0][0], pk[1][0], false, false);
        auto r1 = __builtin_amdgcn_permlane32_swap(pk[0][1], pk[1][1], false, false);
        st16(rw, ob + ((j ^ wm) * 4 + bp * 2) * S_ * 16, (u32x4){r0[0], r1[0], r0[1], r1[1]});
      }
  }
}

DI void qkv_stats(ldsp lds, int sw, int lane, const char* wo_part) {
  const int fr = lane & 15, g = lane >> 4;
  const int off = (sw * 32 + fr) * 128, x = fr >> 1;
  const bf16x8 ones = __builtin_bit_cast(bf16x8, (u32x4){ONES2, ONES2, ONES2, ONES2});
  f32x4 sm[2] = {}, sq[2] = {};
  int slot = 0;
  __builtin_amdgcn_s_setprio(2);
#pragma unroll 1
  for (int t = 0; t < K1_NT; ++t) {
    bar();
    ldsp sb = lds + slot * K1_STG + off;
    slot = slot == K1_NS - 1 ? 0 : slot + 1;
    if (t == 8) touch(wo_part, lane, 58, (unsigned)(uintptr_t)lds + K1_CTL + 3072);
    bf16x8 v[2][2];
#pragma unroll
    for (int ks = 0; ks < 2; ++ks)
#pragma unroll
      for (int i = 0; i < 2; ++i) v[ks][i] = lds_ld16(sb + i * 2048 + (((4 * ks + g) ^ x) * 16));
#pragma unroll
    for (int ks = 0; ks < 2; ++ks)
#pragma unroll
      for (int i = 0; i < 2; ++i) { sq[i] = mfma16(v[ks][i], v[ks][i], sq[i]); sm[i] = mfma16(ones, v[ks][i], sm[i]); }
  }
#pragma unroll
  for (int i = 0; i < 2; ++i) {
    const int p = lane & 3;
    const float q = p == 0 ? sq[i][0] : p == 1 ? sq[i][1] : p == 2 ? sq[i][2] : sq[i][3];
    const float mu = sm[i][0] * (1.f / D_), var = fmaxf(q * (1.f / D_) - mu * mu, 0.f);
    if ((fr >> 2) == g) ((LAS f32x2*)(lds + K1_CTL + 1024))[sw * 32 + i * 16 + fr] = (f32x2){mu, __builtin_amdgcn_rsqf(var + LN_EPS)};
  }
  bar();
}

DI void qkv_loader(ldsp lds, const u16* __restrict__ x, const u16* __restrict__ wqkv, int mt, int nt, int lw, int lane) {
  const bool isx = lw < 2;
  const int half = lw & 1;
  unsigned voff[8];
#pragma unroll
  for (int i = 0; i < 8; ++i) {
    const int r = (half * 8 + i) * 8 + (lane >> 3), c = (lane & 7) ^ ((r >> 1) & 7);
    const int k = (half * 8 + i) * 4 + (lane >> 4), cp = lane & 15;
    voff[i] = (isx ? r * 1280 + c * 16 : k * 3840 + (cp ^ ((k & 3) << 2)) * 16) + 8192 - i * 1024;
  }
  const char* src = (isx ? (const char*)x + (size_t)mt * 128 * 1280 : (const char*)wqkv + nt * 256) - 4096;
  const int sstr = isx ? 128 : 64 * 3840;
  const unsigned dst = (unsigned)(uintptr_t)lds + lw * 8192 + 4096;
  loader_loop<8, K1_NT, K1_NS, 0>([&](int slot) {
    set_m0(dst + slot * K1_STG);
    sfor<0, 8>([&](auto ic) { constexpr int i = decltype(ic)::value; dma<i * 1024 - 4096>(src, voff[i]); });
    src += sstr;
  }, [] {});
}

extern "C" __global__ __launch_bounds__(768, 3) void k_qkv(const u16* __restrict__ x, const u16* __restrict__ wqkv, const u16* __restrict__ wo, const u16* __restrict__ wd, char* __restrict__ ws) {
  extern __shared__ __attribute__((aligned(16))) char smem[];
  ldsp lds = (ldsp)smem;
  const int lane = threadIdx.x & 63, w = __builtin_amdgcn_readfirstlane(threadIdx.x >> 6);
  const int xcd = blockIdx.x & 7, idx = blockIdx.x >> 3;
  const int nt = (xcd & 1) * 8 + (idx >> 2), mt = (xcd >> 1) * 4 + (idx & 3);
  if (w < 4 && nt < 15) { qkv_loader(lds, x, wqkv, mt, nt, w, lane); return; }
  if (w == 11 && lane < 2) ((unsigned*)(ws + WS_CNT))[(blockIdx.x * 2 + lane) * CSTR] = 0u;
  if (w == 10) {
    const char *c0, *c1;
    CODE_ADDR(c0, k_qkv); CODE_ADDR(c1, k_text_end);
    const unsigned lim = c1 > c0 ? (unsigned)(c1 - c0) : 0u, off = (idx * 16 + (lane & 15)) * 128;
    set_m0((unsigned)(uintptr_t)lds + K1_CTL + 3072 + 512);
    asm volatile("global_load_lds_dword %0, %1" ::"v"(off < lim ? off : lim), "s"(c0) : "memory");
  }
  if (w == 8 || w == 9) {
    const int id = blockIdx.x * 64 + lane, u = w - 8;
    const bool isx = blockIdx.x < 128;
    const int i2 = id - 8192 < 7680 ? id - 8192 : 7679;
    const unsigned off = isx ? (id >> 2) * 1280 + 256 + u * 512 + (id & 3) * 128 : (128 + u * 256) * 3840 + i2 * 128;
    set_m0((unsigned)(uintptr_t)lds + K1_CTL + 3072 + u * 256);
    const char* src = isx ? (const char*)x : (const char*)wqkv;
    asm volatile("" : "+s"(src));
    if (u == 0) __builtin_amdgcn_s_sleep(8); else __builtin_amdgcn_s_sleep(24);
    asm volatile("global_load_lds_dword %0, %1" ::"v"(off), "s"(src) : "memory");
  }
  if (nt == 15) {
    const rsrc_t rw = mk_rsrc(ws, WS_END);
    constexpr int NU = (200 + NWD_A + 191) / 192;
    auto iss = [&](int r) {
      const int u = min(mt * 12 + w + r * 192, 200 + NWD_A - 1), v = u - 200;
      if (u < 200) pack_issue<0>(wo, wo, u / 10, u % 10, (unsigned)(uintptr_t)lds + (w * 2 + (r & 1)) * 4096, lane);
      else pack_issue<2>(wd, wd, v / 10, v % 10, (unsigned)(uintptr_t)lds + (w * 2 + (r & 1)) * 4096, lane);
    };
    iss(0); iss(1);
#pragma unroll 1
    for (int r = 0; r < NU; ++r) {
      if (r == 0) wait_vm<4>(); else if (r + 1 < NU) wait_vm<8>(); else wait_vm<4>();
      const int u = min(mt * 12 + w + r * 192, 200 + NWD_A - 1), v = u - 200;
      if (u < 200) pack_finish<0>(lds + (w * 2 + (r & 1)) * 4096, rw, u / 10, u % 10, lane, nullptr);
      else pack_finish<2>(lds + (w * 2 + (r & 1)) * 4096, rw, v / 10, v % 10, lane, nullptr);
      asm volatile("s_waitcnt lgkmcnt(0)" ::: "memory");
      if (r + 2 < NU) iss(r + 2);
    }
    return;
  }
  if (w >= 8) qkv_stats(lds, w - 8, lane, ws + WS_WO + min((idx * 4 + w - 8) * 58, 6400 - 58) * 128);
  else qkv_compute(lds, ws, mt, nt, w - 4, lane);
}

constexpr float LAZY_LIM = 1.0995116e12f;
constexpr int K2_K = 102400, K2_V = K2_K + 24576, K2_VP = 8192 + 64, K2_CS = K2_V + 4 * K2_VP, K2_MSK = K2_CS + 2560, K2_FL = K2_MSK + 832, K2_LDS = K2_FL + 64 + 256;
static_assert(K2_LDS <= 163840 && K2_FL % 16 == 0, "LDS map");

DI void n80_epilogue_h(f32x4 (&acc)[5], rsrc_t rw, int mt, int ct, int w, int lane) {
  const int fr = lane & 15, g = lane >> 4, tok = mt * 64 + w * 16 + fr;
  const int odd = ct & 1, pb = (ct >> 1) * 5 + (odd ? 3 : 0), ps = (ct >> 1) * 5 + 2;
  const int hb = WS_H + (((mt >> 1) & 1) * 40 + (g >> 1)) * PLANE + (mt >> 2) * 4096 + ((mt & 1) * 2 + (w >> 1)) * 1024 + ((g & 1) * 32 + (w & 1) * 16 + fr) * 16;
  u32x2 hv[5];
  float s = 0.f;
#pragma unroll
  for (int j = 0; j < 5; ++j) hv[j] = (u32x2){cvtpk(acc[j][0], acc[j][1]), cvtpk(acc[j][2], acc[j][3])};
  const u32x2 a0 = odd ? hv[1] : hv[0], a1 = odd ? hv[2] : hv[1], b0 = odd ? hv[3] : hv[2], b1 = odd ? hv[4] : hv[3], s1 = odd ? hv[0] : hv[4];
  st16<0>(rw, hb + pb * 2 * PLANE, (u32x4){a0[0], a0[1], a1[0], a1[1]});
  st16<0>(rw, hb + (pb + 1) * 2 * PLANE, (u32x4){b0[0], b0[1], b1[0], b1[1]});
  st8<0>(rw, hb + ps * 2 * PLANE + odd * 8, s1);
  f32x4 v[5];
#pragma unroll
  for (int j = 0; j < 5; ++j) { v[j] = (f32x4){bflo(hv[j][0]), bfhi(hv[j][0]), bflo(hv[j][1]), bfhi(hv[j][1])}; s += (v[j][0] + v[j][1]) + (v[j][2] + v[j][3]); }
  const float mean = xsum16_32(s) * (1.f / 80.f);
  float m2 = 0.f;
#pragma unroll
  for (int j = 0; j < 5; ++j) { const f32x4 d = v[j] - mean; m2 += (d[0] * d[0] + d[1] * d[1]) + (d[2] * d[2] + d[3] * d[3]); }
  m2 = xsum16_32(m2);
  if (g == 0) st8<0>(rw, WS_ST + (((ct >> 2) * S_ + tok) * 4 + (ct & 3)) * 8, (u32x2){__float_as_uint(mean), __float_as_uint(m2)});
}

DI void attn_helper(ldsp lds, char* __restrict__ ws, int c, int h, int hw, int lane, const u16* __restrict__ x, const u16* __restrict__ wg, const u16* __restrict__ wu, const u16* __restrict__ wd) {
  const unsigned lds0 = (unsigned)(uintptr_t)lds;
  const int pm = 5 * (h >> 1) + 3 * (h & 1), pt = 5 * (h >> 1) + 2;
  const bool EDGE = c == 0;
  const int tok0 = 256 * c - 256 + 128 * hw + ((EDGE && hw < 2) ? 256 : 0);
  const char* kb = ws + WS_QK + (80 * S_ + tok0) * 16;
  {
    set_m0(lds0 + K2_K + hw * 2048); dma<0>(kb + pm * S_ * 16, lane * 16); dma<1024>(kb + pm * S_ * 16, lane * 16);
    set_m0(lds0 + K2_K + 8192 + hw * 2048); dma<0>(kb + (pm + 1) * S_ * 16, lane * 16); dma<1024>(kb + (pm + 1) * S_ * 16, lane * 16);
    set_m0(lds0 + K2_K + 16384 + hw * 2048); dma<0>(kb + pt * S_ * 16, lane * 16); dma<1024>(kb + pt * S_ * 16, lane * 16);
  }
  const int b = blockIdx.x, xcd = b & 7, idx = b >> 3;
  if (hw == 0 && lane < 8) *(LAS unsigned*)(lds + K2_FL + lane * 4) = 0u;
  {
    const unsigned tm = (EDGE && hw < 2 ? 0xff80u : 0u) | 0xbf800000u;
    *(LAS u32x4*)(lds + K2_V + 3 * K2_VP + hw * 2048 + lane * 16) = (u32x4){tm, 0xbf80bf80u, ONES2, ONES2};
    *(LAS u32x4*)(lds + K2_V + 3 * K2_VP + hw * 2048 + 1024 + lane * 16) = (u32x4){tm, 0xbf80bf80u, ONES2, ONES2};
  }
  if (hw == 1 && lane < 52) {
    const int d = lane - 20;
    u32x4 mk;
#pragma unroll
    for (int e = 0; e < 4; ++e) { const int k0 = ((2 * e) & 3) + 8 * (e >> 1); mk[e] = (k0 > d ? 0xffffu : 0u) | (k0 + 1 > d ? 0xffff0000u : 0u); }
    *(LAS u32x4*)(lds + K2_MSK + lane * 16) = mk;
  }
  wait_vm<3>();
  {
    const char* vb = kb + 80 * S_ * 16;
    set_m0(lds0 + K2_V + hw * 2048); dma<0>(vb + pm * S_ * 16, lane * 16); dma<1024>(vb + pm * S_ * 16, lane * 16);
    set_m0(lds0 + K2_V + K2_VP + hw * 2048); dma<0>(vb + (pm + 1) * S_ * 16, lane * 16); dma<1024>(vb + (pm + 1) * S_ * 16, lane * 16);
    set_m0(lds0 + K2_V + 2 * K2_VP + hw * 2048); dma<0>(vb + pt * S_ * 16, lane * 16); dma<1024>(vb + pt * S_ * 16, lane * 16);
  }
  const rsrc_t rw = mk_rsrc(ws, WS_END);
  const int mt = xcd * 4 + (idx >> 3), ct = idx & 7;
  const int q0 = hw * 25600;
  const int pcg = b % 24, pks = (b / 24) * 4 + hw, v0 = NWD_A + (b - 240) * 8 + hw * 2;
  wait_vm_bar<6>();
  if (b < 240) { pack_issue<1>(wg, wu, pks, pcg, lds0 + q0, lane); wait_vm<4>(); }
  else { pack_issue<2>(wd, wd, v0 / 10, v0 % 10, lds0 + q0, lane); pack_issue<2>(wd, wd, v0 / 10, v0 % 10 + 1, lds0 + q0 + 4096, lane); wait_vm<8>(); }
  if (lane == 0) *(volatile LAS unsigned*)(lds + K2_FL + 16 + hw * 4) = 1u;
  u32x4 xr[2]; u32x2 xs;
  {
    const char* xp = (const char*)x + (size_t)(mt * 64 + hw * 16) * 1280 + ct * 160;
    const int g = lane >> 4;
    const unsigned o16 = (lane & 15) * 1280 + (g & 1) * 32 + (g >> 1) * 16, o8 = (lane & 15) * 1280 + g * 8;
    asm volatile("global_load_dwordx4 %0, %1, %2" : "=v"(xr[0]) : "v"(o16), "s"(xp) : "memory");
    asm volatile("global_load_dwordx4 %0, %1, %2 offset:64" : "=v"(xr[1]) : "v"(o16), "s"(xp) : "memory");
    asm volatile("global_load_dwordx2 %0, %1, %2 offset:128" : "=v"(xs) : "v"(o8), "s"(xp) : "memory");
  }
  dma_long<15>(ws + WS_WO + ct * 102400 + q0 + 10240, lds0 + q0 + 10240, lane * 16);
  wait_vm<15>();
  pin(xr[0]); pin(xr[1]); pin(xs);
  if (b >= 240) {
    pack_finish<2>(lds + q0, rw, v0 / 10, v0 % 10, lane, nullptr); pack_finish<2>(lds + q0 + 4096, rw, v0 / 10, v0 % 10 + 1, lane, nullptr);
    asm volatile("s_waitcnt lgkmcnt(0)" ::: "memory");
  } else {
    LAS float* ct4 = (LAS float*)(lds + K2_CS);
    pack_finish<1>(lds + q0, rw, pks, pcg, lane, ct4 + hw * 128);
    asm volatile("s_waitcnt lgkmcnt(0)" ::: "memory");
    if (lds_add(lds + K2_FL, lane) == 3) {
      asm volatile("" ::: "memory");
      const LAS f32x2* c2 = (const LAS f32x2*)ct4 + lane;
      const f32x2 a = (c2[0] + c2[64]) + (c2[128] + c2[192]);
      const int e = pcg * 128 + lane * 2;
      const int hf = e / 96;
      st8(rw, WS_CS + hf * 4096 + ((b / 24) * 96 + e - hf * 96) * 4, __builtin_bit_cast(u32x2, a));
    }
  }
  dma_long<10>(ws + WS_WO + ct * 102400 + q0, lds0 + q0, lane * 16);
  if (hw == 3) {
    const char *c0, *c1;
    CODE_ADDR(c0, k_mlp); CODE_ADDR(c1, k_text_end);
    const unsigned lim = c1 > c0 ? (unsigned)(c1 - c0) : 0u, off = (idx * 8 + (lane & 7)) * 128;
    set_m0(lds0 + K2_FL + 64);
    asm volatile("global_load_lds_dword %0, %1" ::"v"(off < lim ? off : lim), "s"(c0) : "memory");
  }
  f32x4 acc[5];
#pragma unroll
  for (int jp = 0; jp < 2; ++jp) {
    auto r0 = __builtin_amdgcn_permlane16_swap(xr[jp][0], xr[jp][2], false, false);
    auto r1 = __builtin_amdgcn_permlane16_swap(xr[jp][1], xr[jp][3], false, false);
    acc[2 * jp] = (f32x4){bflo(r0[0]), bfhi(r0[0]), bflo(r1[0]), bfhi(r1[0])};
    acc[2 * jp + 1] = (f32x4){bflo(r0[1]), bfhi(r0[1]), bflo(r1[1]), bfhi(r1[1])};
  }
  acc[4] = (f32x4){bflo(xs[0]), bfhi(xs[0]), bflo(xs[1]), bfhi(xs[1])};
#pragma unroll
  for (int j = 0; j < 5; ++j) pin(acc[j]);
  wait_vm<0>();
  lds_add(lds + K2_FL + 8, lane);
  lds_wait(lds + K2_FL + 8, 4);
  lds_wait(lds + K2_FL + 12, 1);
  bf16x8 af[20], wf[3][5];
  const int ao = WS_QK + (lane >> 4) * PLANE + (mt * 64 + hw * 16 + (lane & 15)) * 16;
#pragma unroll
  for (int ks = 0; ks < 20; ++ks) af[ks] = ld16_sc1(rw, ao, ks * 4 * PLANE);
  SB0();
  ldsp wp = lds + lane * 16;
#pragma unroll
  for (int j = 0; j < 10; ++j) wf[j / 5][j % 5] = lds_ld16(wp + j * 1024);
#pragma unroll
  for (int ks = 0; ks < 20; ++ks) {
#pragma unroll
    for (int j = 0; j < 5; ++j) {
      if (ks + 2 < 20) wf[(ks + 2) % 3][j] = lds_ld16(wp + (ks + 2) * 5120 + j * 1024);
      acc[j] = mfma16(wf[ks % 3][j], af[ks], acc[j]);
    }
    SB0();
  }
  n80_epilogue_h(acc, rw, mt, ct, hw, lane);
}

template <bool EXACT> DI f32x16 attn_rows(ldsp lds, ldsp kp, ldsp ktp, ldsp vp, bf16x8 qm, u32x2 qtr, int dq, int hi) {
  const float NEG = -__builtin_inff();
  auto mx3 = [](float a, float b, float c) { return __builtin_elementwise_maximum(__builtin_elementwise_maximum(a, b), c); };
  auto qk = [&](int j, u32x2 q2, f32x16 cin) {
    return __builtin_amdgcn_mfma_f32_32x32x8bf16_1k(*(const LAS bf16x4*)(ktp + j * 512), __builtin_bit_cast(bf16x4, q2), mfma32(lds_ld16(kp + j * 512), qm, cin), 0, 0, 0);
  };
  f32x16 s0, s8;
#pragma unroll
  for (int r = 0; r < 16; ++r) { const bool le = (r & 3) + 8 * (r >> 2) <= dq; s8[r] = le ? 0.f : NEG; s0[r] = le ? NEG : 0.f; }
  if (!EXACT) {
    pin(s0); pin(s8); bar();
  }
  bf16x8 km[2]; bf16x4 kt[2];
  s8 = qk(8, qtr, s8); s0 = qk(0, qtr, s0);
  km[1] = lds_ld16(kp + 512); kt[1] = *(const LAS bf16x4*)(ktp + 512);
  km[0] = lds_ld16(kp + 1024); kt[0] = *(const LAS bf16x4*)(ktp + 1024);
  f32x16 sa = mfma32(km[1], qm, (f32x16){});
#pragma unroll
  for (int r = 0; r < 16; ++r) s0[r] = __builtin_elementwise_maximum(s0[r], s8[r]);
  float m = mx3(mx3(mx3(s0[0], s0[1], s0[2]), mx3(s0[3], s0[4], s0[5]), mx3(s0[6], s0[7], s0[8])), mx3(mx3(s0[9], s0[10], s0[11]), s0[12], s0[13]), __builtin_elementwise_maximum(s0[14], s0[15]));
  if (EXACT) {
#pragma unroll 1
    for (int j = 1; j < 8; ++j) {
      const f32x16 t = qk(j, qtr, (f32x16){});
#pragma unroll
      for (int r = 0; r < 16; r += 2) m = mx3(m, t[r], t[r + 1]);
    }
  }
  {
    auto rr = __builtin_amdgcn_permlane32_swap(__float_as_uint(m), __float_as_uint(m), false, false);
    m = __builtin_elementwise_maximum(__uint_as_float(rr[0]), __uint_as_float(rr[1]));
  }
  u32x2 q2 = qtr;
  {
    const unsigned u0 = __float_as_uint(m) & 0xffff0000u;
    const float r1 = m - __uint_as_float(u0);
    const unsigned u1 = __float_as_uint(r1) & 0xffff0000u;
    const float r2 = r1 - __uint_as_float(u1);
    if (hi) q2 = (u32x2){0x3f80u | u0, (u1 >> 16) | (__float_as_uint(r2) & 0xffff0000u)};
  }
  const bf16x4 qs = __builtin_bit_cast(bf16x4, q2);
  s0 = __builtin_amdgcn_mfma_f32_32x32x8bf16_1k(__builtin_bit_cast(bf16x4, hi ? (u32x2){0xbf800000u, 0xbf80bf80u} : (u32x2){0u, 0u}), qs, s0, 0, 0, 0);
  sa = __builtin_amdgcn_mfma_f32_32x32x8bf16_1k(kt[1], qs, sa, 0, 0, 0);
  f32x16 sb = __builtin_amdgcn_mfma_f32_32x32x8bf16_1k(kt[0], qs, mfma32(km[0], qm, (f32x16){}), 0, 0, 0);
  SB0();
  auto ex8 = [&](const f32x16& s, int ks) {
    u32x4 p;
#pragma unroll
    for (int e = 0; e < 4; ++e) p[e] = cvtpk(__builtin_amdgcn_exp2f(s[8 * ks + 2 * e]), __builtin_amdgcn_exp2f(s[8 * ks + 2 * e + 1]));
    return p;
  };
  auto vld = [&](int slot, int ks) { return lds_tr2(vp + slot * 512 + ks * 256, vp + slot * 512 + ks * 256 + 128); };
  f32x16 o = {};
  auto pv = [&](bf16x8 v, u32x4 p) { o = mfma32(v, __builtin_bit_cast(bf16x8, p), o); };
  u32x4 mk0, mk1;
  u32x4 pk[8][2];
  pk[0][0] = ex8(s0, 0); pk[0][1] = ex8(s0, 1);
  km[1] = lds_ld16(kp + 3 * 512); kt[1] = *(const LAS bf16x4*)(ktp + 3 * 512);
  pk[1][0] = ex8(sa, 0); pk[1][1] = ex8(sa, 1);
  SB0();
  bf16x8 vf[2][4];
  u32x4 vrdy;
  sfor<2, 8>([&](auto jc) {
    constexpr int j = decltype(jc)::value;
    f32x16& cur = (j & 1) ? sa : sb;
    f32x16& nxt = (j & 1) ? sb : sa;
    constexpr int ta = j == 4 ? 0 : 2 * j - 9, tb = j == 4 ? 8 : 2 * j - 8;
    constexpr int na = 2 * j - 7, nb = j < 7 ? 2 * j - 6 : 7;
    if constexpr (j == 3) vrdy = *(volatile LAS u32x4*)(lds + K2_FL + 16);
    if constexpr (j >= 4) { vf[(j + 1) & 1][0] = vld(na, 0); vf[(j + 1) & 1][1] = vld(na, 1); if constexpr (j < 7) { vf[(j + 1) & 1][2] = vld(nb, 0); vf[(j + 1) & 1][3] = vld(nb, 1); } }
    if constexpr (j + 2 < 8) { km[j & 1] = lds_ld16(kp + (j + 2) * 512); kt[j & 1] = *(const LAS bf16x4*)(ktp + (j + 2) * 512); }
    if constexpr (j + 1 < 8) nxt = mfma32(km[(j + 1) & 1], qm, (f32x16){});
    SB0();
    pk[j][0] = ex8(cur, 0);
    if constexpr (j == 4) { const u32x4 a = pk[0][0] & mk0; pv(vf[0][0], a); pv(vf[0][2], pk[0][0] ^ a); }
    else if constexpr (j > 4) { pv(vf[j & 1][0], pk[ta][0]); pv(vf[j & 1][1], pk[ta][1]); }
    if constexpr (j + 1 < 8) nxt = __builtin_amdgcn_mfma_f32_32x32x8bf16_1k(kt[(j + 1) & 1], qs, nxt, 0, 0, 0);
    SB0();
    pk[j][1] = ex8(cur, 1);
    if constexpr (j == 4) { const u32x4 a = pk[0][1] & mk1; pv(vf[0][1], a); pv(vf[0][3], pk[0][1] ^ a); }
    else if constexpr (j > 4) { pv(vf[j & 1][2], pk[tb][0]); pv(vf[j & 1][3], pk[tb][1]); }
    if constexpr (j == 3) {
#pragma unroll
      for (int t = 0; t < 4; ++t) { pin(pk[t][0]); pin(pk[t][1]); }
      if (!EXACT && __builtin_expect(__builtin_amdgcn_readfirstlane((vrdy[0] & vrdy[1]) & (vrdy[2] & vrdy[3])) == 0, 0)) lds_wait4(lds + K2_FL + 16, 1);
      asm volatile("" ::: "memory");
      vf[0][0] = vld(0, 0); vf[0][1] = vld(0, 1); vf[0][2] = vld(8, 0); vf[0][3] = vld(8, 1);
      mk0 = *(const LAS u32x4*)(lds + K2_MSK + (dq + 20) * 16); mk1 = *(const LAS u32x4*)(lds + K2_MSK + (dq + 4) * 16);
    }
    SB0();
  });
  pv(vf[0][0], pk[7][0]); pv(vf[0][1], pk[7][1]);
  return o;
}

DI void attn_body(ldsp lds, char* __restrict__ ws, int c, int h, int w, int lane) {
  const int r32 = lane & 31, hi = lane >> 5;
  const int pm = 5 * (h >> 1) + 3 * (h & 1), pt = 5 * (h >> 1) + 2;
  const char* qb = ws + WS_QK + (256 * c + 32 * w) * 16;
  const bf16x8 qm = *(const bf16x8*)(qb + ((pm + hi) * S_ + r32) * 16);
  u32x2 qtr = *(const u32x2*)(qb + (pt * S_ + r32) * 16 + (h & 1) * 8);
  if (hi) qtr = (u32x2){0x3f80u, 0u};
  ldsp kp = lds + K2_K + hi * 8192 + (w * 32 + r32) * 16;
  ldsp ktp = lds + (hi ? K2_V + 3 * K2_VP + w * 512 : K2_K + 16384 + (w * 32 + r32) * 16 + (h & 1) * 8);
  const int vq = (lane & 15) >> 2, vpp = lane & 3, vblk = (lane >> 4) & 1;
  ldsp vp = lds + K2_V + (w * 32 + 4 * hi + vq) * 16 + (vblk == 0 ? (vpp >> 1) * K2_VP + (vpp & 1) * 8 : vpp == 0 ? 2 * K2_VP + (h & 1) * 8 : 3 * K2_VP + 8);
  f32x16 o = attn_rows<false>(lds, kp, ktp, vp, qm, qtr, r32 - 4 * hi, hi);
  float l = __uint_as_float(__builtin_amdgcn_permlane32_swap(__float_as_uint(o[8]), __float_as_uint(o[8]), false, false)[1]);
  if (__builtin_expect(__builtin_amdgcn_ballot_w64(!(l < LAZY_LIM)) != 0, 0)) {
    int dq2 = r32 - 4 * hi, hi2 = hi;
    pin(dq2); pin(hi2);
    o = attn_rows<true>(lds, kp, ktp, vp, qm, qtr, dq2, hi2);
    l = __uint_as_float(__builtin_amdgcn_permlane32_swap(__float_as_uint(o[8]), __float_as_uint(o[8]), false, false)[1]);
  }
  __builtin_amdgcn_s_setprio(3);
  const float il = __builtin_amdgcn_rcpf(l);
  const int tok = 256 * c + 32 * w + r32;
  const rsrc_t rw = mk_rsrc(ws, WS_END);
  const u32x4 w0 = {cvtpk(o[0] * il, o[1] * il), cvtpk(o[2] * il, o[3] * il), cvtpk(o[4] * il, o[5] * il), cvtpk(o[6] * il, o[7] * il)};
  st16(rw, WS_QK + (pm + hi) * PLANE + tok * 16, w0);
  if (hi == 0) st8(rw, WS_QK + pt * PLANE + tok * 16 + (h & 1) * 8, (u32x2){cvtpk(o[8] * il, o[9] * il), cvtpk(o[10] * il, o[11] * il)});
  wait_vm<0>();
  if (lds_add(lds + K2_FL + 4, lane) == 7) {
    if (lane < (32 >> CSH)) cnt_add(ws, (c * 32 >> CSH) + lane);
    cnt_poll_post(ws, ((blockIdx.x & 7) * 32 + (blockIdx.x >> 3)) >> CSH, 32, lds + K2_FL + 12);
  }
}

extern "C" __global__ __launch_bounds__(768, 3) void k_attn(char* __restrict__ ws, const u16* __restrict__ x, const u16* __restrict__ wg, const u16* __restrict__ wu, const u16* __restrict__ wd) {
  extern __shared__ __attribute__((aligned(16))) char smem[];
  const int lane = threadIdx.x & 63, w = __builtin_amdgcn_readfirstlane(threadIdx.x >> 6);
  const int xcd = blockIdx.x & 7, idx = blockIdx.x >> 3, h = xcd * 4 + (idx >> 3), c = idx & 7;
  if (w < 4) attn_helper((ldsp)smem, ws, c, h, w, lane, x, wg, wu, wd);
  else attn_body((ldsp)smem, ws, c, h, w - 4, lane);
}

constexpr int K4_NS = 4, K4_STG = 20480, K4_NT = 20, K4_RING = 61440, K4_CTL = K4_RING + K4_NS * K4_STG, K4_SD = K4_CTL + 4096, K4_LDS = K4_SD + 8192 + 8192;
static_assert(K4_LDS <= 163840, "LDS map");
constexpr float GC0 = -2.f * 0.7978845608028654f * 1.4426950408889634f, GC1 = GC0 * 0.044715f;

extern "C" __global__ __launch_bounds__(512, 2) void k_mlp(char* __restrict__ ws, u16* __restrict__ out) {
  extern __shared__ __attribute__((aligned(16))) char smem[];
  ldsp lds = (ldsp)smem + K4_RING;
  ldsp ctl = (ldsp)smem + K4_CTL;
  const int lane = threadIdx.x & 63, w = __builtin_amdgcn_readfirstlane(threadIdx.x >> 6);
  const unsigned lds00 = (unsigned)(uintptr_t)(ldsp)smem, lds0 = lds00 + K4_RING;
  const int xcd = blockIdx.x & 7, idx = blockIdx.x >> 3, mt = (xcd >> 2) * 8 + (idx >> 2), ft = (xcd & 3) * 4 + (idx & 3);
  const int mt5 = (xcd >> 1) * 8 + (idx >> 2), ct = (xcd & 1) * 4 + (idx & 3);
  const int odd = ct & 1, pb = (ct >> 1) * 5 + (odd ? 3 : 0), ps = (ct >> 1) * 5 + 2;
  if (w >= 4) {
    const int lw = w - 4;
    const char* wd = ws + WS_WD + ct * 245760;
    ldsp sd = (ldsp)smem + K4_SD + lw * 4096;
    if (w < 6) {
      const char* src = ws + WS_H + ((mt & 1) * 40 + lw) * PLANE + (mt >> 1) * 4096;
      const char* st = ws + WS_ST + mt * 4096 + lw * 2048;
      loader_loop<4, K4_NT, K4_NS, 4>([&](int slot) { dma_run<4>(src, lds0 + lw * 4096 + slot * K4_STG, lane * 16); src += 2 * PLANE; },
                                      [&] { dma_run<2>(st, lds00 + K4_SD + lw * 4096, lane * 16); dma_run<2>(st + S_ * 32, lds00 + K4_SD + lw * 4096 + 2048, lane * 16); });
      wait_vm<0>();
      const LAS f32x4* sp = (const LAS f32x4*)(sd + lane * 32);
      const f32x4 v[4] = {sp[0], sp[1], sp[128], sp[129]};
      const float mu = (((v[0][0] + v[0][2]) + (v[1][0] + v[1][2])) + ((v[2][0] + v[2][2]) + (v[3][0] + v[3][2]))) * 0.125f;
      float m2 = 0.f;
#pragma unroll
      for (int e = 0; e < 4; ++e) { const float d0 = v[e][0] - mu, d1 = v[e][2] - mu; m2 += (v[e][1] + v[e][3]) + 80.f * (d0 * d0 + d1 * d1); }
      const float var = m2 * (1.f / D_) + LN_EPS, rstd = __builtin_amdgcn_rsqf(var);
      ((LAS f32x4*)(ctl + 2048))[lw * 64 + lane] = (f32x4){-mu, GC0 * rstd, GC1 * rstd * rstd * rstd, var};
    } else {
      const char* src = ws + WS_WGU + ft * K4_NT * 12288 + (w - 6) * 6144;
      loader_loop<6, K4_NT, K4_NS, 4>([&](int slot) { dma_run<6>(src, lds0 + 8192 + (w - 6) * 6144 + slot * K4_STG, lane * 16); src += 12288; },
                                      [&] { dma_run<4>(ws + WS_CS + ft * 8192 + (w - 6) * 4096, lds00 + K4_SD + lw * 4096, lane * 16); });
      wait_vm<0>();
      const int e = lane < 48 ? lane : 47;
      const LAS f32x2* cp = (const LAS f32x2*)sd + e;
      ((LAS f32x2*)(ctl + 1024))[(w - 6) * 48 + e] = (((cp[0] + cp[48]) + (cp[96] + cp[144])) + ((cp[192] + cp[240]) + (cp[288] + cp[336]))) + (cp[384] + cp[432]);
    }
    asm volatile("s_waitcnt lgkmcnt(0)" ::: "memory");
    lds_post(ctl + 48 + lw * 4, 1, lane);
    dma_long<15>(wd + lw * 15360, lds00 + lw * 15360, lane * 16);
    {
      set_m0(lds00 + K4_SD + lw * 4096);
      const char* hs = ws + WS_H + ((mt5 >> 1) & 1) * 40 * PLANE + (mt5 >> 2) * 4096 + ((mt5 & 1) * 2 + (lw >> 1)) * 1024;
      const unsigned vo = (lane >> 5) * PLANE + (((lane >> 4) & 1) * 32 + (lw & 1) * 16 + (lane & 15)) * 16 + 2048;
      dma<0>(hs + pb * 2 * PLANE - 2048, vo); dma<1024>(hs + (pb + 1) * 2 * PLANE - 3072, vo); dma<2048>(hs + ps * 2 * PLANE - 4096, vo);
    }
    lds_wait(ctl, 4);
    dma_long<20>(wd + 61440 + lw * 20480, lds0 + lw * 20480, lane * 16);
    if (lw == 0) {
      lds_wait(ctl + 4, 2); lds_wait(ctl + 8, 2);
      cnt_poll_post(ws, 256 + ((mt5 * 8 + ct) >> CSH), 16, ctl + 12);
    }
    wait_vm_bar<0>();
#pragma unroll 1
    for (int g = 0; g < 5; ++g) {
      lds_wait4(ctl + 16, g + 1);
      dma_run<5>(wd + 143360 + g * 20480 + lw * 5120, lds00 + g * 20480 + lw * 5120, lane * 16);
      wait_vm<0>();
      lds_post(ctl + 32 + lw * 4, g + 1, lane);
    }
    return;
  }
  if (w == 0 && lane < 16) ((LAS unsigned*)ctl)[lane] = 0u;
  const int r32 = lane & 31, hi = lane >> 5, wm = w >> 1, wn = w & 1;
  f32x16 acc[2][3];
#pragma unroll
  for (int i = 0; i < 2; ++i)
#pragma unroll
    for (int j = 0; j < 3; ++j) { acc[i][j] = (f32x16){}; pin(acc[i][j]); }
  bf16x8 af[2][2], wf[2][3];
  const int aoff = wm * 2048 + lane * 16, woff = 8192 + wn * 3072 + lane * 16;
  auto rda = [&](ldsp sb, int s, int i) { af[s][i] = lds_ld16(sb + aoff + s * 4096 + i * 1024); };
  auto rdw = [&](ldsp sb, int s, int j) { wf[s][j] = lds_ld16(sb + woff + s * 6144 + j * 1024); };
  auto mm = [&](int s, int i, int j) { acc[i][j] = mfma32(wf[s][j], af[s][i], acc[i][j]); };
  bar();
  const int tok = mt * 128 + wm * 64 + r32;
  rda(lds, 0, 0); rdw(lds, 0, 0); rda(lds, 0, 1); rdw(lds, 0, 1); rdw(lds, 0, 2); SB0();
  f32x4 rc[2], cq[3][4];
  const LAS float* csb = (const LAS float*)(ctl + 1024) + wn * 96 + 4 * hi;
  sfor<0, K4_NT>([&](auto tc) {
    constexpr int t = decltype(tc)::value;
    constexpr bool more = t + 1 < K4_NT;
    ldsp sb = lds + (t % K4_NS) * K4_STG;
    ldsp nb = lds + ((t + 1) % K4_NS) * K4_STG;
    mm(0, 0, 0); rda(sb, 1, 0); rdw(sb, 1, 0); SB0();
    mm(0, 1, 0); rda(sb, 1, 1); rdw(sb, 1, 1); SB0();
    mm(0, 0, 1); rdw(sb, 1, 2); SB0();
    mm(0, 1, 1); SB0();
    mm(0, 0, 2); SB0();
    mm(0, 1, 2); SB0();
    if constexpr (more) { bar(); SB0(); }
    if constexpr (!more) { asm volatile("s_waitcnt lgkmcnt(0)" ::: "memory"); lds_add(ctl, lane); SB0(); }
    mm(1, 0, 0); if (more) { rda(nb, 0, 0); rdw(nb, 0, 0); } SB0();
    mm(1, 1, 0); if (more) { rda(nb, 0, 1); rdw(nb, 0, 1); } SB0();
    mm(1, 0, 1); if (more) rdw(nb, 0, 2); SB0();
    if constexpr (!more) {
      lds_wait4(ctl + 48, 1);
      rc[0] = ((const LAS f32x4*)(ctl + 2048))[wm * 64 + r32]; rc[1] = ((const LAS f32x4*)(ctl + 2048))[wm * 64 + 32 + r32];
#pragma unroll
      for (int j = 0; j < 3; ++j)
#pragma unroll
        for (int b = 0; b < 4; ++b) cq[j][b] = *(const LAS f32x4*)(csb + j * 32 + 8 * b);
    }
    mm(1, 1, 1); SB0();
    mm(1, 0, 2); SB0();
    mm(1, 1, 2); SB0();
  });
  const rsrc_t rw = mk_rsrc(ws, WS_END);
#pragma unroll
  for (int i = 0; i < 2; ++i) {
    const int tk = tok + 32 * i;
    const int ob = WS_ACT + (((tk >> 6) * 48 + ft * 3) * 4 + ((tk >> 4) & 3)) * 1024 + (hi * 16 + (tk & 15)) * 16;
#pragma unroll
    for (int j = 0; j < 3; ++j) {
      unsigned pk[4];
#pragma unroll
      for (int r = 0; r < 8; r += 2) {
        const f32x2 gv = (f32x2){acc[i][j][r], acc[i][j][r + 1]} + rc[i][0] * (f32x2){cq[j][r >> 2][r & 3], cq[j][r >> 2][(r & 3) + 1]};
        const f32x2 uv = (f32x2){acc[i][j][8 + r], acc[i][j][9 + r]} + rc[i][0] * (f32x2){cq[j][2 + (r >> 2)][r & 3], cq[j][2 + (r >> 2)][(r & 3) + 1]};
        const f32x2 arg = gv * (gv * gv * rc[i][2] + rc[i][1]);
        const f32x2 ex = {__builtin_amdgcn_exp2f(arg[0]), __builtin_amdgcn_exp2f(arg[1])};
        const f32x2 den = ex * rc[i][3] + rc[i][3];
        const f32x2 ri = {__builtin_amdgcn_rcpf(den[0]), __builtin_amdgcn_rcpf(den[1])};
        const f32x2 a = gv * uv * ri;
        pk[r >> 1] = cvtpk(a[0], a[1]);
      }
      const int T = wn * 3 + j;
      st16(rw, ob + (T >> 1) * 4096 + (T & 1) * 512, (u32x4){pk[0], pk[1], pk[2], pk[3]});
    }
  }
  wait_vm<0>();
  if (lds_add(ctl + 4 + wm * 4, lane) == 1 && lane < (8 >> CSH)) cnt_add(ws, 256 + ((mt * 2 + wm) * 8 >> CSH) + lane);
  lds_wait(ctl + 12, 1);
  constexpr int WIN = 10, REL0 = 2, REL1 = 6, REL2 = 10, REL3 = 13, REL4 = 16;
  bf16x8 a5[WIN], w5[3][5];
  const int ao = WS_ACT + mt5 * 196608 + w * 1024 + lane * 16;
#pragma unroll
  for (int ks = 0; ks < WIN; ++ks) a5[ks] = ld16_sc1(rw, ao, ks * 4096);
  SB0();
  bar();
  f32x4 ac5[5];
  {
    ldsp rp = (ldsp)smem + K4_SD + w * 4096 + lane * 16;
#pragma unroll
    for (int j = 0; j < 5; ++j) {
      const int e = odd ? (j == 0 ? 5 : j - 1) : j;
      const u32x2 r = *(const LAS u32x2*)(rp + (e >> 1) * 1024 + (e & 1) * 8);
      ac5[j] = (f32x4){bflo(r[0]), bfhi(r[0]), bflo(r[1]), bfhi(r[1])};
    }
  }
  ldsp wp = (ldsp)smem + lane * 16;
#pragma unroll
  for (int j = 0; j < 10; ++j) w5[j / 5][j % 5] = lds_ld16(wp + j * 1024);
  sfor<0, 48>([&](auto kc) {
    constexpr int ks = decltype(kc)::value, kn = ks + 2, tn = kn / 2;
    if constexpr ((kn & 3) == 0 && kn >= 28 && kn < 48) { lds_wait4(ctl + 32, (kn - 28) / 4 + 1); SB0(); }
    if constexpr ((kn & 1) == 0 && (tn == REL0 || tn == REL1 || tn == REL2 || tn == REL3 || tn == REL4)) {
      asm volatile("" ::: "memory");
      lds_post(ctl + 16 + w * 4, tn == REL0 ? 1 : tn == REL1 ? 2 : tn == REL2 ? 3 : tn == REL3 ? 4 : 5, lane);
    }
#pragma unroll
    for (int j = 0; j < 5; ++j) {
      if constexpr (kn < 48) w5[kn % 3][j] = lds_ld16(wp + (kn % 28) * 5120 + j * 1024);
      ac5[j] = mfma16(w5[ks % 3][j], a5[ks % WIN], ac5[j]);
    }
    if constexpr (ks + WIN < 48) a5[ks % WIN] = ld16_sc1(rw, ao, (ks + WIN) * 4096);
    SB0();
  });
  {
    const int g = lane >> 4;
    u32x2 hv[5];
#pragma unroll
    for (int j = 0; j < 5; ++j) hv[j] = (u32x2){cvtpk(ac5[j][0], ac5[j][1]), cvtpk(ac5[j][2], ac5[j][3])};
    const rsrc_t ro = mk_rsrc(out, S_ * D_ * 2);
    const int oo = (mt5 * 64 + w * 16 + (lane & 15)) * 1280 + ct * 160;
#pragma unroll
    for (int jp = 0; jp < 2; ++jp) {
      auto r0 = __builtin_amdgcn_permlane16_swap(hv[2 * jp][0], hv[2 * jp + 1][0], false, false);
      auto r1 = __builtin_amdgcn_permlane16_swap(hv[2 * jp][1], hv[2 * jp + 1][1], false, false);
      st16<0>(ro, oo + jp * 64 + (g & 1) * 32 + (g >> 1) * 16, (u32x4){r0[0], r1[0], r0[1], r1[1]});
    }
    st8<0>(ro, oo + 128 + g * 8, hv[4]);
  }
}

extern "C" __global__ void k_text_end() {}

extern "C" void kernel_launch(void* const* d_in, const int* in_sizes, int n_in, void* d_out, int out_size, void* d_ws, size_t ws_size,
                              hipStream_t stream) {
  const u16* x = (const u16*)d_in[0];
  const u16* wqkv = (const u16*)d_in[1];
  const u16* wo = (const u16*)d_in[2];
  const u16* wg = (const u16*)d_in[4];
  const u16* wu = (const u16*)d_in[5];
  const u16* wd = (const u16*)d_in[6];
  char* ws = (char*)d_ws;
  u16* out = (u16*)d_out;
  (void)hipFuncSetAttribute((const void*)k_qkv, hipFuncAttributeMaxDynamicSharedMemorySize, K1_LDS);
  (void)hipFuncSetAttribute((const void*)k_attn, hipFuncAttributeMaxDynamicSharedMemorySize, K2_LDS);
  (void)hipFuncSetAttribute((const void*)k_mlp, hipFuncAttributeMaxDynamicSharedMemorySize, K4_LDS);
  k_qkv<<<256, 768, K1_LDS, stream>>>(x, wqkv, wo, wd, ws);
  k_attn<<<256, 768, K2_LDS, stream>>>(ws, x, wg, wu, wd);
  k_mlp<<<256, 512, K4_LDS, stream>>>(ws, out);
}
```
